```python
import torch

def _softmax(x, axis=-1):
    return torch.softmax(x, dim=axis)

def _gelu(x):
    return torch.nn.functional.gelu(x, approximate='tanh')

def _silu(x):
    return torch.nn.functional.silu(x)

def _sigmoid(x):
    return torch.sigmoid(x)

def _layernorm(x, axis=-1, eps=1e-05):
    m = x.mean(dim=axis, keepdim=True)
    v = x.var(dim=axis, keepdim=True, unbiased=False)
    return (x - m) / torch.sqrt(v + eps)

def _scatter_add(tgt, idx, src, axis=0):
    return tgt.clone().index_add_(axis, idx, src)

def _segment_sum(src, seg, n):
    out = torch.zeros((n,) + src.shape[1:], dtype=src.dtype)
    return out.index_add_(0, seg, src)

def _pad(x, pw):
    flat = tuple((v for pair in reversed(pw) for v in pair))
    return torch.nn.functional.pad(x, flat)
kernel_name = 'gated_mix__mha_block__mixer_gmlp__prompt'

def setup_inputs():

    _w = lambda a: (lambda t: t.to(torch.bfloat16) if t.is_floating_point() else t)(torch.as_tensor(a))

    def _A_setup():
        B, S, D, H = (1, 2048, 576, 32)
        return {'x': _w((lambda *s: torch.randn(*s, dtype=torch.bfloat16) * 0.05)(B, S, D)), 'wqkv': _w((lambda *s: torch.randn(*s, dtype=torch.bfloat16) * 1.6)(D, 3 * D)), 'wo': _w((lambda *s: torch.randn(*s, dtype=torch.bfloat16) * 0.1)(D, D))}
    _A_in = _A_setup()

    def _B_setup():
        B, S, D, F = (1, 2048, 576, 4608)
        Fh = F // 2
        return {'x': _w((lambda *s: torch.randn(*s, dtype=torch.bfloat16) * 0.05)(B, S, D)), 'w_in': _w((lambda *s: torch.randn(*s, dtype=torch.bfloat16) * 1.6)(D, 2 * Fh)), 'w_sp': _w(0.01 * (lambda *s: torch.randn(*s, dtype=torch.bfloat16) * 1.6)(S, S)), 'b_sp': _w(1.0 + 0.01 * (lambda *s: torch.randn(*s, dtype=torch.bfloat16) * 0.05)(S)), 'w_out': _w((lambda *s: torch.randn(*s, dtype=torch.bfloat16) * 0.05)(Fh, D))}
    _B_in = _B_setup()
    return {'A_x': _A_in['x'], 'A_wqkv': _A_in['wqkv'], 'A_wo': _A_in['wo'], 'B_x': _B_in['x'], 'B_w_in': _B_in['w_in'], 'B_w_sp': _B_in['w_sp'], 'B_b_sp': _B_in['b_sp'], 'B_w_out': _B_in['w_out'], 'gate_w': _w((lambda *s: torch.randn(*s, dtype=torch.bfloat16) * 0.05)(576) * 16.0)}

def reference(A_x, A_wqkv, A_wo, B_x, B_w_in, B_w_sp, B_b_sp, B_w_out, gate_w):

    def _A_ref(x, wqkv, wo):
        B, S, D = x.shape
        H = 32
        Dh = D // H
        qkv = (lambda x, *d: x.permute(*d))((x @ wqkv).reshape(B, S, 3, H, Dh), 2, 0, 3, 1, 4)
        q, k, v = (qkv[0], qkv[1], qkv[2])
        scores = torch.einsum('bhqd,bhkd->bhqk', q, k) * Dh ** (-0.5)
        p = _softmax(scores, axis=-1)
        o = torch.einsum('bhqk,bhkd->bhqd', p, v)
        return (lambda x, *d: x.permute(*d))(o, 0, 2, 1, 3).reshape(B, S, D) @ wo

    def _B_ref(x, w_in, w_sp, b_sp, w_out):
        B, S, D = x.shape
        F = w_in.shape[-1]
        Fh = F // 2
        z = _gelu(torch.einsum('bsd,df->bsf', x, w_in))
        z1 = z[..., :Fh].clone()
        z2 = _layernorm(z[..., Fh:], axis=-1)
        g = torch.einsum('ts,bsf->btf', w_sp, z2) + b_sp[None, :, None]
        return torch.einsum('bsf,fd->bsd', z1 * g, w_out)
    x = A_x
    g = _sigmoid((x * gate_w).sum(-1, keepdims=True))
    a = _A_ref(x, A_wqkv, A_wo)
    b = _B_ref(x, B_w_in, B_w_sp, B_b_sp, B_w_out)
    return g * a + (1 - g) * b
```

```cpp
#include <hip/hip_runtime.h>
#include <stdint.h>

typedef __bf16 bf16x8 __attribute__((ext_vector_type(8)));
typedef __bf16 bf16x2 __attribute__((ext_vector_type(2)));
typedef short s16x4 __attribute__((ext_vector_type(4)));
typedef float f32x2 __attribute__((ext_vector_type(2)));
typedef float f32x4 __attribute__((ext_vector_type(4)));
typedef float f32x16 __attribute__((ext_vector_type(16)));
typedef unsigned u32x2 __attribute__((ext_vector_type(2)));
typedef unsigned u32x4 __attribute__((ext_vector_type(4)));
typedef unsigned short u16;
#define LAS __attribute__((address_space(3)))
#define DI __device__ __forceinline__

constexpr int S_ = 2048, D_ = 576, NH = 32, FH = 2304;
constexpr size_t WS_Z2 = 0;
constexpr size_t WS_Z1 = WS_Z2 + 144 * 64 * 1024;
constexpr size_t WS_QM = WS_Z1 + 64 * 144 * 1024;
constexpr size_t WS_QR = WS_QM + 32 * 64 * 1024;
constexpr size_t WS_KM = WS_QR + 32 * 2048 * 4;
constexpr size_t WS_KR = WS_KM + 32 * 64 * 1024;
constexpr size_t WS_VT = WS_KR + 32 * 2048 * 4;
constexpr size_t WS_HC = WS_VT + 32 * 128 * 640;
constexpr size_t WS_WC = WS_HC + 128 * 90 * 1024;
constexpr size_t WS_GT = WS_WC + 36 * 90 * 1024;
constexpr size_t WS_ST = WS_GT + 2048 * 4;
constexpr size_t WS_END = WS_ST + 2048 * 16 * 8;

struct Params {
    const u16* x; const u16* wqkv; const u16* wo; const u16* win; const u16* wsp; const u16* bsp; const u16* wout; const u16* gatew;
    u16* out; char* ws;
};

DI float bf2f(u16 v) { return __uint_as_float((unsigned)v << 16); }
DI unsigned pk2(float lo, float hi) { f32x2 v = {lo, hi}; bf16x2 b = __builtin_convertvector(v, bf16x2); return __builtin_bit_cast(unsigned, b); }
DI unsigned rfl(unsigned v) { return __builtin_amdgcn_readfirstlane(v); }
DI u32x4 make_srd(const void* p, unsigned bytes) {
    uint64_t a = (uint64_t)p;
    u32x4 r; r[0] = rfl((unsigned)a); r[1] = rfl((unsigned)(a >> 32) & 0xffffu); r[2] = rfl(bytes); r[3] = 0x00020000u; return r;
}
DI void dma16(u32x4 srd, unsigned voff, unsigned soff, unsigned ldsaddr) {
    asm volatile("s_mov_b32 m0, %0\n\ts_nop 0\n\tbuffer_load_dwordx4 %1, %2, %3 offen lds" ::"s"(ldsaddr), "v"(voff), "s"(srd), "s"(soff) : "memory");
}
constexpr size_t WS_DBG = 200u << 20;
#define STAMPV(P_, idx) do {} while (0)
#define STAMP3(P_, i) do {} while (0)
#define WAIT_BAR0() asm volatile("s_waitcnt vmcnt(0) lgkmcnt(0)\n\ts_barrier" ::: "memory")
DI bf16x8 lds_rd128(unsigned a) { return *(const LAS bf16x8*)(uintptr_t)a; }
DI s16x4 lds_rdtr(unsigned a) { return __builtin_amdgcn_ds_read_tr16_b64_v4i16((LAS s16x4*)(uintptr_t)a); }
DI bf16x8 cat8(s16x4 a, s16x4 b) { typedef short s16x8 __attribute__((ext_vector_type(8))); s16x8 v = __builtin_shufflevector(a, b, 0, 1, 2, 3, 4, 5, 6, 7); return __builtin_bit_cast(bf16x8, v); }

#define SB() __builtin_amdgcn_sched_barrier(0)
DI void set_m0(unsigned base, unsigned imm) { asm volatile("s_add_u32 m0, %0, %1" ::"s"(base), "n"(imm) : "scc"); }
DI void set_m0s(unsigned a, unsigned b) { asm volatile("s_add_u32 m0, %0, %1" ::"s"(a), "s"(b) : "scc"); }
DI void dma_go(u32x4 srd, unsigned voff, unsigned soff) { asm volatile("buffer_load_dwordx4 %0, %1, %2 offen lds" ::"v"(voff), "s"(srd), "s"(soff) : "memory"); }
DI void mfma16a(f32x4& c, bf16x8 a, bf16x8 b) { asm volatile("v_mfma_f32_16x16x32_bf16 %0, %1, %2, %0" : "+a"(c) : "v"(a), "v"(b)); }

#define PIN18(acc, txt) asm volatile(txt : "+a"(acc[0][0]), "+a"(acc[0][1]), "+a"(acc[1][0]), "+a"(acc[1][1]), "+a"(acc[2][0]), "+a"(acc[2][1]), "+a"(acc[3][0]), "+a"(acc[3][1]), "+a"(acc[4][0]), "+a"(acc[4][1]), \
    "+a"(acc[5][0]), "+a"(acc[5][1]), "+a"(acc[6][0]), "+a"(acc[6][1]), "+a"(acc[7][0]), "+a"(acc[7][1]), "+a"(acc[8][0]), "+a"(acc[8][1]))
#define PIN5(acc, txt) asm volatile(txt : "+a"(acc[0]), "+a"(acc[1]), "+a"(acc[2]), "+a"(acc[3]), "+a"(acc[4]))
#define LGKM(n) __builtin_amdgcn_s_waitcnt(0xC07F | ((n) << 8))
#define OPAQUE(x) asm volatile("" : "+v"(x))
constexpr unsigned OOB = 0x40000000u;
#define EX2(x) __builtin_amdgcn_exp2f(x)
#define BC8(x) __builtin_bit_cast(bf16x8, x)
DI unsigned lds_rd32(unsigned a) { return *(const LAS unsigned*)(uintptr_t)a; }
#define MFMA16(a, b, c) __builtin_amdgcn_mfma_f32_16x16x32_bf16(a, b, c, 0, 0, 0)
#define MFMA32(a, b, c) __builtin_amdgcn_mfma_f32_32x32x16_bf16(a, b, c, 0, 0, 0)

DI float gelu_t(float x) {
    const float c0 = -2.0f * 0.7978845608028654f * 1.4426950408889634f, c1 = c0 * 0.044715f;
    const float e = __builtin_amdgcn_exp2f(x * __builtin_fmaf(x * x, c1, c0));
    return x * __builtin_amdgcn_rcpf(1.0f + e);
}
template <int N> DI float dpp_ror_add(float v) { return v + __builtin_bit_cast(float, __builtin_amdgcn_update_dpp(0, __builtin_bit_cast(int, v), 0x120 + N, 0xf, 0xf, false)); }
DI float row16_sum(float v) { v = dpp_ror_add<8>(v); v = dpp_ror_add<4>(v); v = dpp_ror_add<2>(v); v = dpp_ror_add<1>(v); return v; }

constexpr int G1_STAGE = 16384 + 20480, G1_LDS = 2 * G1_STAGE;

struct G1Regs { bf16x8 wf[9], xa[2], xb[2]; unsigned vX[4], vW[5], vw0[2], vw1[2], vx0[2], vx1[2]; };
template <bool TRANS> DI void g1_mm(f32x4& c, bf16x8 wf, bf16x8 xf) { if (TRANS) mfma16a(c, wf, xf); else mfma16a(c, xf, wf); }
DI bf16x8 g1_rdw(unsigned lo, unsigned hi, int off) { return cat8(lds_rdtr(lo + off), lds_rdtr(hi + off)); }

template <bool TRANS, int SLOT>
DI void g1_stage(f32x4 (&acc)[9][2], G1Regs& r, u32x4 srdX, u32x4 srdW, unsigned sWX, unsigned sWW, unsigned sX, unsigned sW) {
    constexpr int NXT = SLOT ^ 1;
#pragma unroll
    for (int n = 0; n < 9; ++n) {
        if (n == 0) LGKM(8);
        if (n == 5) LGKM(12);
        g1_mm<TRANS>(acc[n][0], r.wf[n], r.xa[0]);
        if (n < 2) r.xb[n] = lds_rd128(r.vx1[SLOT] + n * 2048);
        g1_mm<TRANS>(acc[n][1], r.wf[n], r.xa[1]);
        r.wf[n] = g1_rdw(r.vw0[SLOT], r.vw1[SLOT], 32 * 288 + n * 32);
        SB();
    }
    asm volatile("s_waitcnt vmcnt(0) lgkmcnt(0)\n\ts_barrier" ::: "memory");
    LGKM(0);
    SB();
#pragma unroll
    for (int n = 0; n < 9; ++n) {
        if (n < 4) set_m0(sWX, SLOT * G1_STAGE + n * 1024); else set_m0(sWW, SLOT * G1_STAGE + (n - 4) * 4096);
        g1_mm<TRANS>(acc[n][0], r.wf[n], r.xb[0]);
        if (n < 4) dma_go(srdX, r.vX[n], sX); else dma_go(srdW, r.vW[n - 4], sW);
        g1_mm<TRANS>(acc[n][1], r.wf[n], r.xb[1]);
        if (n < 2) r.xa[n] = lds_rd128(r.vx0[NXT] + n * 2048);
        r.wf[n] = g1_rdw(r.vw0[NXT], r.vw1[NXT], n * 32);
        SB();
    }
}

template <bool TRANS>
DI void gemm1_main(const Params& P, const u16* x, const u16* W, int ldw, int row0, int col0, unsigned lds0, f32x4 (&acc)[9][2]) {
    const int tid = threadIdx.x, l = tid & 63; const unsigned w = rfl(tid >> 6);
    const u32x4 srdX = make_srd(x, S_ * D_ * 2), srdW = make_srd(W, (unsigned)(D_ * ldw * 2));
    const int g = l >> 4, q = (l & 15) >> 2, p = l & 3;
    G1Regs r;
#pragma unroll
    for (int i = 0; i < 4; ++i) { r.vX[i] = (unsigned)((row0 + 32 * w + 8 * i + (l >> 3)) * (D_ * 2) + (((l & 7) ^ (l >> 3)) << 4)); OPAQUE(r.vX[i]); }
#pragma unroll
    for (int i = 0; i < 5; ++i) { const int pc = (int)w + 4 * i, idx = 64 * pc + l, rho = idx / 18, c = idx - rho * 18, k = rho ^ (((rho >> 3) & 1) << 2);
        r.vW[i] = pc < 18 ? (unsigned)(k * ldw * 2 + (col0 + 8 * c) * 2) : OOB; OPAQUE(r.vW[i]); }
#pragma unroll
    for (int s = 0; s < 2; ++s) {
        const unsigned sb = lds0 + s * G1_STAGE;
        r.vw0[s] = sb + (unsigned)(16384 + (8 * g + (q ^ (4 * (g & 1)))) * 288 + p * 8); r.vw1[s] = sb + (unsigned)(16384 + (8 * g + ((q + 4) ^ (4 * (g & 1)))) * 288 + p * 8);
        r.vx0[s] = sb + (unsigned)((32 * w + (l & 15)) * 128) + ((g ^ (l & 7)) << 4); r.vx1[s] = sb + (unsigned)((32 * w + (l & 15)) * 128) + (((4 + g) ^ (l & 7)) << 4);
        OPAQUE(r.vw0[s]); OPAQUE(r.vw1[s]); OPAQUE(r.vx0[s]); OPAQUE(r.vx1[s]);
    }
    const unsigned sWX = lds0 + w * 4096, sWW = lds0 + 16384 + w * 1024, kst = (unsigned)(64 * ldw * 2);
#pragma unroll
    for (int st = 0; st < 2; ++st) {
#pragma unroll
        for (int i = 0; i < 4; ++i) { set_m0(sWX, st * G1_STAGE + i * 1024); asm volatile("s_nop 0"); dma_go(srdX, r.vX[i], st * 128); }
#pragma unroll
        for (int i = 0; i < 5; ++i) { set_m0(sWW, st * G1_STAGE + i * 4096); asm volatile("s_nop 0"); dma_go(srdW, r.vW[i], st * kst); }
    }
#pragma unroll
    for (int n = 0; n < 9; ++n) { acc[n][0] = f32x4{0, 0, 0, 0}; acc[n][1] = f32x4{0, 0, 0, 0}; }
    PIN18(acc, "s_nop 1");
    asm volatile("s_waitcnt vmcnt(9)\n\ts_barrier" ::: "memory");
#pragma unroll
    for (int tb = 0; tb < 2; ++tb) r.xa[tb] = lds_rd128(r.vx0[0] + tb * 2048);
#pragma unroll
    for (int n = 0; n < 9; ++n) r.wf[n] = g1_rdw(r.vw0[0], r.vw1[0], n * 32);
    LGKM(0);
    for (int st = 0; st < 8; st += 2) {
        g1_stage<TRANS, 0>(acc, r, srdX, srdW, sWX, sWW, st + 2 < 9 ? (st + 2) * 128u : OOB, st + 2 < 9 ? (st + 2) * kst : OOB);
        g1_stage<TRANS, 1>(acc, r, srdX, srdW, sWX, sWW, st + 3 < 9 ? (st + 3) * 128u : OOB, st + 3 < 9 ? (st + 3) * kst : OOB);
    }
    g1_stage<TRANS, 0>(acc, r, srdX, srdW, sWX, sWW, OOB, OOB);
    asm volatile("s_waitcnt vmcnt(0) lgkmcnt(0)\n\ts_barrier" ::: "memory");
    PIN18(acc, "s_nop 7\n\ts_nop 1");
}

DI void k1_z2(const Params& P, int panel, int jt, unsigned lds0) {
    f32x4 acc[9][2];
    gemm1_main<false>(P, P.x, P.win, 4608, panel * 128, FH + jt * 144, lds0, acc);
    const int l = threadIdx.x & 63; const unsigned w = rfl(threadIdx.x >> 6); const int g = l >> 4;
    float s1[2][4], s2[2][4];
#pragma unroll
    for (int tb = 0; tb < 2; ++tb)
#pragma unroll
        for (int r = 0; r < 4; ++r) { s1[tb][r] = 0.f; s2[tb][r] = 0.f; }
    char* dst = P.ws + WS_Z2 + ((size_t)(jt * 9) * 64 + panel * 4 + w) * 1024 + l * 16;
#pragma unroll
    for (int n = 0; n < 9; ++n) {
        unsigned pk[2][2];
#pragma unroll
        for (int tb = 0; tb < 2; ++tb) {
            float y[4];
#pragma unroll
            for (int r = 0; r < 4; ++r) { y[r] = gelu_t(acc[n][tb][r]); s1[tb][r] += y[r]; s2[tb][r] = __builtin_fmaf(y[r], y[r], s2[tb][r]); }
            pk[tb][0] = pk2(y[0], y[1]); pk[tb][1] = pk2(y[2], y[3]);
        }
        u32x4 o;
#pragma unroll
        for (int i = 0; i < 2; ++i) {
            auto a = __builtin_amdgcn_permlane32_swap(pk[0][i], pk[1][i], false, false);
            auto b = __builtin_amdgcn_permlane16_swap(a[0], a[1], false, false);
            o[i] = b[0]; o[2 + i] = b[1];
        }
        *(u32x4*)(dst + (size_t)n * 64 * 1024) = o;
    }
    float* st = (float*)(P.ws + WS_ST);
#pragma unroll
    for (int tb = 0; tb < 2; ++tb)
#pragma unroll
        for (int r = 0; r < 4; ++r) {
            const float a = row16_sum(s1[tb][r]), b = row16_sum(s2[tb][r]);
            if ((l & 15) == 0) { const int s = panel * 128 + 32 * w + 16 * tb + 4 * g + r; *(f32x2*)(st + (s * 16 + jt) * 2) = f32x2{a, b}; }
        }
}

DI void k1_z1(const Params& P, int panel, int jt, unsigned lds0) {
    f32x4 acc[9][2];
    gemm1_main<true>(P, P.x, P.win, 4608, panel * 128, jt * 144, lds0, acc);
    const int l = threadIdx.x & 63; const unsigned w = rfl(threadIdx.x >> 6);
    char* dst = P.ws + WS_Z1 + ((size_t)(panel * 4 + w) * 144 + jt * 9) * 1024 + l * 16;
#pragma unroll
    for (int n = 0; n < 9; ++n) {
        u32x4 o;
#pragma unroll
        for (int tb = 0; tb < 2; ++tb) { o[2 * tb] = pk2(gelu_t(acc[n][tb][0]), gelu_t(acc[n][tb][1])); o[2 * tb + 1] = pk2(gelu_t(acc[n][tb][2]), gelu_t(acc[n][tb][3])); }
        *(u32x4*)(dst + n * 1024) = o;
    }
}

DI void k1_qk(const Params& P, int panel, int which, int hq, unsigned lds0, char* smem) {
    f32x4 acc[9][2];
    gemm1_main<true>(P, P.x, P.wqkv, 1728, panel * 128, which * 576 + hq * 144, lds0, acc);
    const int l = threadIdx.x & 63; const unsigned w = rfl(threadIdx.x >> 6); const int g = l >> 4;
    const float sc = which == 0 ? 0.23570226039551584f * 1.4426950408889634f : 1.0f;
    char* st = smem + w * 9216;
#pragma unroll
    for (int n = 0; n < 9; ++n)
#pragma unroll
        for (int pp = 0; pp < 2; ++pp) {
            const int c = 16 * n + 4 * g + 2 * pp, hl = c / 18, d = c - 18 * hl;
#pragma unroll
            for (int tb = 0; tb < 2; ++tb) {
                const int tok = 16 * tb + (l & 15);
                const int off = d < 16 ? hl * 1024 + (tok + 32 * (d >> 3)) * 16 + (d & 7) * 2 : 8192 + hl * 128 + tok * 4;
                *(unsigned*)(st + off) = pk2(acc[n][tb][2 * pp] * sc, acc[n][tb][2 * pp + 1] * sc);
            }
        }
    asm volatile("s_waitcnt lgkmcnt(0)" ::: "memory");
    char* dm = P.ws + (which == 0 ? WS_QM : WS_KM) + ((size_t)(hq * 8) * 64 + panel * 4 + w) * 1024 + l * 16;
#pragma unroll
    for (int h = 0; h < 8; ++h) *(u32x4*)(dm + (size_t)h * 64 * 1024) = *(const u32x4*)(st + h * 1024 + l * 16);
    char* dr = P.ws + (which == 0 ? WS_QR : WS_KR) + ((size_t)(hq * 8 + (l >> 3)) * 2048 + panel * 128 + 32 * w) * 4 + (l & 7) * 16;
    *(u32x4*)dr = *(const u32x4*)(st + 8192 + l * 16);
}

DI void k1_v(const Params& P, int panel, int hq, unsigned lds0, char* smem) {
    f32x4 acc[9][2];
    gemm1_main<false>(P, P.x, P.wqkv, 1728, panel * 128, 2 * 576 + hq * 144, lds0, acc);
    const int l = threadIdx.x & 63; const unsigned w = rfl(threadIdx.x >> 6); const int g = l >> 4;
    char* st = smem + w * 10240;
    if (l < 32) {
        const int blk = l >> 1, hh = l & 1;
        *(u32x4*)(st + blk * 640 + (hh * 20 + 18) * 16) = u32x4{0x3f803f80u, 0x3f803f80u, 0x3f803f80u, 0x3f803f80u};
        *(u32x4*)(st + blk * 640 + (hh * 20 + 19) * 16) = u32x4{0, 0, 0, 0};
    }
#pragma unroll
    for (int n = 0; n < 9; ++n) {
        const int c = 16 * n + (l & 15), hl = c / 18, d = c - 18 * hl;
#pragma unroll
        for (int tb = 0; tb < 2; ++tb)
            *(u32x2*)(st + (hl * 2 + tb) * 640 + ((g & 1) * 20 + d) * 16 + (g >> 1) * 8) = u32x2{pk2(acc[n][tb][0], acc[n][tb][1]), pk2(acc[n][tb][2], acc[n][tb][3])};
    }
    asm volatile("s_waitcnt lgkmcnt(0)" ::: "memory");
    char* dv = P.ws + WS_VT + ((size_t)(hq * 8) * 128 + panel * 8 + 2 * w) * 640;
#pragma unroll
    for (int i = 0; i < 10; ++i) { const int idx = 64 * i + l, h = idx / 80, o = idx - 80 * h; *(u32x4*)(dv + (size_t)h * 128 * 640 + o * 16) = *(const u32x4*)(st + idx * 16); }
}

DI void k1_prep(const Params& P, int nb) {
    const int tid = threadIdx.x, l = tid & 63, w = tid >> 6, g = l >> 4;
    for (int blk = nb * 4 + w; blk < 36 * 90; blk += 256) {
        const int db = blk / 90, kb = blk - db * 90, col = db * 16 + (l & 15);
        u16 v[8];
#pragma unroll
        for (int j = 0; j < 8; ++j) {
            if (kb < 16) { const int head = 2 * kb + (g >> 1), h = g & 1, d = j < 4 ? 4 * h + j : 8 + 4 * h + (j - 4); v[j] = P.wo[(18 * head + d) * 576 + col]; }
            else if (kb < 18) { const int head = 16 * (kb - 16) + 4 * g + (j >> 1); v[j] = P.wo[(18 * head + 16 + (j & 1)) * 576 + col]; }
            else { const int f = 32 * (kb - 18) + (j < 4 ? 4 * g + j : 16 + 4 * g + (j - 4)); v[j] = P.wout[f * 576 + col]; }
        }
        u32x4 o; for (int i = 0; i < 4; ++i) o[i] = (unsigned)v[2 * i] | ((unsigned)v[2 * i + 1] << 16);
        *(u32x4*)(P.ws + WS_WC + (size_t)blk * 1024 + l * 16) = o;
    }
    float* gt = (float*)(P.ws + WS_GT);
    for (int i = 0; i < 8; ++i) {
        const int row = nb * 32 + w * 8 + i; float a = 0.f;
        for (int c = l; c < 72; c += 64) {
            const u32x4 xv = *(const u32x4*)(P.x + row * 576 + c * 8), gv = *(const u32x4*)(P.gatew + c * 8);
#pragma unroll
            for (int e = 0; e < 4; ++e) { a = __builtin_fmaf(__uint_as_float(xv[e] << 16), __uint_as_float(gv[e] << 16), a); a = __builtin_fmaf(__uint_as_float(xv[e] & 0xffff0000u), __uint_as_float(gv[e] & 0xffff0000u), a); }
        }
        for (int o = 32; o > 0; o >>= 1) a += __shfl_xor(a, o);
        if (l == 0) gt[row] = 1.0f / (1.0f + __expf(-a));
    }
}

__global__ __launch_bounds__(256, 2) void k1_kernel(Params P) {
    extern __shared__ __attribute__((aligned(16))) char smem[];
    const unsigned lds0 = (unsigned)(uintptr_t)smem;
    const int b = blockIdx.x;
    if (b < 256) k1_z2(P, b >> 4, b & 15, lds0);
    else if (b < 448) { const int i = b - 256, panel = i / 12, j = i - panel * 12; if (j < 8) k1_qk(P, panel, j >> 2, j & 3, lds0, smem); else k1_v(P, panel, j - 8, lds0, smem); }
    else if (b < 704) { const int i = b - 448; k1_z1(P, i >> 4, i & 15, lds0); }
    else k1_prep(P, b - 704);
}

constexpr int AT_HALF = 10240, AT_SLOT = 2 * AT_HALF, AT_LDS = 2 * AT_SLOT + 64;
struct AttnState {
    f32x16 S[2], O[2]; u32x4 pl[2], ph[2];
    bf16x8 k0, v0, v1; u32x4 k1; u32x4 q0[2], q1[2];
};
template <int T, class F>
DI void attn_slot(AttnState& s, unsigned aK, unsigned aR, unsigned aV, const f32x16& zero, F&& hook) {
    constexpr int U = T ^ 1;
    s.S[U] = MFMA32(s.k0, BC8(s.q0[U]), zero);
    SB();
#pragma unroll
    for (int i = 0; i < 4; ++i) s.S[T][i] = EX2(s.S[T][i]);
    SB();
    s.S[U] = MFMA32(BC8(s.k1), BC8(s.q1[U]), s.S[U]);
    SB();
    if (T == 0) { s.k0 = lds_rd128(aK); s.k1[0] = lds_rd32(aR); }
#pragma unroll
    for (int i = 4; i < 8; ++i) s.S[T][i] = EX2(s.S[T][i]);
    s.pl[T][0] = pk2(s.S[T][0], s.S[T][1]); s.pl[T][1] = pk2(s.S[T][2], s.S[T][3]);
    SB();
    s.O[U] = MFMA32(s.v1, BC8(s.ph[U]), s.O[U]);
    SB();
    if (T == 0) s.v1 = lds_rd128(aV);
    hook();
    s.pl[T][2] = pk2(s.S[T][4], s.S[T][5]); s.pl[T][3] = pk2(s.S[T][6], s.S[T][7]);
    SB();
#pragma unroll
    for (int i = 8; i < 12; ++i) s.S[T][i] = EX2(s.S[T][i]);
    SB();
    s.O[T] = MFMA32(s.v0, BC8(s.pl[T]), s.O[T]);
    SB();
    if (T == 1) s.v0 = lds_rd128(aV);
#pragma unroll
    for (int i = 12; i < 16; ++i) s.S[T][i] = EX2(s.S[T][i]);
    SB();
    s.ph[T][0] = pk2(s.S[T][8], s.S[T][9]); s.ph[T][1] = pk2(s.S[T][10], s.S[T][11]);
    s.ph[T][2] = pk2(s.S[T][12], s.S[T][13]); s.ph[T][3] = pk2(s.S[T][14], s.S[T][15]);
    SB();
}

DI void attn_unit(const Params& P, int head, int qb, unsigned lds0, char* smem) {
    const int tid = threadIdx.x, l = tid & 63; const unsigned w = rfl(tid >> 6); const int h = l >> 5, r32 = l & 31;
    const unsigned qp = w & 1, kh = w >> 1;
    const u32x4 srd = make_srd(P.ws + WS_KM, (unsigned)(WS_HC - WS_KM));
    unsigned vl = (unsigned)l * 16; OPAQUE(vl);
    unsigned sLds[5], sSrc[5], sStep[5];
#pragma unroll
    for (int i = 0; i < 5; ++i) {
        const unsigned p = w + 4 * i, hf = p >= 10 ? 1u : 0u, j = p - 10 * hf;
        sLds[i] = lds0 + hf * AT_HALF + j * 1024;
        sSrc[i] = j < 4 ? (unsigned)(head * 65536) + (hf * 32 + j) * 1024
                : j < 9 ? (unsigned)(WS_VT - WS_KM) + (unsigned)(head * 81920) + hf * 64 * 640 + (j - 4) * 1024
                        : (unsigned)(WS_KR - WS_KM) + (unsigned)(head * 8192) + hf * 4096;
        sStep[i] = j < 4 ? 4096u : (j < 9 ? 5120u : 512u);
    }
    auto issue = [&](int ch, int buf) {
#pragma unroll
        for (int i = 0; i < 5; ++i) { set_m0s(sLds[i], (unsigned)(buf * AT_SLOT)); const unsigned so = ch < 8 ? sSrc[i] + (unsigned)ch * sStep[i] : OOB; asm volatile("s_nop 0"); dma_go(srd, vl, so); }
    };
    AttnState s;
    const int tok0 = qb * 128 + 64 * (int)qp;
    issue(0, 0); issue(1, 1);
#pragma unroll
    for (int t = 0; t < 2; ++t) {
        s.q0[t] = *(const u32x4*)(P.ws + WS_QM + ((size_t)head * 64 + (tok0 >> 5) + t) * 1024 + l * 16);
        const unsigned qr = *(const unsigned*)(P.ws + WS_QR + ((size_t)head * 2048 + tok0 + 32 * t + r32) * 4);
        s.q1[t] = u32x4{h ? 0u : qr, 0u, 0u, 0u};
    }
    const unsigned hb = lds0 + kh * AT_HALF;
    unsigned ka = hb + (unsigned)l * 16, ra = hb + 9216u + (unsigned)r32 * 4, va = hb + 4096u + (unsigned)(h * 20 + (r32 < 19 ? r32 : 19)) * 16;
    OPAQUE(ka); OPAQUE(ra); OPAQUE(va);
    const f32x16 zero = {};
    float* xch = (float*)(smem);
    volatile LAS unsigned* flag = (volatile LAS unsigned*)(uintptr_t)(lds0 + 2 * AT_SLOT);

    for (int attempt = 0; attempt < 2; ++attempt) {
        float mx[2] = {-3.0e38f, -3.0e38f};
        u32x4 qz[2];
#pragma unroll
        for (int t = 0; t < 2; ++t) qz[t] = u32x4{s.q1[t][0], 0u, 0u, 0u};
        if (attempt == 0) {
            WAIT_BAR0();
            const bf16x8 k0 = lds_rd128(lds0 + (unsigned)l * 16);
            const u32x4 k1 = u32x4{lds_rd32(lds0 + 9216u + (unsigned)r32 * 4), 0u, 0u, 0u};
#pragma unroll
            for (int t = 0; t < 2; ++t) {
                f32x16 sc = MFMA32(k0, BC8(s.q0[t]), zero); sc = MFMA32(BC8(k1), BC8(qz[t]), sc);
#pragma unroll
                for (int r = 0; r < 16; ++r) mx[t] = __builtin_fmaxf(mx[t], sc[r]);
            }
        } else {
            for (int ch = 0; ch < 8; ++ch) {
                WAIT_BAR0(); issue(ch, 0); WAIT_BAR0();
                for (int kb = 0; kb < 4; ++kb) {
                    const bf16x8 k0 = lds_rd128(ka + kb * 1024);
                    const u32x4 k1 = u32x4{lds_rd32(ra + kb * 128), 0u, 0u, 0u};
#pragma unroll
                    for (int t = 0; t < 2; ++t) {
                        f32x16 sc = MFMA32(k0, BC8(s.q0[t]), zero); sc = MFMA32(BC8(k1), BC8(qz[t]), sc);
#pragma unroll
                        for (int r = 0; r < 16; ++r) mx[t] = __builtin_fmaxf(mx[t], sc[r]);
                    }
                }
            }
            WAIT_BAR0();
#pragma unroll
            for (int t = 0; t < 2; ++t) xch[(w * 2 + t) * 64 + l] = mx[t];
            WAIT_BAR0();
#pragma unroll
            for (int t = 0; t < 2; ++t) mx[t] = __builtin_fmaxf(mx[t], xch[((w ^ 2) * 2 + t) * 64 + l]);
            WAIT_BAR0();
            issue(0, 0); issue(1, 1);
            WAIT_BAR0();
        }
#pragma unroll
        for (int t = 0; t < 2; ++t) {
            auto rr = __builtin_amdgcn_permlane32_swap(__float_as_uint(mx[t]), __float_as_uint(mx[t]), false, false);
            const float m = __builtin_fmaxf(__uint_as_float(rr[0]), __uint_as_float(rr[1]));
            const unsigned nb = __float_as_uint(-m), hi = nb & 0xffff0000u;
            const float r1 = -m - __uint_as_float(hi); const unsigned mid = __float_as_uint(r1) & 0xffff0000u;
            const float r2 = r1 - __uint_as_float(mid); const unsigned lo = __float_as_uint(r2) & 0xffff0000u;
            s.q1[t][1] = h ? 0u : ((hi >> 16) | mid); s.q1[t][2] = h ? 0u : (lo >> 16);
        }
        s.O[0] = zero; s.O[1] = zero;
        s.k0 = lds_rd128(ka); s.k1 = u32x4{lds_rd32(ra), 0x3f803f80u, 0x00003f80u, 0u};
        s.v0 = lds_rd128(va); s.v1 = s.v0; s.ph[1] = u32x4{0u, 0u, 0u, 0u};
        s.S[0] = MFMA32(s.k0, BC8(s.q0[0]), zero); s.S[0] = MFMA32(BC8(s.k1), BC8(s.q1[0]), s.S[0]);
        unsigned cur = 0, nxt = AT_SLOT;
        auto nohook = [] {};
        for (int ch = 0; ch < 8; ++ch) {
            const unsigned cK = ka + cur, cR = ra + cur, cV = va + cur, nK = ka + nxt, nR = ra + nxt, nV = va + nxt;
#pragma unroll
            for (int j = 0; j < 3; ++j) {
                attn_slot<0>(s, cK + (j + 1) * 1024, cR + (j + 1) * 128, cV + j * 1280 + 640, zero, nohook);
                attn_slot<1>(s, 0, 0, cV + (j + 1) * 1280, zero, nohook);
            }
            asm volatile("s_waitcnt vmcnt(0)\n\ts_barrier" ::: "memory");
            SB();
            attn_slot<0>(s, nK, nR, cV + 3 * 1280 + 640, zero, nohook);
            attn_slot<1>(s, 0, 0, nV, zero, [&] { asm volatile("s_waitcnt lgkmcnt(0)\n\ts_barrier" ::: "memory"); issue(ch + 2, ch & 1); });
            const unsigned t_ = cur; cur = nxt; nxt = t_;
        }
        s.O[1] = MFMA32(s.v1, BC8(s.ph[1]), s.O[1]);
        WAIT_BAR0();
        if (kh == 1) {
#pragma unroll
            for (int t = 0; t < 2; ++t)
#pragma unroll
                for (int r = 0; r < 11; ++r) xch[((qp * 2 + t) * 11 + r) * 64 + l] = s.O[t][r];
        }
        if (tid == 0) *flag = 0u;
        WAIT_BAR0();
        bool bad = false;
        if (kh == 0) {
#pragma unroll
            for (int t = 0; t < 2; ++t) {
                float am = 0.f; bool b = false;
#pragma unroll
                for (int r = 0; r < 11; ++r) { s.O[t][r] += xch[((qp * 2 + t) * 11 + r) * 64 + l]; am = __builtin_fmaxf(am, __builtin_fabsf(s.O[t][r])); b = b || (s.O[t][r] != s.O[t][r]); }
                bad = bad || b || !(am < 1.0e30f);
            }
        }
        if (attempt == 1) break;
        if (__any(bad)) *flag = 1u;
        WAIT_BAR0();
        if (rfl(*flag) == 0u) break;
    }
    if (kh == 0) {
#pragma unroll
        for (int t = 0; t < 2; ++t) {
            auto rr = __builtin_amdgcn_permlane32_swap(__float_as_uint(s.O[t][10]), __float_as_uint(s.O[t][10]), false, false);
            const float inv = 1.0f / __uint_as_float(rr[0]);
            const int tok = tok0 + 32 * t + r32, tb = tok >> 4;
            char* base = P.ws + WS_HC + (size_t)tb * 90 * 1024;
            const u32x4 m = {pk2(s.O[t][0] * inv, s.O[t][1] * inv), pk2(s.O[t][2] * inv, s.O[t][3] * inv), pk2(s.O[t][4] * inv, s.O[t][5] * inv), pk2(s.O[t][6] * inv, s.O[t][7] * inv)};
            *(u32x4*)(base + (head >> 1) * 1024 + ((tok & 15) + 16 * (2 * (head & 1) + h)) * 16) = m;
            if (h == 0) *(unsigned*)(base + (16 + (head >> 4)) * 1024 + ((tok & 15) + 16 * ((head & 15) >> 2)) * 16 + (head & 3) * 4) = pk2(s.O[t][8] * inv, s.O[t][9] * inv);
        }
    }
}

DI void norm_unit(const Params& P, int n) {
    const int tid = threadIdx.x, l = tid & 63, w = tid >> 6, sb = n >> 2, qt = n & 3, g = l >> 4;
    const float* st = (const float*)(P.ws + WS_ST);
    float mu[8], rs[8];
#pragma unroll
    for (int j = 0; j < 8; ++j) {
        const int s = sb * 32 + 8 * g + j; float a = 0.f, b = 0.f;
        const f32x2 v = *(const f32x2*)(st + (s * 16 + (l & 15)) * 2);
        a = row16_sum(v[0]); b = row16_sum(v[1]);
        const float m = a * (1.0f / FH), var = __builtin_fmaxf(b * (1.0f / FH) - m * m, 0.f);
        mu[j] = m; rs[j] = __builtin_amdgcn_rsqf(var + 1e-5f);
    }
    char* base = P.ws + WS_Z2 + (size_t)sb * 1024 + l * 16;
#pragma unroll
    for (int i = 0; i < 9; ++i) {
        const int fb = qt * 36 + w * 9 + i;
        u32x4 v = *(const u32x4*)(base + (size_t)fb * 65536);
#pragma unroll
        for (int e = 0; e < 4; ++e) {
            const float lo = (__uint_as_float(v[e] << 16) - mu[2 * e]) * rs[2 * e], hi = (__uint_as_float(v[e] & 0xffff0000u) - mu[2 * e + 1]) * rs[2 * e + 1];
            v[e] = pk2(lo, hi);
        }
        *(u32x4*)(base + (size_t)fb * 65536) = v;
    }
}

__global__ __launch_bounds__(256, 3) void k2_kernel(Params P) {
    extern __shared__ __attribute__((aligned(16))) char smem[];
    const unsigned lds0 = (unsigned)(uintptr_t)smem;
    const int b = blockIdx.x;
    if (b < 512) attn_unit(P, (b & 7) * 4 + ((b >> 3) & 3), b >> 5, lds0, smem);
    else norm_unit(P, b - 512);
}

constexpr int SP_NS = 4, SP_STAGE = 16384 + 20480, SP_LDS = SP_NS * SP_STAGE;

template <int SLOT>
DI void sp_stage(f32x4 (&acc)[9][2], bf16x8 (&zf)[9], bf16x8 (&ta)[2], bf16x8 (&tb_)[2], const unsigned (&vz)[4], const unsigned (&vt)[4][2], const unsigned (&vA)[4], const unsigned (&vZ)[5],
                 u32x4 srdA, u32x4 srdZ, unsigned sWA, unsigned sWZ, unsigned sA0, unsigned sZ0, unsigned sA1, unsigned sZ1) {
    constexpr int NXT = (SLOT + 1) & 3, D0 = (SLOT + 2) & 3, D1 = (SLOT + 3) & 3;
#pragma unroll
    for (int n = 0; n < 9; ++n) {
        if (n == 0) LGKM(4);
        if (n == 5) LGKM(7);
        if (n & 1) { set_m0(sWZ, D0 * SP_STAGE + (1 + (n >> 1)) * 4096); }
        mfma16a(acc[n][0], zf[n], ta[0]);
        if (n & 1) { SB(); dma_go(srdZ, vZ[1 + (n >> 1)], sZ0); }
        mfma16a(acc[n][1], zf[n], ta[1]);
        if (n < 2) tb_[n] = lds_rd128(vt[SLOT][1] + n * 2048);
        zf[n] = lds_rd128(vz[SLOT] + (2 * n + 1) * 1024);
        SB();
    }
    asm volatile("s_waitcnt vmcnt(9)\n\ts_barrier" ::: "memory");
    SB();
#pragma unroll
    for (int n = 0; n < 9; ++n) {
        if (n == 0) LGKM(4);
        if (n == 5) LGKM(7);
        if ((n & 1) == 0) { if (n < 8) set_m0(sWA, D1 * SP_STAGE + (n >> 1) * 1024); else set_m0(sWZ, D1 * SP_STAGE); }
        mfma16a(acc[n][0], zf[n], tb_[0]);
        if ((n & 1) == 0) { SB(); if (n < 8) dma_go(srdA, vA[n >> 1], sA1); else dma_go(srdZ, vZ[0], sZ1); }
        mfma16a(acc[n][1], zf[n], tb_[1]);
        if (n < 2) ta[n] = lds_rd128(vt[NXT][0] + n * 2048);
        zf[n] = lds_rd128(vz[NXT] + (2 * n) * 1024);
        SB();
    }
}

DI void spatial_tile(const Params& P, int panel, int jt, unsigned lds0) {
    const int tid = threadIdx.x, l = tid & 63; const unsigned w = rfl(tid >> 6); const int g = l >> 4;
    const int lpanel = panel, ljt = jt;
    const u32x4 srdA = make_srd(P.wsp, S_ * S_ * 2), srdZ = make_srd(P.ws + WS_Z2 + (size_t)ljt * 9 * 65536, 9 * 65536);
    unsigned vA[4], vZ[5], vz[4], vt[4][2];
#pragma unroll
    for (int i = 0; i < 4; ++i) vA[i] = (unsigned)((lpanel * 128 + 32 * w + 8 * i + (l >> 3)) * (S_ * 2) + (((l & 7) ^ (l >> 3)) << 4));
#pragma unroll
    for (int i = 0; i < 5; ++i) { const unsigned p = w + 4 * i; vZ[i] = p < 18 ? (p >> 1) * 65536 + (p & 1) * 1024 + l * 16 : OOB; }
#pragma unroll
    for (int s = 0; s < 4; ++s) {
        vz[s] = lds0 + s * SP_STAGE + 16384 + l * 16;
#pragma unroll
        for (int kk = 0; kk < 2; ++kk) vt[s][kk] = lds0 + s * SP_STAGE + (unsigned)((32 * w + (l & 15)) * 128) + (((4 * kk + g) ^ (l & 7)) << 4);
    }
#pragma unroll
    for (int s = 0; s < 4; ++s) { asm volatile("" : "+v"(vz[s])); asm volatile("" : "+v"(vt[s][0])); asm volatile("" : "+v"(vt[s][1])); asm volatile("" : "+v"(vA[s])); }
#pragma unroll
    for (int i = 0; i < 5; ++i) asm volatile("" : "+v"(vZ[i]));
    const unsigned sWA = lds0 + w * 4096, sWZ = lds0 + 16384 + w * 1024;
    f32x4 acc[9][2];
    STAMP3(P, 0);
#pragma unroll
    for (int n = 0; n < 9; ++n) { acc[n][0] = f32x4{0, 0, 0, 0}; acc[n][1] = f32x4{0, 0, 0, 0}; }
    PIN18(acc, "s_nop 1");
#pragma unroll
    for (int st = 0; st < 3; ++st) {
#pragma unroll
        for (int i = 0; i < 4; ++i) { set_m0(sWA, st * SP_STAGE + i * 1024); asm volatile("s_nop 0"); dma_go(srdA, vA[i], st * 128); }
#pragma unroll
        for (int i = 0; i < (st < 2 ? 5 : 1); ++i) { set_m0(sWZ, st * SP_STAGE + i * 4096); asm volatile("s_nop 0"); dma_go(srdZ, vZ[i], st * 2048); }
    }
    asm volatile("s_waitcnt vmcnt(14)\n\ts_barrier" ::: "memory");
    STAMP3(P, 1);
    bf16x8 zf[9], ta[2], tb_[2];
#pragma unroll
    for (int tb = 0; tb < 2; ++tb) ta[tb] = lds_rd128(vt[0][0] + tb * 2048);
#pragma unroll
    for (int n = 0; n < 9; ++n) zf[n] = lds_rd128(vz[0] + (2 * n) * 1024);
    LGKM(0);
    for (int st = 0; st < 32; st += 4) {
#define OFA(s) ((s) < 32 ? (unsigned)(s) * 128u : OOB)
#define OFZ(s) ((s) < 32 ? (unsigned)(s) * 2048u : OOB)
        sp_stage<0>(acc, zf, ta, tb_, vz, vt, vA, vZ, srdA, srdZ, sWA, sWZ, 0, OFZ(st + 2), OFA(st + 3), OFZ(st + 3));
        sp_stage<1>(acc, zf, ta, tb_, vz, vt, vA, vZ, srdA, srdZ, sWA, sWZ, 0, OFZ(st + 3), OFA(st + 4), OFZ(st + 4));
        sp_stage<2>(acc, zf, ta, tb_, vz, vt, vA, vZ, srdA, srdZ, sWA, sWZ, 0, OFZ(st + 4), OFA(st + 5), OFZ(st + 5));
        sp_stage<3>(acc, zf, ta, tb_, vz, vt, vA, vZ, srdA, srdZ, sWA, sWZ, 0, OFZ(st + 5), OFA(st + 6), OFZ(st + 6));
    }
    asm volatile("s_waitcnt vmcnt(0) lgkmcnt(0)" ::: "memory");
    PIN18(acc, "s_nop 7\n\ts_nop 1");
    STAMP3(P, 2);
    const char* z1 = P.ws + WS_Z1 + ((size_t)(panel * 4 + w) * 144 + jt * 9) * 1024 + l * 16;
    float bs[2];
#pragma unroll
    for (int tb = 0; tb < 2; ++tb) bs[tb] = bf2f(P.bsp[panel * 128 + 32 * w + 16 * tb + (l & 15)]);
    unsigned hp[9][2][2];
#pragma unroll
    for (int n = 0; n < 9; ++n) {
        const u32x4 z = *(const u32x4*)(z1 + n * 1024);
#pragma unroll
        for (int tb = 0; tb < 2; ++tb) {
            hp[n][tb][0] = pk2(__uint_as_float(z[2 * tb] << 16) * (acc[n][tb][0] + bs[tb]), __uint_as_float(z[2 * tb] & 0xffff0000u) * (acc[n][tb][1] + bs[tb]));
            hp[n][tb][1] = pk2(__uint_as_float(z[2 * tb + 1] << 16) * (acc[n][tb][2] + bs[tb]), __uint_as_float(z[2 * tb + 1] & 0xffff0000u) * (acc[n][tb][3] + bs[tb]));
        }
    }
    const int fb0 = jt * 9;
#pragma unroll
    for (int tb = 0; tb < 2; ++tb) {
        char* base = P.ws + WS_HC + ((size_t)(panel * 8 + 2 * w + tb) * 90 + 18) * 1024 + l * 16;
        if ((jt & 1) == 0) {
#pragma unroll
            for (int n = 0; n < 8; n += 2) *(u32x4*)(base + (size_t)((fb0 + n) >> 1) * 1024) = u32x4{hp[n][tb][0], hp[n][tb][1], hp[n + 1][tb][0], hp[n + 1][tb][1]};
            *(u32x2*)(base + (size_t)((fb0 + 8) >> 1) * 1024) = u32x2{hp[8][tb][0], hp[8][tb][1]};
        } else {
            *(u32x2*)(base + (size_t)(fb0 >> 1) * 1024 + 8) = u32x2{hp[0][tb][0], hp[0][tb][1]};
#pragma unroll
            for (int n = 1; n < 9; n += 2) *(u32x4*)(base + (size_t)((fb0 + n) >> 1) * 1024) = u32x4{hp[n][tb][0], hp[n][tb][1], hp[n + 1][tb][0], hp[n + 1][tb][1]};
        }
    }
}

__global__ __launch_bounds__(256, 2) void k3_kernel(Params P) {
    extern __shared__ __attribute__((aligned(16))) char smem[];
    const unsigned lds0 = (unsigned)(uintptr_t)smem;
    const int b = blockIdx.x, x = b & 7, i = b >> 3;
    spatial_tile(P, (x >> 2) * 8 + (i & 7), (x & 3) * 4 + (i >> 3), lds0);
    STAMP3(P, 3);
}

constexpr int FN_KS = 6, FN_STAGE = 32 * 1024, FN_LDS = 3 * FN_STAGE;

struct FnRegs { bf16x8 wf[5]; u32x4 h[2][FN_KS]; unsigned vW[8], vrd[3], vl; };
DI void fn_ldh(u32x4& d, u32x4 srd, unsigned voff, unsigned soff, int imm) {
    if (imm == 0) asm volatile("buffer_load_dwordx4 %0, %1, %2, %3 offen" : "=v"(d) : "v"(voff), "s"(srd), "s"(soff) : "memory");
    else if (imm == 1) asm volatile("buffer_load_dwordx4 %0, %1, %2, %3 offen offset:1024" : "=v"(d) : "v"(voff), "s"(srd), "s"(soff) : "memory");
    else if (imm == 2) asm volatile("buffer_load_dwordx4 %0, %1, %2, %3 offen offset:2048" : "=v"(d) : "v"(voff), "s"(srd), "s"(soff) : "memory");
    else asm volatile("buffer_load_dwordx4 %0, %1, %2, %3 offen offset:3072" : "=v"(d) : "v"(voff), "s"(srd), "s"(soff) : "memory");
}
template <int SLOT, int HB>
DI void fn_stage(f32x4 (&acc)[5], FnRegs& r, u32x4 srdW, u32x4 srdH, unsigned sWW, unsigned sH, unsigned sH4, unsigned sW) {
    constexpr int D = (SLOT + 2) % 3;
    asm volatile("s_waitcnt vmcnt(8)\n\ts_barrier" : "+v"(r.h[HB][0]), "+v"(r.h[HB][1]), "+v"(r.h[HB][2]), "+v"(r.h[HB][3]), "+v"(r.h[HB][4]), "+v"(r.h[HB][5])::"memory");
    SB();
#pragma unroll
    for (int n = 0; n < 5; ++n) r.wf[n] = lds_rd128(r.vrd[SLOT] + (n * FN_KS) * 1024);
#pragma unroll
    for (int kk = 0; kk < FN_KS; ++kk) fn_ldh(r.h[HB ^ 1][kk], srdH, r.vl, kk < 4 ? sH : sH4, kk & 3);
    SB();
#pragma unroll
    for (int kk = 0; kk < FN_KS; ++kk) {
#pragma unroll
        for (int n = 0; n < 5; ++n) {
            const int i = kk * 5 + n;
            const bool dm = (i % 4 == 0) && (i / 4 < 8);
            if (dm) set_m0(sWW, D * FN_STAGE + (i / 4) * 4096);
            mfma16a(acc[n], r.wf[n], BC8(r.h[HB][kk]));
            if (dm) dma_go(srdW, r.vW[i / 4], sW);
            if (kk + 1 < FN_KS) r.wf[n] = lds_rd128(r.vrd[SLOT] + (n * FN_KS + kk + 1) * 1024);
            SB();
        }
    }
}

DI void final_tile(const Params& P, int tt, int j, unsigned lds0) {
    const int tid = threadIdx.x, l = tid & 63; const unsigned w = rfl(tid >> 6); const int g = l >> 4;
    const int db0 = (9 * j) >> 1;
    const u32x4 srdW = make_srd(P.ws + WS_WC + (size_t)db0 * 90 * 1024, 5 * 90 * 1024);
    const u32x4 srdH = make_srd(P.ws + WS_HC + (size_t)(tt * 4 + w) * 90 * 1024, 90 * 1024);
    FnRegs r;
    r.vl = (unsigned)l * 16; OPAQUE(r.vl);
#pragma unroll
    for (int i = 0; i < 8; ++i) { const unsigned p = w + 4 * i, db = p / 6, kk = p - db * 6; r.vW[i] = p < 30 ? (db * 90 + kk) * 1024 + l * 16 : OOB; OPAQUE(r.vW[i]); }
#pragma unroll
    for (int s = 0; s < 3; ++s) { r.vrd[s] = lds0 + s * FN_STAGE + l * 16; OPAQUE(r.vrd[s]); }
    const unsigned sWW = lds0 + w * 1024;
    f32x4 accA[5], accB[5];
#pragma unroll
    for (int n = 0; n < 5; ++n) { accA[n] = f32x4{0, 0, 0, 0}; accB[n] = f32x4{0, 0, 0, 0}; }
    PIN5(accA, "s_nop 1"); PIN5(accB, "s_nop 1");
#pragma unroll
    for (int i = 0; i < 8; ++i) { set_m0(sWW, i * 4096); asm volatile("s_nop 0"); dma_go(srdW, r.vW[i], 0); }
#pragma unroll
    for (int kk = 0; kk < FN_KS; ++kk) fn_ldh(r.h[0][kk], srdH, r.vl, kk < 4 ? 0u : 4096u, kk & 3);
#pragma unroll
    for (int i = 0; i < 8; ++i) { set_m0(sWW, FN_STAGE + i * 4096); asm volatile("s_nop 0"); dma_go(srdW, r.vW[i], FN_KS * 1024); }
#define FH_(s) ((s) < 15 ? (unsigned)(s) * (FN_KS * 1024u) : OOB)
#define FH4(s) ((s) < 15 ? (unsigned)(s) * (FN_KS * 1024u) + 4096u : OOB)
    fn_stage<0, 0>(accA, r, srdW, srdH, sWW, FH_(1), FH4(1), FH_(2));
    fn_stage<1, 1>(accA, r, srdW, srdH, sWW, FH_(2), FH4(2), FH_(3));
    fn_stage<2, 0>(accA, r, srdW, srdH, sWW, FH_(3), FH4(3), FH_(4));
    PIN5(accA, "s_nop 7\n\ts_nop 1");
    for (int s = 3; s < 15; s += 6) {
        fn_stage<0, 1>(accB, r, srdW, srdH, sWW, FH_(s + 1), FH4(s + 1), FH_(s + 2));
        fn_stage<1, 0>(accB, r, srdW, srdH, sWW, FH_(s + 2), FH4(s + 2), FH_(s + 3));
        fn_stage<2, 1>(accB, r, srdW, srdH, sWW, FH_(s + 3), FH4(s + 3), FH_(s + 4));
        fn_stage<0, 0>(accB, r, srdW, srdH, sWW, FH_(s + 4), FH4(s + 4), FH_(s + 5));
        fn_stage<1, 1>(accB, r, srdW, srdH, sWW, FH_(s + 5), FH4(s + 5), FH_(s + 6));
        fn_stage<2, 0>(accB, r, srdW, srdH, sWW, FH_(s + 6), FH4(s + 6), FH_(s + 7));
    }
    asm volatile("s_waitcnt vmcnt(0) lgkmcnt(0)\n\ts_nop 7" : "+v"(r.h[0][0]), "+v"(r.h[0][1]), "+v"(r.h[0][2]), "+v"(r.h[0][3]), "+v"(r.h[0][4]), "+v"(r.h[0][5]),
                 "+v"(r.h[1][0]), "+v"(r.h[1][1]), "+v"(r.h[1][2]), "+v"(r.h[1][3]), "+v"(r.h[1][4]), "+v"(r.h[1][5])::"memory");
    PIN5(accB, "s_nop 7\n\ts_nop 1"); PIN5(accA, "");
    const int tok = tt * 64 + 16 * w + (l & 15);
    const float gt = ((const float*)(P.ws + WS_GT))[tok];
#pragma unroll
    for (int n = 0; n < 5; ++n) {
        const int d = (db0 + n) * 16 + 4 * g;
        if (d >= 72 * j && d < 72 * j + 72) {
            float v[4];
#pragma unroll
            for (int rr = 0; rr < 4; ++rr) v[rr] = gt * accA[n][rr] + (1.0f - gt) * accB[n][rr];
            *(u32x2*)(P.out + tok * 576 + d) = u32x2{pk2(v[0], v[1]), pk2(v[2], v[3])};
        }
    }
}

__global__ __launch_bounds__(256, 2) void k4_kernel(Params P) {
    extern __shared__ __attribute__((aligned(16))) char smem[];
    const unsigned lds0 = (unsigned)(uintptr_t)smem;
    const int b = blockIdx.x, x = b & 7, i = b >> 3;
    final_tile(P, x * 4 + (i & 3), i >> 2, lds0);
}

extern "C" void kernel_launch(void* const* d_in, const int* in_sizes, int n_in, void* d_out, int out_size, void* d_ws, size_t ws_size, hipStream_t stream) {
    Params P;
    P.x = (const u16*)d_in[0]; P.wqkv = (const u16*)d_in[1]; P.wo = (const u16*)d_in[2]; P.win = (const u16*)d_in[4]; P.wsp = (const u16*)d_in[5];
    P.bsp = (const u16*)d_in[6]; P.wout = (const u16*)d_in[7]; P.gatew = (const u16*)d_in[8]; P.out = (u16*)d_out; P.ws = (char*)d_ws;
    hipFuncSetAttribute((const void*)k1_kernel, hipFuncAttributeMaxDynamicSharedMemorySize, G1_LDS);
    hipFuncSetAttribute((const void*)k2_kernel, hipFuncAttributeMaxDynamicSharedMemorySize, 60*1024);
    hipFuncSetAttribute((const void*)k3_kernel, hipFuncAttributeMaxDynamicSharedMemorySize, SP_LDS);
    hipFuncSetAttribute((const void*)k4_kernel, hipFuncAttributeMaxDynamicSharedMemorySize, FN_LDS);
    k1_kernel<<<768, 256, G1_LDS, stream>>>(P);
    k2_kernel<<<768, 256, AT_LDS, stream>>>(P);
    k3_kernel<<<256, 256, SP_LDS, stream>>>(P);
    k4_kernel<<<256, 256, FN_LDS, stream>>>(P);
}
```

```cpp
#include <hip/hip_runtime.h>
#include <stdint.h>

typedef __bf16 bf16x8 __attribute__((ext_vector_type(8)));
typedef __bf16 bf16x2 __attribute__((ext_vector_type(2)));
typedef short s16x4 __attribute__((ext_vector_type(4)));
typedef float f32x2 __attribute__((ext_vector_type(2)));
typedef float f32x4 __attribute__((ext_vector_type(4)));
typedef float f32x16 __attribute__((ext_vector_type(16)));
typedef unsigned u32x2 __attribute__((ext_vector_type(2)));
typedef unsigned u32x4 __attribute__((ext_vector_type(4)));
typedef unsigned short u16;
typedef unsigned long long u64;
typedef __attribute__((address_space(1))) unsigned long long gu64;
#define LAS __attribute__((address_space(3)))
#define DI __device__ __forceinline__

constexpr int S_ = 2048, D_ = 576, FH = 2304;
constexpr unsigned WS_EP = 0;
constexpr unsigned WS_FL = 256;
constexpr unsigned FL_ST = 0, FL_Z = 256, FL_H = 512, FL_QKV = 768, FL_WP = 960, FL_WC = 1024, FL_O = 1088, FL_N = 1344;
constexpr unsigned WS_GT = 16384;
constexpr unsigned WS_ST = WS_GT + 8192;
constexpr unsigned WS_KM = WS_ST + 16 * 16 * 1024;
constexpr unsigned WS_KR = WS_KM + 32 * 64 * 1024;
constexpr unsigned WS_VT = WS_KR + 32 * 2048 * 4;
constexpr unsigned WS_QM = WS_VT + 32 * 128 * 640;
constexpr unsigned WS_QR = WS_QM + 32 * 64 * 1024;
constexpr unsigned WS_Z2 = WS_QR + 32 * 2048 * 4;
constexpr unsigned WS_WP = WS_Z2 + 144 * 64 * 1024;
constexpr unsigned WS_HC = WS_WP + 128 * 64 * 1024;
constexpr unsigned WS_WC = WS_HC + 128 * 90 * 1024;
constexpr unsigned WS_END = WS_WC + 36 * 90 * 1024;
constexpr size_t WS_DBG = 200u << 20;

struct Params {
    const u16* x; const u16* wqkv; const u16* wo; const u16* win; const u16* wsp; const u16* bsp; const u16* wout; const u16* gatew;
    u16* out; char* ws;
};

#define STAMPV(P_, idx) do {} while (0)

DI float bf2f(u16 v) { return __uint_as_float((unsigned)v << 16); }
DI float bflo(unsigned v) { return __uint_as_float(v << 16); }
DI float bfhi(unsigned v) { return __uint_as_float(v & 0xffff0000u); }
DI unsigned pk2(float lo, float hi) { f32x2 v = {lo, hi}; bf16x2 b = __builtin_convertvector(v, bf16x2); return __builtin_bit_cast(unsigned, b); }
DI unsigned rfl(unsigned v) { return __builtin_amdgcn_readfirstlane(v); }
DI u32x4 make_srd(const void* p, unsigned bytes) {
    uint64_t a = (uint64_t)p;
    u32x4 r; r[0] = rfl((unsigned)a); r[1] = rfl((unsigned)(a >> 32) & 0xffffu); r[2] = rfl(bytes); r[3] = 0x00020000u; return r;
}
#define SB() __builtin_amdgcn_sched_barrier(0)
#define BAR() asm volatile("s_waitcnt lgkmcnt(0)\n\ts_barrier" ::: "memory")
#define WAIT_BAR0() asm volatile("s_waitcnt vmcnt(0) lgkmcnt(0)\n\ts_barrier" ::: "memory")
DI void set_m0(unsigned base, unsigned imm) { asm volatile("s_add_u32 m0, %0, %1" ::"s"(base), "n"(imm) : "scc"); }
DI void set_m0s(unsigned a, unsigned b) { asm volatile("s_add_u32 m0, %0, %1" ::"s"(a), "s"(b) : "scc"); }
DI void dma_go(u32x4 srd, unsigned voff, unsigned soff) { asm volatile("buffer_load_dwordx4 %0, %1, %2 offen lds" ::"v"(voff), "s"(srd), "s"(soff) : "memory"); }
DI void dma_now(u32x4 srd, unsigned voff, unsigned soff, unsigned ldsaddr) { asm volatile("s_mov_b32 m0, %0\n\ts_nop 0\n\tbuffer_load_dwordx4 %1, %2, %3 offen lds" ::"s"(ldsaddr), "v"(voff), "s"(srd), "s"(soff) : "memory"); }
template <int IMM> DI void ld_frag(u32x4& d, u32x4 srd, unsigned voff, unsigned soff) {
    asm volatile("buffer_load_dwordx4 %0, %1, %2, %3 offen offset:%4" : "=v"(d) : "v"(voff), "s"(srd), "s"(soff), "n"(IMM) : "memory");
}
DI bf16x8 lds_rd128(unsigned a) { return *(const LAS bf16x8*)(uintptr_t)a; }
DI unsigned lds_rd32(unsigned a) { return *(const LAS unsigned*)(uintptr_t)a; }
DI s16x4 lds_rdtr(unsigned a) { return __builtin_amdgcn_ds_read_tr16_b64_v4i16((LAS s16x4*)(uintptr_t)a); }
DI bf16x8 cat8(s16x4 a, s16x4 b) { typedef short s16x8 __attribute__((ext_vector_type(8))); s16x8 v = __builtin_shufflevector(a, b, 0, 1, 2, 3, 4, 5, 6, 7); return __builtin_bit_cast(bf16x8, v); }
DI void mfma16v(f32x4& c, bf16x8 a, bf16x8 b) { asm volatile("v_mfma_f32_16x16x32_bf16 %0, %1, %2, %0" : "+v"(c) : "v"(a), "v"(b)); }
#define PIN18(acc, txt) asm volatile(txt : "+v"(acc[0][0]), "+v"(acc[0][1]), "+v"(acc[1][0]), "+v"(acc[1][1]), "+v"(acc[2][0]), "+v"(acc[2][1]), "+v"(acc[3][0]), "+v"(acc[3][1]), "+v"(acc[4][0]), "+v"(acc[4][1]), \
    "+v"(acc[5][0]), "+v"(acc[5][1]), "+v"(acc[6][0]), "+v"(acc[6][1]), "+v"(acc[7][0]), "+v"(acc[7][1]), "+v"(acc[8][0]), "+v"(acc[8][1]))
#define PIN5(acc, txt) asm volatile(txt : "+v"(acc[0]), "+v"(acc[1]), "+v"(acc[2]), "+v"(acc[3]), "+v"(acc[4]))
#define LGKM(n) __builtin_amdgcn_s_waitcnt(0xC07F | ((n) << 8))
#define OPAQUE(x) asm volatile("" : "+v"(x))
constexpr unsigned OOB = 0x40000000u;
#define EX2(x) __builtin_amdgcn_exp2f(x)
#define BC8(x) __builtin_bit_cast(bf16x8, x)
#define MFMA32(a, b, c) __builtin_amdgcn_mfma_f32_32x32x16_bf16(a, b, c, 0, 0, 0)

DI float gelu_t(float x) {
    const float c0 = -2.0f * 0.7978845608028654f * 1.4426950408889634f, c1 = c0 * 0.044715f;
    const float e = __builtin_amdgcn_exp2f(x * __builtin_fmaf(x * x, c1, c0));
    return x * __builtin_amdgcn_rcpf(1.0f + e);
}
template <int N> DI float dpp_ror_add(float v) { return v + __builtin_bit_cast(float, __builtin_amdgcn_update_dpp(0, __builtin_bit_cast(int, v), 0x120 + N, 0xf, 0xf, false)); }
DI float row16_sum(float v) { v = dpp_ror_add<8>(v); v = dpp_ror_add<4>(v); v = dpp_ror_add<2>(v); v = dpp_ror_add<1>(v); return v; }

struct Ctx { const Params& P; __amdgpu_buffer_rsrc_t ws; u64 tag; unsigned lds0; char* smem; };
DI void st128(const Ctx& c, unsigned off, u32x4 v) { __builtin_amdgcn_raw_buffer_store_b128(v, c.ws, (int)off, 0, 16); }
DI void st64(const Ctx& c, unsigned off, u32x2 v) { __builtin_amdgcn_raw_buffer_store_b64(v, c.ws, (int)off, 0, 16); }
DI void st32(const Ctx& c, unsigned off, unsigned v) { __builtin_amdgcn_raw_buffer_store_b32(v, c.ws, (int)off, 0, 16); }
DI gu64* flag_ptr(const Ctx& c, unsigned idx) { return (gu64*)(c.P.ws + WS_FL) + idx; }
DI void publish(const Ctx& c, unsigned idx) {
    asm volatile("s_waitcnt vmcnt(0) lgkmcnt(0)\n\ts_barrier" ::: "memory");
    if (threadIdx.x == 0) __hip_atomic_store(flag_ptr(c, idx), c.tag, __ATOMIC_RELAXED, __HIP_MEMORY_SCOPE_AGENT);
}
#define SPIN_CAP (1u << 16)
DI void wait_flags(const Ctx& c, unsigned idx) {
    if (threadIdx.x < 64) {
        gu64* p = flag_ptr(c, idx == ~0u ? 0u : idx);
        for (unsigned spins = 0; spins < SPIN_CAP; ++spins) {
            const u64 v = idx == ~0u ? c.tag : __hip_atomic_load(p, __ATOMIC_RELAXED, __HIP_MEMORY_SCOPE_AGENT);
            if (__all(v == c.tag)) break;
            __builtin_amdgcn_s_sleep(2);
        }
        __builtin_amdgcn_fence(__ATOMIC_ACQUIRE, "agent");
        asm volatile("s_waitcnt vmcnt(0)" ::: "memory");
    }
    asm volatile("s_waitcnt lgkmcnt(0)\n\ts_barrier" ::: "memory");
}

constexpr int G1_STAGE = 16384 + 20480;
struct G1Regs { bf16x8 wf[9], xa[2], xb[2]; unsigned vX[4], vW[5], vw0[2], vw1[2], vx0[2], vx1[2]; };
template <bool TRANS> DI void g1_mm(f32x4& c, bf16x8 wf, bf16x8 xf) { if (TRANS) mfma16v(c, wf, xf); else mfma16v(c, xf, wf); }
DI bf16x8 g1_rdw(unsigned lo, unsigned hi, int off) { return cat8(lds_rdtr(lo + off), lds_rdtr(hi + off)); }

template <bool TRANS, int SLOT>
DI void g1_stage(f32x4 (&acc)[9][2], G1Regs& r, u32x4 srdX, u32x4 srdW, unsigned sWX, unsigned sWW, unsigned sX, unsigned sW) {
    constexpr int NXT = SLOT ^ 1;
#pragma unroll
    for (int n = 0; n < 9; ++n) {
        if (n == 0) LGKM(8);
        if (n == 5) LGKM(12);
        g1_mm<TRANS>(acc[n][0], r.wf[n], r.xa[0]);
        if (n < 2) r.xb[n] = lds_rd128(r.vx1[SLOT] + n * 2048);
        g1_mm<TRANS>(acc[n][1], r.wf[n], r.xa[1]);
        r.wf[n] = g1_rdw(r.vw0[SLOT], r.vw1[SLOT], 32 * 288 + n * 32);
        SB();
    }
    asm volatile("s_waitcnt vmcnt(0) lgkmcnt(0)\n\ts_barrier" ::: "memory");
    LGKM(0);
    SB();
#pragma unroll
    for (int n = 0; n < 9; ++n) {
        if (n < 4) set_m0(sWX, SLOT * G1_STAGE + n * 1024); else set_m0(sWW, SLOT * G1_STAGE + (n - 4) * 4096);
        g1_mm<TRANS>(acc[n][0], r.wf[n], r.xb[0]);
        if (n < 4) dma_go(srdX, r.vX[n], sX); else dma_go(srdW, r.vW[n - 4], sW);
        g1_mm<TRANS>(acc[n][1], r.wf[n], r.xb[1]);
        if (n < 2) r.xa[n] = lds_rd128(r.vx0[NXT] + n * 2048);
        r.wf[n] = g1_rdw(r.vw0[NXT], r.vw1[NXT], n * 32);
        SB();
    }
}

template <bool TRANS>
DI void gemm1_main(const u16* x, const u16* W, int ldw, int row0, int col0, unsigned lds0, f32x4 (&acc)[9][2]) {
    const int tid = threadIdx.x, l = tid & 63; const unsigned w = rfl(tid >> 6);
    const u32x4 srdX = make_srd(x, S_ * D_ * 2), srdW = make_srd(W, (unsigned)(D_ * ldw * 2));
    const int g = l >> 4, q = (l & 15) >> 2, p = l & 3;
    G1Regs r;
#pragma unroll
    for (int i = 0; i < 4; ++i) { r.vX[i] = (unsigned)((row0 + 32 * w + 8 * i + (l >> 3)) * (D_ * 2) + (((l & 7) ^ (l >> 3)) << 4)); OPAQUE(r.vX[i]); }
#pragma unroll
    for (int i = 0; i < 5; ++i) { const int pc = (int)w + 4 * i, idx = 64 * pc + l, rho = idx / 18, c = idx - rho * 18, k = rho ^ (((rho >> 3) & 1) << 2);
        r.vW[i] = pc < 18 ? (unsigned)(k * ldw * 2 + (col0 + 8 * c) * 2) : OOB; OPAQUE(r.vW[i]); }
#pragma unroll
    for (int s = 0; s < 2; ++s) {
        const unsigned sb = lds0 + s * G1_STAGE;
        r.vw0[s] = sb + (unsigned)(16384 + (8 * g + (q ^ (4 * (g & 1)))) * 288 + p * 8); r.vw1[s] = sb + (unsigned)(16384 + (8 * g + ((q + 4) ^ (4 * (g & 1)))) * 288 + p * 8);
        r.vx0[s] = sb + (unsigned)((32 * w + (l & 15)) * 128) + ((g ^ (l & 7)) << 4); r.vx1[s] = sb + (unsigned)((32 * w + (l & 15)) * 128) + (((4 + g) ^ (l & 7)) << 4);
        OPAQUE(r.vw0[s]); OPAQUE(r.vw1[s]); OPAQUE(r.vx0[s]); OPAQUE(r.vx1[s]);
    }
    const unsigned sWX = lds0 + w * 4096, sWW = lds0 + 16384 + w * 1024, kst = (unsigned)(64 * ldw * 2);
#pragma unroll
    for (int st = 0; st < 2; ++st) {
#pragma unroll
        for (int i = 0; i < 4; ++i) { set_m0(sWX, st * G1_STAGE + i * 1024); asm volatile("s_nop 0"); dma_go(srdX, r.vX[i], st * 128); }
#pragma unroll
        for (int i = 0; i < 5; ++i) { set_m0(sWW, st * G1_STAGE + i * 4096); asm volatile("s_nop 0"); dma_go(srdW, r.vW[i], st * kst); }
    }
#pragma unroll
    for (int n = 0; n < 9; ++n) { acc[n][0] = f32x4{0, 0, 0, 0}; acc[n][1] = f32x4{0, 0, 0, 0}; }
    PIN18(acc, "s_nop 1");
    asm volatile("s_waitcnt vmcnt(9)\n\ts_barrier" ::: "memory");
#pragma unroll
    for (int tb = 0; tb < 2; ++tb) r.xa[tb] = lds_rd128(r.vx0[0] + tb * 2048);
#pragma unroll
    for (int n = 0; n < 9; ++n) r.wf[n] = g1_rdw(r.vw0[0], r.vw1[0], n * 32);
    LGKM(0);
    for (int st = 0; st < 8; st += 2) {
        g1_stage<TRANS, 0>(acc, r, srdX, srdW, sWX, sWW, st + 2 < 9 ? (st + 2) * 128u : OOB, st + 2 < 9 ? (st + 2) * kst : OOB);
        g1_stage<TRANS, 1>(acc, r, srdX, srdW, sWX, sWW, st + 3 < 9 ? (st + 3) * 128u : OOB, st + 3 < 9 ? (st + 3) * kst : OOB);
    }
    g1_stage<TRANS, 0>(acc, r, srdX, srdW, sWX, sWW, OOB, OOB);
    asm volatile("s_waitcnt vmcnt(0) lgkmcnt(0)\n\ts_barrier" ::: "memory");
    PIN18(acc, "s_nop 7\n\ts_nop 1");
}

DI void tile_z2(const Ctx& c, int panel, int jt) {
    f32x4 acc[9][2];
    gemm1_main<false>(c.P.x, c.P.win, 4608, panel * 128, FH + jt * 144, c.lds0, acc);
    STAMPV(c.P, 1);
    const int l = threadIdx.x & 63; const unsigned w = rfl(threadIdx.x >> 6); const int g = l >> 4;
    float s1[2][4], s2[2][4];
#pragma unroll
    for (int tb = 0; tb < 2; ++tb)
#pragma unroll
        for (int r = 0; r < 4; ++r) { s1[tb][r] = 0.f; s2[tb][r] = 0.f; }
#pragma unroll
    for (int n = 0; n < 9; ++n)
#pragma unroll
        for (int tb = 0; tb < 2; ++tb)
#pragma unroll
            for (int r = 0; r < 4; ++r) { const float y = gelu_t(acc[n][tb][r]); acc[n][tb][r] = y; s1[tb][r] += y; s2[tb][r] = __builtin_fmaf(y, y, s2[tb][r]); }
#pragma unroll
    for (int tb = 0; tb < 2; ++tb)
#pragma unroll
        for (int r = 0; r < 4; ++r) {
            const float a = row16_sum(s1[tb][r]), b = row16_sum(s2[tb][r]);
            if ((l & 15) == 0) st64(c, WS_ST + (unsigned)((panel * 16 + jt) * 1024 + (32 * w + 16 * tb + 4 * g + r) * 8), u32x2{__float_as_uint(a), __float_as_uint(b)});
        }
    publish(c, FL_ST + panel * 16 + jt);
    wait_flags(c, threadIdx.x < 16 ? FL_ST + panel * 16 + threadIdx.x : ~0u);
    {
        float a = 0.f, b = 0.f;
#pragma unroll
        for (int i = 0; i < 8; ++i) {
            const u32x2 v = __builtin_amdgcn_raw_buffer_load_b64(c.ws, (int)(WS_ST + (panel * 16 + 8 * (l >> 5) + i) * 1024 + (32 * w + (l & 31)) * 8), 0, 16);
            a += __uint_as_float(v[0]); b += __uint_as_float(v[1]);
        }
        auto ra = __builtin_amdgcn_permlane32_swap(__float_as_uint(a), __float_as_uint(a), false, false);
        auto rb = __builtin_amdgcn_permlane32_swap(__float_as_uint(b), __float_as_uint(b), false, false);
        a = __uint_as_float(ra[0]) + __uint_as_float(ra[1]); b = __uint_as_float(rb[0]) + __uint_as_float(rb[1]);
        const float m = a * (1.0f / FH), var = __builtin_fmaxf(b * (1.0f / FH) - m * m, 0.f);
        const float rs = 1.0f / __builtin_sqrtf(var + 1e-5f);
        if (l < 32) *(f32x2*)(c.smem + (w * 32 + l) * 8) = f32x2{m, rs};
        asm volatile("s_waitcnt lgkmcnt(0)" ::: "memory");
    }
    f32x2 ms[2][4];
#pragma unroll
    for (int tb = 0; tb < 2; ++tb)
#pragma unroll
        for (int r = 0; r < 4; ++r) ms[tb][r] = *(const f32x2*)(c.smem + (w * 32 + 16 * tb + 4 * g + r) * 8);
    const unsigned dst = WS_Z2 + (unsigned)(((jt * 9) * 64 + panel * 4 + w) * 1024 + l * 16);
#pragma unroll
    for (int n = 0; n < 9; ++n) {
        unsigned pk[2][2];
#pragma unroll
        for (int tb = 0; tb < 2; ++tb) {
            float y[4];
#pragma unroll
            for (int r = 0; r < 4; ++r) y[r] = (acc[n][tb][r] - ms[tb][r][0]) * ms[tb][r][1];
            pk[tb][0] = pk2(y[0], y[1]); pk[tb][1] = pk2(y[2], y[3]);
        }
        u32x4 o;
#pragma unroll
        for (int i = 0; i < 2; ++i) {
            auto a = __builtin_amdgcn_permlane32_swap(pk[0][i], pk[1][i], false, false);
            auto b = __builtin_amdgcn_permlane16_swap(a[0], a[1], false, false);
            o[i] = b[0]; o[2 + i] = b[1];
        }
        st128(c, dst + n * 65536, o);
    }
    publish(c, FL_Z + jt * 16 + panel);
}

DI void tile_z1(const Ctx& c, int panel, int jt, u32x4 (&z1p)[9]) {
    f32x4 acc[9][2];
    gemm1_main<true>(c.P.x, c.P.win, 4608, panel * 128, jt * 144, c.lds0, acc);
#pragma unroll
    for (int n = 0; n < 9; ++n)
#pragma unroll
        for (int tb = 0; tb < 2; ++tb) { z1p[n][2 * tb] = pk2(gelu_t(acc[n][tb][0]), gelu_t(acc[n][tb][1])); z1p[n][2 * tb + 1] = pk2(gelu_t(acc[n][tb][2]), gelu_t(acc[n][tb][3])); }
}

DI void tile_qk(const Ctx& c, int panel, int which, int hq) {
    f32x4 acc[9][2];
    gemm1_main<true>(c.P.x, c.P.wqkv, 1728, panel * 128, which * 576 + hq * 144, c.lds0, acc);
    const int l = threadIdx.x & 63; const unsigned w = rfl(threadIdx.x >> 6); const int g = l >> 4;
    const float sc = which == 0 ? 0.23570226039551584f * 1.4426950408889634f : 1.0f;
    char* st = c.smem + w * 9216;
#pragma unroll
    for (int n = 0; n < 9; ++n)
#pragma unroll
        for (int pp = 0; pp < 2; ++pp) {
            const int cc = 16 * n + 4 * g + 2 * pp, hl = cc / 18, d = cc - 18 * hl;
#pragma unroll
            for (int tb = 0; tb < 2; ++tb) {
                const int tok = 16 * tb + (l & 15);
                const int off = d < 16 ? hl * 1024 + (tok + 32 * (d >> 3)) * 16 + (d & 7) * 2 : 8192 + hl * 128 + tok * 4;
                *(unsigned*)(st + off) = pk2(acc[n][tb][2 * pp] * sc, acc[n][tb][2 * pp + 1] * sc);
            }
        }
    asm volatile("s_waitcnt lgkmcnt(0)" ::: "memory");
    const unsigned dm = (which == 0 ? WS_QM : WS_KM) + (unsigned)(((hq * 8) * 64 + panel * 4 + w) * 1024 + l * 16);
#pragma unroll
    for (int h = 0; h < 8; ++h) st128(c, dm + h * 65536, *(const u32x4*)(st + h * 1024 + l * 16));
    st128(c, (which == 0 ? WS_QR : WS_KR) + (unsigned)(((hq * 8 + (l >> 3)) * 2048 + panel * 128 + 32 * w) * 4 + (l & 7) * 16), *(const u32x4*)(st + 8192 + l * 16));
    publish(c, FL_QKV + (which * 4 + hq) * 16 + panel);
}

DI void tile_v(const Ctx& c, int panel, int hq) {
    f32x4 acc[9][2];
    gemm1_main<false>(c.P.x, c.P.wqkv, 1728, panel * 128, 2 * 576 + hq * 144, c.lds0, acc);
    const int l = threadIdx.x & 63; const unsigned w = rfl(threadIdx.x >> 6); const int g = l >> 4;
    char* st = c.smem + w * 10240;
    if (l < 32) {
        const int blk = l >> 1, hh = l & 1;
        *(u32x4*)(st + blk * 640 + (hh * 20 + 18) * 16) = u32x4{0x3f803f80u, 0x3f803f80u, 0x3f803f80u, 0x3f803f80u};
        *(u32x4*)(st + blk * 640 + (hh * 20 + 19) * 16) = u32x4{0, 0, 0, 0};
    }
#pragma unroll
    for (int n = 0; n < 9; ++n) {
        const int cc = 16 * n + (l & 15), hl = cc / 18, d = cc - 18 * hl;
#pragma unroll
        for (int tb = 0; tb < 2; ++tb)
            *(u32x2*)(st + (hl * 2 + tb) * 640 + ((g & 1) * 20 + d) * 16 + (g >> 1) * 8) = u32x2{pk2(acc[n][tb][0], acc[n][tb][1]), pk2(acc[n][tb][2], acc[n][tb][3])};
    }
    asm volatile("s_waitcnt lgkmcnt(0)" ::: "memory");
    const unsigned dv = WS_VT + (unsigned)(((hq * 8) * 128 + panel * 8 + 2 * w) * 640);
#pragma unroll
    for (int i = 0; i < 10; ++i) { const int idx = 64 * i + l, h = idx / 80, o = idx - 80 * h; st128(c, dv + (unsigned)(h * 128 * 640 + o * 16), *(const u32x4*)(st + idx * 16)); }
    publish(c, FL_QKV + (8 + hq) * 16 + panel);
}

DI void prep_unit(const Ctx& c, int n) {
    const int tid = threadIdx.x, l = tid & 63; const unsigned w = rfl(tid >> 6); const int g = l >> 4;
    {
        const u32x4 srd = make_srd(c.P.wsp, S_ * S_ * 2);
        const int tb = 2 * n + (int)(w & 1), s0 = (int)(w >> 1) * 16;
        unsigned vsrc = (unsigned)((tb * 16 + (l >> 3)) * (S_ * 2) + (((l & 7) ^ (l >> 3)) << 4));
        const unsigned lw = c.lds0 + w * 16384;
        const unsigned rd0 = lw + (unsigned)((l & 15) * 128) + ((g ^ (l & 7)) << 4), rd1 = lw + (unsigned)((l & 15) * 128) + (((4 + g) ^ (l & 7)) << 4);
        for (int bt = 0; bt < 2; ++bt) {
#pragma unroll
            for (int i = 0; i < 8; ++i) {
                dma_now(srd, vsrc, (unsigned)((s0 + bt * 8 + i) * 128), lw + i * 2048);
                dma_now(srd, vsrc, (unsigned)((s0 + bt * 8 + i) * 128 + 8 * S_ * 2), lw + i * 2048 + 1024);
            }
            asm volatile("s_waitcnt vmcnt(0)" ::: "memory");
#pragma unroll
            for (int i = 0; i < 8; ++i) {
                const unsigned dst = WS_WP + (unsigned)((tb * 64 + 2 * (s0 + bt * 8 + i)) * 1024 + l * 16);
                st128(c, dst, __builtin_bit_cast(u32x4, lds_rd128(rd0 + i * 2048)));
                st128(c, dst + 1024, __builtin_bit_cast(u32x4, lds_rd128(rd1 + i * 2048)));
            }
            asm volatile("s_waitcnt lgkmcnt(0)" ::: "memory");
        }
        publish(c, FL_WP + n);
    }
    for (int kb = n; kb < 90; kb += 64) {
        const bool isO = kb < 18;
        const u32x4 srd = isO ? make_srd(c.P.wo, D_ * D_ * 2) : make_srd(c.P.wout, FH * D_ * 2);
#pragma unroll
        for (int i = 0; i < 9; ++i) {
            const int idx = 64 * ((int)w + 4 * i) + l, sg = idx / 72, ch = idx - 72 * sg, gg = sg >> 3, j = sg & 7;
            int row;
            if (kb < 16) { const int head = 2 * kb + (gg >> 1), h = gg & 1; row = 18 * head + (j < 4 ? 4 * h + j : 8 + 4 * h + (j - 4)); }
            else if (kb < 18) row = 18 * (16 * (kb - 16) + 4 * gg + (j >> 1)) + 16 + (j & 1);
            else row = 32 * (kb - 18) + (j < 4 ? 4 * gg + j : 16 + 4 * gg + (j - 4));
            dma_now(srd, (unsigned)(row * 1152 + ch * 16), 0u, c.lds0 + (w + 4 * i) * 1024);
        }
        WAIT_BAR0();
        const int q = (l & 15) >> 2, p = l & 3;
        const unsigned r0 = c.lds0 + (unsigned)((8 * g + q) * 1152 + p * 8);
#pragma unroll
        for (int i = 0; i < 9; ++i) {
            const int db = (int)w + 4 * i;
            st128(c, WS_WC + (unsigned)((db * 90 + kb) * 1024 + l * 16), __builtin_bit_cast(u32x4, cat8(lds_rdtr(r0 + db * 32), lds_rdtr(r0 + 4 * 1152 + db * 32))));
        }
        BAR();
    }
    for (int i = 0; i < 8; ++i) {
        const int row = n * 32 + (int)w * 8 + i; float a = 0.f;
        for (int ch = l; ch < 72; ch += 64) {
            const u32x4 xv = *(const u32x4*)(c.P.x + row * 576 + ch * 8), gv = *(const u32x4*)(c.P.gatew + ch * 8);
#pragma unroll
            for (int e = 0; e < 4; ++e) { a = __builtin_fmaf(bflo(xv[e]), bflo(gv[e]), a); a = __builtin_fmaf(bfhi(xv[e]), bfhi(gv[e]), a); }
        }
        for (int o = 32; o > 0; o >>= 1) a += __shfl_xor(a, o);
        if (l == 0) st32(c, WS_GT + row * 4, __float_as_uint(1.0f / (1.0f + __expf(-a))));
    }
    publish(c, FL_WC + n);
}

constexpr int AB_SLOT = 20 * 1024;
struct AttnState {
    f32x16 S[2], O[2]; u32x4 pl[2], ph[2];
    bf16x8 k0, v0, v1; u32x4 k1; u32x4 q0[2], q1[2];
};
#define AT_PRIO 1
#define AT_HI() do { __builtin_amdgcn_s_setprio(AT_PRIO); SB(); } while (0)
#define AT_LO() __builtin_amdgcn_s_setprio(0)
template <int T, class F>
DI void attn_slot(AttnState& s, unsigned aK, unsigned aR, unsigned aV, const f32x16& zero, F&& hook) {
    constexpr int U = T ^ 1;
    AT_HI(); s.S[U] = MFMA32(s.k0, BC8(s.q0[U]), zero); SB(); AT_LO();
    SB();
#pragma unroll
    for (int i = 0; i < 4; ++i) s.S[T][i] = EX2(s.S[T][i]);
    SB();
    AT_HI(); s.S[U] = MFMA32(BC8(s.k1), BC8(s.q1[U]), s.S[U]); SB(); AT_LO();
    SB();
    if (T == 0) { s.k0 = lds_rd128(aK); s.k1[0] = lds_rd32(aR); }
#pragma unroll
    for (int i = 4; i < 8; ++i) s.S[T][i] = EX2(s.S[T][i]);
    s.pl[T][0] = pk2(s.S[T][0], s.S[T][1]); s.pl[T][1] = pk2(s.S[T][2], s.S[T][3]);
    SB();
    AT_HI(); s.O[U] = MFMA32(s.v1, BC8(s.ph[U]), s.O[U]); SB(); AT_LO();
    SB();
    if (T == 0) s.v1 = lds_rd128(aV);
    hook();
    s.pl[T][2] = pk2(s.S[T][4], s.S[T][5]); s.pl[T][3] = pk2(s.S[T][6], s.S[T][7]);
    SB();
#pragma unroll
    for (int i = 8; i < 12; ++i) s.S[T][i] = EX2(s.S[T][i]);
    SB();
    AT_HI(); s.O[T] = MFMA32(s.v0, BC8(s.pl[T]), s.O[T]); SB(); AT_LO();
    SB();
    if (T == 1) s.v0 = lds_rd128(aV);
#pragma unroll
    for (int i = 12; i < 16; ++i) s.S[T][i] = EX2(s.S[T][i]);
    SB();
    s.ph[T][0] = pk2(s.S[T][8], s.S[T][9]); s.ph[T][1] = pk2(s.S[T][10], s.S[T][11]);
    s.ph[T][2] = pk2(s.S[T][12], s.S[T][13]); s.ph[T][3] = pk2(s.S[T][14], s.S[T][15]);
    SB();
}

DI void attn_unit(const Ctx& c, int head, int qb) {
    const int tid = threadIdx.x, l = tid & 63; const unsigned w = rfl(tid >> 6); const int h = l >> 5, r32 = l & 31; const unsigned lds0 = c.lds0;
    {
        const int hq = head >> 3;
        wait_flags(c, tid < 16 ? FL_QKV + (4 + hq) * 16 + tid : tid < 32 ? FL_QKV + (8 + hq) * 16 + (tid - 16) : tid < 34 ? FL_QKV + hq * 16 + 2 * qb + (tid - 32) : ~0u);
    }
    STAMPV(c.P, 2);
    const u32x4 srd = make_srd(c.P.ws + WS_KM, WS_QM - WS_KM);
    unsigned vl = (unsigned)l * 16; OPAQUE(vl);
    unsigned sLds[5], sSrc[5], sStep[5];
#pragma unroll
    for (int i = 0; i < 5; ++i) {
        const unsigned p = w + 4 * i;
        sLds[i] = lds0 + p * 1024;
        sSrc[i] = p < 8 ? (unsigned)(head * 65536) + p * 1024 : p < 18 ? (WS_VT - WS_KM) + (unsigned)(head * 81920) + (p - 8) * 1024
                : p == 18 ? (WS_KR - WS_KM) + (unsigned)(head * 8192) : OOB;
        sStep[i] = p < 8 ? 8192u : (p < 18 ? 10240u : (p == 18 ? 1024u : 0u));
    }
    auto piece = [&](int i, int ch, unsigned boff) { set_m0s(sLds[i], rfl(boff)); const unsigned so = ch < 8 ? sSrc[i] + (unsigned)ch * sStep[i] : OOB; asm volatile("s_nop 0"); dma_go(srd, vl, so); };
    auto issue = [&](int ch, unsigned boff) {
#pragma unroll
        for (int i = 0; i < 5; ++i) piece(i, ch, boff); };
    AttnState s;
    const int tok0 = qb * 256 + 64 * (int)w;
    issue(0, 0);
#pragma unroll
    for (int t = 0; t < 2; ++t) {
        s.q0[t] = *(const u32x4*)(c.P.ws + WS_QM + ((size_t)head * 64 + (tok0 >> 5) + t) * 1024 + l * 16);
        const unsigned qr = *(const unsigned*)(c.P.ws + WS_QR + ((size_t)head * 2048 + tok0 + 32 * t + r32) * 4);
        s.q1[t] = u32x4{h ? 0u : qr, 0u, 0u, 0u};
    }
    asm volatile("" : "+v"(s.q0[0]), "+v"(s.q0[1]), "+v"(s.q1[0]), "+v"(s.q1[1]));
    unsigned ka = lds0 + (unsigned)l * 16, ra = lds0 + 18432u + (unsigned)r32 * 4, va = lds0 + 8192u + (unsigned)(h * 20 + (r32 < 19 ? r32 : 19)) * 16;
    OPAQUE(ka); OPAQUE(ra); OPAQUE(va);
    const f32x16 zero = {};
    volatile LAS unsigned* flag = (volatile LAS unsigned*)(uintptr_t)(lds0 + 2 * AB_SLOT);

    for (int attempt = 0; attempt < 2; ++attempt) {
        float mx[2] = {-3.0e38f, -3.0e38f};
        {
            const u32x4 qz[2] = {u32x4{s.q1[0][0], 0u, 0u, 0u}, u32x4{s.q1[1][0], 0u, 0u, 0u}};
            const int nch = attempt ? 8 : 1, nblk = attempt ? 8 : 1;
            for (int ch = 0; ch < nch; ++ch) {
                if (attempt) { WAIT_BAR0(); issue(ch, 0); }
                WAIT_BAR0();
                for (int kb = 0; kb < nblk; ++kb) {
                    const bf16x8 k0 = lds_rd128(ka + kb * 1024);
                    const u32x4 k1 = u32x4{lds_rd32(ra + kb * 128), 0u, 0u, 0u};
#pragma unroll
                    for (int t = 0; t < 2; ++t) {
                        f32x16 sc = MFMA32(k0, BC8(s.q0[t]), zero); sc = MFMA32(BC8(k1), BC8(qz[t]), sc);
#pragma unroll
                        for (int r = 0; r < 16; ++r) mx[t] = __builtin_fmaxf(mx[t], sc[r]);
                    }
                }
            }
            if (attempt) { WAIT_BAR0(); issue(0, 0); WAIT_BAR0(); }
        }
#pragma unroll
        for (int t = 0; t < 2; ++t) {
            auto rr = __builtin_amdgcn_permlane32_swap(__float_as_uint(mx[t]), __float_as_uint(mx[t]), false, false);
            const float m = __builtin_fmaxf(__uint_as_float(rr[0]), __uint_as_float(rr[1]));
            const unsigned nb = __float_as_uint(-m), hi = nb & 0xffff0000u;
            const float r1 = -m - __uint_as_float(hi); const unsigned mid = __float_as_uint(r1) & 0xffff0000u;
            const float r2 = r1 - __uint_as_float(mid); const unsigned lo = __float_as_uint(r2) & 0xffff0000u;
            s.q1[t][1] = h ? 0u : ((hi >> 16) | mid); s.q1[t][2] = h ? 0u : (lo >> 16);
        }
        STAMPV(c.P, 3);
        s.O[0] = zero; s.O[1] = zero;
        s.k0 = lds_rd128(ka); s.k1 = u32x4{lds_rd32(ra), 0x3f803f80u, 0x00003f80u, 0u};
        s.v0 = lds_rd128(va); s.v1 = s.v0; s.ph[1] = u32x4{0u, 0u, 0u, 0u};
        s.S[0] = MFMA32(s.k0, BC8(s.q0[0]), zero); s.S[0] = MFMA32(BC8(s.k1), BC8(s.q1[0]), s.S[0]);
        unsigned cur = 0, nxt = AB_SLOT;
        auto nohook = [] {};
        for (int ch = 0; ch < 8; ++ch) {
            const unsigned cK = ka + cur, cR = ra + cur, cV = va + cur, nK = ka + nxt, nR = ra + nxt, nV = va + nxt;
#pragma unroll
            for (int j = 0; j < 7; ++j) {
                attn_slot<0>(s, cK + (j + 1) * 1024, cR + (j + 1) * 128, cV + j * 1280 + 640, zero, nohook);
                attn_slot<1>(s, 0, 0, cV + (j + 1) * 1280, zero, [&] { if (j < 5) piece(j, ch + 1, nxt); });
            }
            asm volatile("s_waitcnt vmcnt(0)\n\ts_barrier" ::: "memory");
            SB();
            attn_slot<0>(s, nK, nR, cV + 7 * 1280 + 640, zero, nohook);
            attn_slot<1>(s, 0, 0, nV, zero, [&] { asm volatile("s_barrier" ::: "memory"); });
            const unsigned t_ = cur; cur = nxt; nxt = t_;
        }
        s.O[1] = MFMA32(s.v1, BC8(s.ph[1]), s.O[1]);
        if (attempt == 1) break;
        bool bad = false;
#pragma unroll
        for (int t = 0; t < 2; ++t) {
            float am = 0.f; bool b = false;
#pragma unroll
            for (int r = 0; r < 11; ++r) { am = __builtin_fmaxf(am, __builtin_fabsf(s.O[t][r])); b = b || (s.O[t][r] != s.O[t][r]); }
            bad = bad || b || !(am < 1.0e30f);
        }
        if (tid == 0) *flag = 0u;
        WAIT_BAR0();
        if (__any(bad)) *flag = 1u;
        WAIT_BAR0();
        if (rfl(*flag) == 0u) break;
    }
    STAMPV(c.P, 4);
#pragma unroll
    for (int t = 0; t < 2; ++t) {
        auto rr = __builtin_amdgcn_permlane32_swap(__float_as_uint(s.O[t][10]), __float_as_uint(s.O[t][10]), false, false);
        const float inv = 1.0f / __uint_as_float(rr[0]);
        const int tok = tok0 + 32 * t + r32, tb = tok >> 4;
        const unsigned base = WS_HC + (unsigned)(tb * 90 * 1024);
        const u32x4 m = {pk2(s.O[t][0] * inv, s.O[t][1] * inv), pk2(s.O[t][2] * inv, s.O[t][3] * inv), pk2(s.O[t][4] * inv, s.O[t][5] * inv), pk2(s.O[t][6] * inv, s.O[t][7] * inv)};
        st128(c, base + (unsigned)((head >> 1) * 1024 + ((tok & 15) + 16 * (2 * (head & 1) + h)) * 16), m);
        if (h == 0) st32(c, base + (unsigned)((16 + (head >> 4)) * 1024 + ((tok & 15) + 16 * ((head & 15) >> 2)) * 16 + (head & 3) * 4), pk2(s.O[t][8] * inv, s.O[t][9] * inv));
    }
    publish(c, FL_O + qb * 32 + head);
}

constexpr int SD_N = 8, SD_SLOT = 9216;
struct SpdRegs { bf16x8 zf[9]; u32x4 t[4][2]; unsigned vZ[3], vz, vT; };
template <int U>
DI void spd_sub(f32x4 (&acc)[9][2], SpdRegs& r, u32x4 srdT, u32x4 srdZ, unsigned sWZ, unsigned w, unsigned sTa, unsigned sTb, unsigned sZ) {
    constexpr int NX = (U + 1) & (SD_N - 1), PF = (U + SD_N - 1) & (SD_N - 1), TU = U & 3, TP = (U + 3) & 3;
    asm volatile("s_waitcnt vmcnt(10)\n\ts_barrier" : "+v"(r.t[TU][0]), "+v"(r.t[TU][1])::"memory");
    SB();
#pragma unroll
    for (int n = 0; n < 9; ++n) {
        if (n == 0) LGKM(4);
        if (n == 5) LGKM(5);
        if (n == 4 || n == 6) set_m0(sWZ, PF * SD_SLOT + (n == 4 ? 0 : 4096));
        mfma16v(acc[n][0], r.zf[n], BC8(r.t[TU][0]));
        if (n == 0) ld_frag<0>(r.t[TP][0], srdT, r.vT, sTa);
        if (n == 2) ld_frag<0>(r.t[TP][1], srdT, r.vT, sTb);
        if (n == 4) dma_go(srdZ, r.vZ[0], sZ);
        if (n == 6) dma_go(srdZ, r.vZ[1], sZ);
        mfma16v(acc[n][1], r.zf[n], BC8(r.t[TU][1]));
        r.zf[n] = lds_rd128(r.vz + NX * SD_SLOT + n * 1024);
        SB();
    }
    if (w == 0) { set_m0(sWZ, PF * SD_SLOT + 8192); asm volatile("s_nop 0"); dma_go(srdZ, r.vZ[2], sZ); }
    SB();
}

DI void spatial_tile(const Ctx& c, int panel, int jt, const u32x4 (&z1p)[9]) {
    const int tid = threadIdx.x, l = tid & 63; const unsigned w = rfl(tid >> 6); const unsigned lds0 = c.lds0;
    wait_flags(c, tid < 16 ? FL_Z + jt * 16 + tid : tid < 20 ? FL_WP + 4 * panel + (tid - 16) : ~0u);
    STAMPV(c.P, 4);
    const u32x4 srdT = make_srd(c.P.ws + WS_WP + (size_t)(panel * 8 + 2 * w) * 65536, 2 * 65536), srdZ = make_srd(c.P.ws + WS_Z2 + (size_t)jt * 9 * 65536, 9 * 65536);
    SpdRegs r;
#pragma unroll
    for (int i = 0; i < 3; ++i) { r.vZ[i] = (w + 4 * i) * 65536 + l * 16; OPAQUE(r.vZ[i]); }
    r.vz = lds0 + l * 16; OPAQUE(r.vz);
    r.vT = l * 16; OPAQUE(r.vT);
    const unsigned sWZ = lds0 + w * 1024;
    f32x4 acc[9][2];
#pragma unroll
    for (int n = 0; n < 9; ++n) { acc[n][0] = f32x4{0, 0, 0, 0}; acc[n][1] = f32x4{0, 0, 0, 0}; }
    PIN18(acc, "s_nop 1");
#pragma unroll
    for (int u = 0; u < SD_N - 1; ++u) {
        if (u >= 4) { ld_frag<0>(r.t[u - 4][0], srdT, r.vT, (u - 4) * 1024); ld_frag<0>(r.t[u - 4][1], srdT, r.vT, 65536 + (u - 4) * 1024); }
#pragma unroll
        for (int i = 0; i < 2; ++i) { set_m0(sWZ, u * SD_SLOT + i * 4096); asm volatile("s_nop 0"); dma_go(srdZ, r.vZ[i], u * 1024); }
        if (w == 0) { set_m0(sWZ, u * SD_SLOT + 8192); asm volatile("s_nop 0"); dma_go(srdZ, r.vZ[2], u * 1024); }
    }
    asm volatile("s_waitcnt vmcnt(18)\n\ts_barrier" ::: "memory");
#pragma unroll
    for (int n = 0; n < 9; ++n) r.zf[n] = lds_rd128(r.vz + n * 1024);
    LGKM(0);
#define SO(v) ((v) < 64 ? (unsigned)(v) * 1024u : OOB)
#define SO2(v) ((v) < 64 ? (unsigned)(v) * 1024u + 65536u : OOB)
    for (int u = 0; u < 64; u += 8) {
        spd_sub<0>(acc, r, srdT, srdZ, sWZ, w, SO(u + 3), SO2(u + 3), SO(u + 7));
        spd_sub<1>(acc, r, srdT, srdZ, sWZ, w, SO(u + 4), SO2(u + 4), SO(u + 8));
        spd_sub<2>(acc, r, srdT, srdZ, sWZ, w, SO(u + 5), SO2(u + 5), SO(u + 9));
        spd_sub<3>(acc, r, srdT, srdZ, sWZ, w, SO(u + 6), SO2(u + 6), SO(u + 10));
        spd_sub<4>(acc, r, srdT, srdZ, sWZ, w, SO(u + 7), SO2(u + 7), SO(u + 11));
        spd_sub<5>(acc, r, srdT, srdZ, sWZ, w, SO(u + 8), SO2(u + 8), SO(u + 12));
        spd_sub<6>(acc, r, srdT, srdZ, sWZ, w, SO(u + 9), SO2(u + 9), SO(u + 13));
        spd_sub<7>(acc, r, srdT, srdZ, sWZ, w, SO(u + 10), SO2(u + 10), SO(u + 14));
    }
    asm volatile("s_waitcnt vmcnt(0) lgkmcnt(0)" : "+v"(r.t[0][0]), "+v"(r.t[0][1]), "+v"(r.t[1][0]), "+v"(r.t[1][1]), "+v"(r.t[2][0]), "+v"(r.t[2][1]), "+v"(r.t[3][0]), "+v"(r.t[3][1])::"memory");
    PIN18(acc, "s_nop 7\n\ts_nop 1");
    STAMPV(c.P, 5);
    float bs[2];
#pragma unroll
    for (int tb = 0; tb < 2; ++tb) bs[tb] = bf2f(c.P.bsp[panel * 128 + 32 * w + 16 * tb + (l & 15)]);
    unsigned hp[9][2][2];
#pragma unroll
    for (int n = 0; n < 9; ++n)
#pragma unroll
        for (int tb = 0; tb < 2; ++tb) {
            hp[n][tb][0] = pk2(bflo(z1p[n][2 * tb]) * (acc[n][tb][0] + bs[tb]), bfhi(z1p[n][2 * tb]) * (acc[n][tb][1] + bs[tb]));
            hp[n][tb][1] = pk2(bflo(z1p[n][2 * tb + 1]) * (acc[n][tb][2] + bs[tb]), bfhi(z1p[n][2 * tb + 1]) * (acc[n][tb][3] + bs[tb]));
        }
    const int fb0 = jt * 9;
#pragma unroll
    for (int tb = 0; tb < 2; ++tb) {
        const unsigned base = WS_HC + (unsigned)(((panel * 8 + 2 * w + tb) * 90 + 18) * 1024 + l * 16);
        if ((jt & 1) == 0) {
#pragma unroll
            for (int n = 0; n < 8; n += 2) st128(c, base + (unsigned)(((fb0 + n) >> 1) * 1024), u32x4{hp[n][tb][0], hp[n][tb][1], hp[n + 1][tb][0], hp[n + 1][tb][1]});
            st64(c, base + (unsigned)(((fb0 + 8) >> 1) * 1024), u32x2{hp[8][tb][0], hp[8][tb][1]});
        } else {
            st64(c, base + (unsigned)((fb0 >> 1) * 1024 + 8), u32x2{hp[0][tb][0], hp[0][tb][1]});
#pragma unroll
            for (int n = 1; n < 9; n += 2) st128(c, base + (unsigned)(((fb0 + n) >> 1) * 1024), u32x4{hp[n][tb][0], hp[n][tb][1], hp[n + 1][tb][0], hp[n + 1][tb][1]});
        }
    }
    publish(c, FL_H + panel * 16 + jt);
}

constexpr int FN_STAGE = 16 * 1024;
struct FnRegs { bf16x8 wf[5]; u32x4 h[4][3]; unsigned vW[4], vrd, vl; };
template <int U>
DI void fn_stage(f32x4 (&acc)[5], FnRegs& r, u32x4 srdW, u32x4 srdH, unsigned sWW, unsigned sNext) {
    constexpr int PF = (U + 3) & 3;
    asm volatile("s_waitcnt vmcnt(14)\n\ts_barrier" : "+v"(r.h[U][0]), "+v"(r.h[U][1]), "+v"(r.h[U][2])::"memory");
    SB();
#pragma unroll
    for (int n = 0; n < 5; ++n) r.wf[n] = lds_rd128(r.vrd + U * FN_STAGE + (n * 3) * 1024);
    ld_frag<0>(r.h[PF][0], srdH, r.vl, sNext); ld_frag<1024>(r.h[PF][1], srdH, r.vl, sNext); ld_frag<2048>(r.h[PF][2], srdH, r.vl, sNext);
    SB();
#pragma unroll
    for (int kk = 0; kk < 3; ++kk) {
#pragma unroll
        for (int n = 0; n < 5; ++n) {
            const int i = kk * 5 + n;
            const bool dm = (i % 4 == 1);
            if (dm) set_m0(sWW, PF * FN_STAGE + (i / 4) * 4096);
            mfma16v(acc[n], r.wf[n], BC8(r.h[U][kk]));
            if (dm) dma_go(srdW, r.vW[i / 4], sNext);
            if (kk + 1 < 3) r.wf[n] = lds_rd128(r.vrd + U * FN_STAGE + (n * 3 + kk + 1) * 1024);
            SB();
        }
    }
}
template <int NST>
DI void fn_range(f32x4 (&acc)[5], FnRegs& r, u32x4 srdW, u32x4 srdH, unsigned sWW, int kb0) {
    const unsigned o0 = (unsigned)kb0 * 1024u;
#define FO(s) ((s) < NST ? o0 + (unsigned)(s) * 3072u : OOB)
#pragma unroll
    for (int s = 0; s < 3; ++s) {
        ld_frag<0>(r.h[s][0], srdH, r.vl, o0 + s * 3072); ld_frag<1024>(r.h[s][1], srdH, r.vl, o0 + s * 3072); ld_frag<2048>(r.h[s][2], srdH, r.vl, o0 + s * 3072);
#pragma unroll
        for (int i = 0; i < 4; ++i) { set_m0(sWW, s * FN_STAGE + i * 4096); asm volatile("s_nop 0"); dma_go(srdW, r.vW[i], o0 + s * 3072); }
    }
    for (int s = 0; s + 4 <= NST; s += 4) {
        fn_stage<0>(acc, r, srdW, srdH, sWW, FO(s + 3)); fn_stage<1>(acc, r, srdW, srdH, sWW, FO(s + 4));
        fn_stage<2>(acc, r, srdW, srdH, sWW, FO(s + 5)); fn_stage<3>(acc, r, srdW, srdH, sWW, FO(s + 6));
    }
    if (NST % 4 == 2) { fn_stage<0>(acc, r, srdW, srdH, sWW, OOB); fn_stage<1>(acc, r, srdW, srdH, sWW, OOB); }
    asm volatile("s_waitcnt vmcnt(0) lgkmcnt(0)\n\ts_barrier" : "+v"(r.h[0][0]), "+v"(r.h[0][1]), "+v"(r.h[0][2]), "+v"(r.h[1][0]), "+v"(r.h[1][1]), "+v"(r.h[1][2]),
                 "+v"(r.h[2][0]), "+v"(r.h[2][1]), "+v"(r.h[2][2]), "+v"(r.h[3][0]), "+v"(r.h[3][1]), "+v"(r.h[3][2])::"memory");
    PIN5(acc, "s_nop 7\n\ts_nop 1");
}

DI void final_tile(const Ctx& c, int tt, int j) {
    const int tid = threadIdx.x, l = tid & 63; const unsigned w = rfl(tid >> 6); const int g = l >> 4;
    wait_flags(c, tid < 64 ? FL_WC + tid : ~0u);
    wait_flags(c, tid < 16 ? FL_H + (tt >> 1) * 16 + tid : ~0u);
    STAMPV(c.P, 6);
    const int db0 = (9 * j) >> 1;
    const u32x4 srdW = make_srd(c.P.ws + WS_WC + (size_t)db0 * 90 * 1024, 5 * 90 * 1024);
    const u32x4 srdH = make_srd(c.P.ws + WS_HC + (size_t)(tt * 4 + w) * 90 * 1024, 90 * 1024);
    FnRegs r;
    r.vl = (unsigned)l * 16; OPAQUE(r.vl);
#pragma unroll
    for (int i = 0; i < 4; ++i) { const unsigned p = w + 4 * i, db = p / 3, kk = p - db * 3; r.vW[i] = p < 15 ? (db * 90 + kk) * 1024 + l * 16 : OOB; OPAQUE(r.vW[i]); }
    r.vrd = c.lds0 + l * 16; OPAQUE(r.vrd);
    const unsigned sWW = c.lds0 + w * 1024;
    f32x4 accA[5], accB[5];
#pragma unroll
    for (int n = 0; n < 5; ++n) { accA[n] = f32x4{0, 0, 0, 0}; accB[n] = f32x4{0, 0, 0, 0}; }
    PIN5(accA, "s_nop 1"); PIN5(accB, "s_nop 1");
    fn_range<24>(accB, r, srdW, srdH, sWW, 18);
    STAMPV(c.P, 7);
    wait_flags(c, tid < 32 ? FL_O + (tt >> 2) * 32 + tid : ~0u);
    STAMPV(c.P, 8);
    fn_range<6>(accA, r, srdW, srdH, sWW, 0);
    const int tok = tt * 64 + 16 * w + (l & 15);
    const float gt = ((const float*)(c.P.ws + WS_GT))[tok];
#pragma unroll
    for (int n = 0; n < 5; ++n) {
        const int d = (db0 + n) * 16 + 4 * g;
        if (d >= 72 * j && d < 72 * j + 72) {
            float v[4];
#pragma unroll
            for (int rr = 0; rr < 4; ++rr) v[rr] = gt * accA[n][rr] + (1.0f - gt) * accB[n][rr];
            *(u32x2*)(c.P.out + tok * 576 + d) = u32x2{pk2(v[0], v[1]), pk2(v[2], v[3])};
        }
    }
}

constexpr int MEGA_LDS = 2 * G1_STAGE + 1024;
#define SP_PRIO 0
__global__ __launch_bounds__(256, 2) void mega_kernel(Params P) {
    extern __shared__ __attribute__((aligned(16))) char smem[];
    const int b = blockIdx.x, tid = threadIdx.x;
    STAMPV(P, 0);
    const u64 ep = __hip_atomic_load((gu64*)(P.ws + WS_EP), __ATOMIC_RELAXED, __HIP_MEMORY_SCOPE_AGENT);
    const u64 tag = (((u64)rfl((unsigned)(ep >> 32)) << 32) | rfl((unsigned)ep)) + 1ull;
    const Ctx c{P, __builtin_amdgcn_make_buffer_rsrc(P.ws, (short)0, (int)WS_END, 0x00020000), tag, (unsigned)(uintptr_t)smem, smem};
    if (b < 256) {
        const int x = b & 7, i = b >> 3, panel = (x >> 2) * 8 + (i & 7), jt = (x & 3) * 4 + (i >> 3);
        tile_z2(c, panel, jt);
        STAMPV(P, 2);
        u32x4 z1p[9];
        tile_z1(c, panel, jt, z1p);
        STAMPV(P, 3);
        __builtin_amdgcn_s_setprio(SP_PRIO);
        spatial_tile(c, panel, jt, z1p);
        final_tile(c, 2 * panel + (jt >> 3), jt & 7);
        STAMPV(P, 9);
        if (b == 0) {
            if (tid < 64) {
                for (unsigned spins = 0; spins < SPIN_CAP; ++spins) {
                    bool ok = true;
#pragma unroll
                    for (int k = 0; k < 4; ++k) {
                        ok = ok && __hip_atomic_load(flag_ptr(c, FL_H + 64 * k + tid), __ATOMIC_RELAXED, __HIP_MEMORY_SCOPE_AGENT) == tag;
                        ok = ok && __hip_atomic_load(flag_ptr(c, FL_O + 64 * k + tid), __ATOMIC_RELAXED, __HIP_MEMORY_SCOPE_AGENT) == tag;
                    }
                    if (__all(ok)) break;
                    __builtin_amdgcn_s_sleep(2);
                }
                if (tid == 0) __hip_atomic_store((gu64*)(P.ws + WS_EP), tag, __ATOMIC_RELAXED, __HIP_MEMORY_SCOPE_AGENT);
            }
        }
    } else {
        const int a = b - 256, x = a & 7, idx = a >> 3;
        if (idx < 24) {
            const int hq = x >> 1, ty = idx % 3, panel = (idx / 3) * 2 + (x & 1);
            if (ty < 2) tile_qk(c, panel, ty, hq); else tile_v(c, panel, hq);
        } else prep_unit(c, (idx - 24) * 8 + x);
        STAMPV(P, 1);
        attn_unit(c, x * 4 + (idx & 3), idx >> 2);
        STAMPV(P, 5);
    }
}

extern "C" void kernel_launch(void* const* d_in, const int* in_sizes, int n_in, void* d_out, int out_size, void* d_ws, size_t ws_size, hipStream_t stream) {
    Params P;
    P.x = (const u16*)d_in[0]; P.wqkv = (const u16*)d_in[1]; P.wo = (const u16*)d_in[2]; P.win = (const u16*)d_in[4]; P.wsp = (const u16*)d_in[5];
    P.bsp = (const u16*)d_in[6]; P.wout = (const u16*)d_in[7]; P.gatew = (const u16*)d_in[8]; P.out = (u16*)d_out; P.ws = (char*)d_ws;
    hipFuncSetAttribute((const void*)mega_kernel, hipFuncAttributeMaxDynamicSharedMemorySize, MEGA_LDS);
    mega_kernel<<<512, 256, MEGA_LDS, stream>>>(P);
}
```

```cpp
#include <hip/hip_runtime.h>
#include <stdint.h>

typedef __bf16 bf16x8 __attribute__((ext_vector_type(8)));
typedef __bf16 bf16x2 __attribute__((ext_vector_type(2)));
typedef short s16x4 __attribute__((ext_vector_type(4)));
typedef float f32x2 __attribute__((ext_vector_type(2)));
typedef float f32x4 __attribute__((ext_vector_type(4)));
typedef float f32x16 __attribute__((ext_vector_type(16)));
typedef unsigned u32x2 __attribute__((ext_vector_type(2)));
typedef unsigned u32x4 __attribute__((ext_vector_type(4)));
typedef unsigned short u16;
typedef unsigned long long u64;
typedef __attribute__((address_space(1))) unsigned long long gu64;
#define LAS __attribute__((address_space(3)))
#define DI __device__ __forceinline__

constexpr int S_ = 2048, D_ = 576, FH = 2304;
constexpr unsigned WS_EP = 0;
constexpr unsigned WS_FL = 256;
constexpr unsigned FL_ST = 0, FL_Z = 256, FL_H = 512, FL_QKV = 768, FL_WP = 960, FL_O = 1024, FL_WC = 1280, FL_N = FL_WC + 154;
constexpr unsigned WS_GT = 16384;
constexpr unsigned WS_ST = WS_GT + 8192;
constexpr unsigned WS_KM = WS_ST + 16 * 16 * 1024;
constexpr unsigned WS_KR = WS_KM + 32 * 64 * 1024;
constexpr unsigned WS_VT = WS_KR + 32 * 2048 * 4;
constexpr unsigned WS_QM = WS_VT + 32 * 128 * 640;
constexpr unsigned WS_QR = WS_QM + 32 * 64 * 1024;
constexpr unsigned WS_Z2 = WS_QR + 32 * 2048 * 4;
constexpr unsigned WS_WP = WS_Z2 + 144 * 64 * 1024;
constexpr unsigned WS_HC = WS_WP + 128 * 64 * 1024;
constexpr unsigned WS_WC = WS_HC + 128 * 90 * 1024;
constexpr unsigned WS_END = WS_WC + 36 * 90 * 1024;
constexpr size_t WS_DBG = 200u << 20;

#define ROT 1
#define PREP_NT 1
#define SC1_LOADS 1
struct Params {
    const u16* x; const u16* wqkv; const u16* wo; const u16* win; const u16* wsp; const u16* bsp; const u16* wout; const u16* gatew;
    u16* out; char* ws;
};

#define STAMPV(P_, idx) do {} while (0)

DI float bf2f(u16 v) { return __uint_as_float((unsigned)v << 16); }
DI float bflo(unsigned v) { return __uint_as_float(v << 16); }
DI float bfhi(unsigned v) { return __uint_as_float(v & 0xffff0000u); }
DI unsigned pk2(float lo, float hi) { f32x2 v = {lo, hi}; bf16x2 b = __builtin_convertvector(v, bf16x2); return __builtin_bit_cast(unsigned, b); }
DI unsigned rfl(unsigned v) { return __builtin_amdgcn_readfirstlane(v); }
DI u32x4 make_srd(const void* p, unsigned bytes) {
    uint64_t a = (uint64_t)p;
    u32x4 r; r[0] = rfl((unsigned)a); r[1] = rfl((unsigned)(a >> 32) & 0xffffu); r[2] = rfl(bytes); r[3] = 0x00020000u; return r;
}
#define SB() __builtin_amdgcn_sched_barrier(0)
#define BAR() asm volatile("s_waitcnt lgkmcnt(0)\n\ts_barrier" ::: "memory")
#define WAIT_BAR0() asm volatile("s_waitcnt vmcnt(0) lgkmcnt(0)\n\ts_barrier" ::: "memory")
DI void set_m0(unsigned base, unsigned imm) { asm volatile("s_add_u32 m0, %0, %1" ::"s"(base), "n"(imm) : "scc"); }
DI void set_m0s(unsigned a, unsigned b) { asm volatile("s_add_u32 m0, %0, %1" ::"s"(a), "s"(b) : "scc"); }
DI void dma_go(u32x4 srd, unsigned voff, unsigned soff) { asm volatile("buffer_load_dwordx4 %0, %1, %2 offen lds" ::"v"(voff), "s"(srd), "s"(soff) : "memory"); }
DI void dma_go_c(u32x4 srd, unsigned voff, unsigned soff) { asm volatile("buffer_load_dwordx4 %0, %1, %2 offen sc1 lds" ::"v"(voff), "s"(srd), "s"(soff) : "memory"); }
#define LDSC " sc1"
#define ACQUIRE() do {} while (0)
DI void dma_now(u32x4 srd, unsigned voff, unsigned soff, unsigned ldsaddr) { asm volatile("s_mov_b32 m0, %0\n\ts_nop 0\n\tbuffer_load_dwordx4 %1, %2, %3 offen nt lds" ::"s"(ldsaddr), "v"(voff), "s"(srd), "s"(soff) : "memory"); }
template <int IMM> DI void ld_frag(u32x4& d, u32x4 srd, unsigned voff, unsigned soff) {
    asm volatile("buffer_load_dwordx4 %0, %1, %2, %3 offen offset:%4" LDSC : "=v"(d) : "v"(voff), "s"(srd), "s"(soff), "n"(IMM) : "memory");
}
DI bf16x8 lds_rd128(unsigned a) { return *(const LAS bf16x8*)(uintptr_t)a; }
DI unsigned lds_rd32(unsigned a) { return *(const LAS unsigned*)(uintptr_t)a; }
DI s16x4 lds_rdtr(unsigned a) { return __builtin_amdgcn_ds_read_tr16_b64_v4i16((LAS s16x4*)(uintptr_t)a); }
DI bf16x8 cat8(s16x4 a, s16x4 b) { typedef short s16x8 __attribute__((ext_vector_type(8))); s16x8 v = __builtin_shufflevector(a, b, 0, 1, 2, 3, 4, 5, 6, 7); return __builtin_bit_cast(bf16x8, v); }
DI void mfma16v(f32x4& c, bf16x8 a, bf16x8 b) { asm volatile("v_mfma_f32_16x16x32_bf16 %0, %1, %2, %0" : "+v"(c) : "v"(a), "v"(b)); }
#define PIN18(acc, txt) asm volatile(txt : "+v"(acc[0][0]), "+v"(acc[0][1]), "+v"(acc[1][0]), "+v"(acc[1][1]), "+v"(acc[2][0]), "+v"(acc[2][1]), "+v"(acc[3][0]), "+v"(acc[3][1]), "+v"(acc[4][0]), "+v"(acc[4][1]), \
    "+v"(acc[5][0]), "+v"(acc[5][1]), "+v"(acc[6][0]), "+v"(acc[6][1]), "+v"(acc[7][0]), "+v"(acc[7][1]), "+v"(acc[8][0]), "+v"(acc[8][1]))
#define PIN5(acc, txt) asm volatile(txt : "+v"(acc[0]), "+v"(acc[1]), "+v"(acc[2]), "+v"(acc[3]), "+v"(acc[4]))
#define LGKM(n) __builtin_amdgcn_s_waitcnt(0xC07F | ((n) << 8))
#define OPAQUE(x) asm volatile("" : "+v"(x))
constexpr unsigned OOB = 0x40000000u;
#define EX2(x) __builtin_amdgcn_exp2f(x)
#define BC8(x) __builtin_bit_cast(bf16x8, x)
#define MFMA32(a, b, c) __builtin_amdgcn_mfma_f32_32x32x16_bf16(a, b, c, 0, 0, 0)

DI float gelu_t(float x) {
    const float c0 = -2.0f * 0.7978845608028654f * 1.4426950408889634f, c1 = c0 * 0.044715f;
    const float e = __builtin_amdgcn_exp2f(x * __builtin_fmaf(x * x, c1, c0));
    return x * __builtin_amdgcn_rcpf(1.0f + e);
}
template <int N> DI float dpp_ror_add(float v) { return v + __builtin_bit_cast(float, __builtin_amdgcn_update_dpp(0, __builtin_bit_cast(int, v), 0x120 + N, 0xf, 0xf, false)); }
DI float row16_sum(float v) { v = dpp_ror_add<8>(v); v = dpp_ror_add<4>(v); v = dpp_ror_add<2>(v); v = dpp_ror_add<1>(v); return v; }

struct Ctx { const Params& P; __amdgpu_buffer_rsrc_t ws; u64 tag; unsigned lds0; char* smem; };
#define ST_AUX 16
DI void st128(const Ctx& c, unsigned off, u32x4 v) { __builtin_amdgcn_raw_buffer_store_b128(v, c.ws, (int)off, 0, ST_AUX); }
DI void st64(const Ctx& c, unsigned off, u32x2 v) { __builtin_amdgcn_raw_buffer_store_b64(v, c.ws, (int)off, 0, 16); }
DI void st32(const Ctx& c, unsigned off, unsigned v) { __builtin_amdgcn_raw_buffer_store_b32(v, c.ws, (int)off, 0, 16); }
DI gu64* flag_ptr(const Ctx& c, unsigned idx) { return (gu64*)(c.P.ws + WS_FL) + idx; }
DI void publish(const Ctx& c, unsigned idx) {
    asm volatile("s_waitcnt vmcnt(0) lgkmcnt(0)\n\ts_barrier" ::: "memory");
    if (threadIdx.x == 0) __hip_atomic_store(flag_ptr(c, idx), c.tag, __ATOMIC_RELAXED, __HIP_MEMORY_SCOPE_AGENT);
}
#define POLL_SLEEP 8
#define SPIN_CAP (1u << 16)
DI void wait_flags(const Ctx& c, unsigned idx) {
    if (threadIdx.x < 64) {
        gu64* p = flag_ptr(c, idx == ~0u ? 0u : idx);
        for (unsigned spins = 0; spins < SPIN_CAP; ++spins) {
            const u64 v = idx == ~0u ? c.tag : __hip_atomic_load(p, __ATOMIC_RELAXED, __HIP_MEMORY_SCOPE_AGENT);
            if (__all(v == c.tag)) break;
            __builtin_amdgcn_s_sleep(POLL_SLEEP);
        }
        ACQUIRE();
        asm volatile("s_waitcnt vmcnt(0)" ::: "memory");
    }
    asm volatile("s_waitcnt lgkmcnt(0)\n\ts_barrier" ::: "memory");
}

DI void poll_flags(const Ctx& c, unsigned idx) {
    gu64* p = flag_ptr(c, idx == ~0u ? 0u : idx);
    for (unsigned spins = 0; spins < SPIN_CAP; ++spins) {
        const u64 v = idx == ~0u ? c.tag : __hip_atomic_load(p, __ATOMIC_RELAXED, __HIP_MEMORY_SCOPE_AGENT);
        if (__all(v == c.tag)) break;
        __builtin_amdgcn_s_sleep(POLL_SLEEP);
    }
}
DI void raise_flag(const Ctx& c, unsigned idx) { if (threadIdx.x == 0) __hip_atomic_store(flag_ptr(c, idx), c.tag, __ATOMIC_RELAXED, __HIP_MEMORY_SCOPE_AGENT); }
DI void wait_begin(const Ctx& c, unsigned idx) { if (threadIdx.x < 64) { poll_flags(c, idx); ACQUIRE(); } }
DI void wait_end() { asm volatile("s_waitcnt vmcnt(0) lgkmcnt(0)\n\ts_barrier" ::: "memory"); }

constexpr int G1_XS = 16384, G1_WS = 9216, G1_WOFF = 2 * G1_XS, G1_LDS = G1_WOFF + 4 * G1_WS;
struct G1Regs { bf16x8 wf[9], xf[2][2]; unsigned vX[4], vW[3], vw0, vw1, vx[2]; };
template <bool TRANS> DI void g1_mm(f32x4& c, bf16x8 wf, bf16x8 xf) { if (TRANS) mfma16v(c, wf, xf); else mfma16v(c, xf, wf); }
DI bf16x8 g1_rdw(unsigned lo, unsigned hi, int off) { return cat8(lds_rdtr(lo + off), lds_rdtr(hi + off)); }

template <bool TRANS, int J>
DI void g1_sub(f32x4 (&acc)[9][2], G1Regs& r, u32x4 srdX, u32x4 srdW, unsigned sWX, unsigned sWW, unsigned w, unsigned sX, unsigned sW) {
    constexpr int N1 = (J + 1) & 3, CUR = J & 1, NXT = CUR ^ 1, PW = (J + 3) & 3;
    constexpr int XS_RD = N1 >> 1, XS_WR = J >> 1;
    if (J & 1) asm volatile("s_waitcnt vmcnt(2) lgkmcnt(8)\n\ts_barrier" ::: "memory");
    else asm volatile("s_waitcnt vmcnt(6) lgkmcnt(8)\n\ts_barrier" ::: "memory");
    LGKM(8);
    SB();
#pragma unroll
    for (int n = 0; n < 9; ++n) {
        if (n == 5) LGKM(12);
        const bool dw = (n == 1 || n == 3), dx = (J & 1) && n >= 4 && n < 8;
        if (dw) set_m0(sWW, PW * G1_WS + (n == 1 ? 0 : 4096));
        if (dx) set_m0(sWX, XS_WR * G1_XS + (n - 4) * 1024);
        g1_mm<TRANS>(acc[n][0], r.wf[n], r.xf[CUR][0]);
        if (dw) dma_go(srdW, r.vW[n == 1 ? 0 : 1], sW);
        if (dx) dma_go(srdX, r.vX[n - 4], sX);
        g1_mm<TRANS>(acc[n][1], r.wf[n], r.xf[CUR][1]);
        if (n < 2) r.xf[NXT][n] = lds_rd128(r.vx[N1 & 1] + XS_RD * G1_XS + n * 2048);
        r.wf[n] = g1_rdw(r.vw0, r.vw1, N1 * G1_WS + n * 32);
        SB();
    }
    if (w == 0) { set_m0(sWW, PW * G1_WS + 8192); asm volatile("s_nop 0"); dma_go(srdW, r.vW[2], sW); }
    SB();
}

template <bool TRANS>
DI void gemm1_main(const Ctx& c, const u16* x, const u16* W, int ldw, int row0, int col0, unsigned lds0, f32x4 (&acc)[9][2], int s0, unsigned deferred_flag = ~0u) {
    const int tid = threadIdx.x, l = tid & 63; const unsigned w = rfl(tid >> 6);
    const u32x4 srdX = make_srd(x, S_ * D_ * 2), srdW = make_srd(W, (unsigned)(D_ * ldw * 2));
    const int g = l >> 4, q = (l & 15) >> 2, p = l & 3;
    G1Regs r;
#pragma unroll
    for (int i = 0; i < 4; ++i) { r.vX[i] = (unsigned)((row0 + 32 * w + 8 * i + (l >> 3)) * (D_ * 2) + (((l & 7) ^ (l >> 3)) << 4)); OPAQUE(r.vX[i]); }
#pragma unroll
    for (int i = 0; i < 3; ++i) { const int pc = (int)w + 4 * i, idx = 64 * pc + l, rho = idx / 18, cc = idx - rho * 18, k = rho ^ (((rho >> 3) & 1) << 2);
        r.vW[i] = pc < 9 ? (unsigned)(k * ldw * 2 + (col0 + 8 * cc) * 2) : OOB; OPAQUE(r.vW[i]); }
    r.vw0 = lds0 + (unsigned)(G1_WOFF + (8 * g + (q ^ (4 * (g & 1)))) * 288 + p * 8); r.vw1 = lds0 + (unsigned)(G1_WOFF + (8 * g + ((q + 4) ^ (4 * (g & 1)))) * 288 + p * 8);
    r.vx[0] = lds0 + (unsigned)((32 * w + (l & 15)) * 128) + ((g ^ (l & 7)) << 4); r.vx[1] = lds0 + (unsigned)((32 * w + (l & 15)) * 128) + (((4 + g) ^ (l & 7)) << 4);
    OPAQUE(r.vw0); OPAQUE(r.vw1); OPAQUE(r.vx[0]); OPAQUE(r.vx[1]);
    const unsigned sWX = lds0 + w * 4096, sWW = lds0 + G1_WOFF + w * 1024, kst = (unsigned)(32 * ldw * 2);
    auto xs = [&](int slot, unsigned so) {
#pragma unroll
        for (int i = 0; i < 4; ++i) { set_m0(sWX, slot * G1_XS + i * 1024); asm volatile("s_nop 0"); dma_go(srdX, r.vX[i], so); } };
    auto wsl = [&](int slot, unsigned so) {
#pragma unroll
        for (int i = 0; i < 2; ++i) { set_m0(sWW, slot * G1_WS + i * 4096); asm volatile("s_nop 0"); dma_go(srdW, r.vW[i], so); }
        if (w == 0) { set_m0(sWW, slot * G1_WS + 8192); asm volatile("s_nop 0"); dma_go(srdW, r.vW[2], so); } };
    auto rot9 = [&](int s) { const int t = s + s0; return (unsigned)(t >= 9 ? t - 9 : t); };
#define GW(v) ((v) < 18 ? (rot9((v) >> 1) * 2 + ((v) & 1)) * kst : OOB)
#define GX(s) ((s) < 9 ? rot9(s) * 128u : OOB)
    xs(0, GX(0)); wsl(0, GW(0)); wsl(1, GW(1)); wsl(2, GW(2)); xs(1, GX(1));
#pragma unroll
    for (int n = 0; n < 9; ++n) { acc[n][0] = f32x4{0, 0, 0, 0}; acc[n][1] = f32x4{0, 0, 0, 0}; }
    PIN18(acc, "s_nop 1");
    asm volatile("s_waitcnt vmcnt(8)\n\ts_barrier" ::: "memory");
    if (deferred_flag != ~0u) raise_flag(c, deferred_flag); else STAMPV(c.P, 10);
#pragma unroll
    for (int tb = 0; tb < 2; ++tb) r.xf[0][tb] = lds_rd128(r.vx[0] + tb * 2048);
#pragma unroll
    for (int n = 0; n < 9; ++n) r.wf[n] = g1_rdw(r.vw0, r.vw1, n * 32);
    LGKM(0);
    for (int u = 0; u < 16; u += 4) {
        g1_sub<TRANS, 0>(acc, r, srdX, srdW, sWX, sWW, w, 0, GW(u + 3));
        g1_sub<TRANS, 1>(acc, r, srdX, srdW, sWX, sWW, w, GX(u / 2 + 2), GW(u + 4));
        g1_sub<TRANS, 2>(acc, r, srdX, srdW, sWX, sWW, w, 0, GW(u + 5));
        g1_sub<TRANS, 3>(acc, r, srdX, srdW, sWX, sWW, w, GX(u / 2 + 3), GW(u + 6));
    }
    g1_sub<TRANS, 0>(acc, r, srdX, srdW, sWX, sWW, w, 0, OOB);
    g1_sub<TRANS, 1>(acc, r, srdX, srdW, sWX, sWW, w, OOB, OOB);
    asm volatile("s_waitcnt vmcnt(0) lgkmcnt(0)\n\ts_barrier" ::: "memory");
    PIN18(acc, "s_nop 7\n\ts_nop 1");
}

DI void tile_z(const Ctx& c, int panel, int jt, int s0, u32x4 (&z1p)[9]) {
    const int tid = threadIdx.x, l = tid & 63; const unsigned w = rfl(tid >> 6); const int g = l >> 4;
    f32x4 y[9][2];
    gemm1_main<false>(c, c.P.x, c.P.win, 4608, panel * 128, FH + jt * 144, c.lds0, y, s0);
    STAMPV(c.P, 1);
    {
        float s1[2][4], s2[2][4];
#pragma unroll
        for (int tb = 0; tb < 2; ++tb)
#pragma unroll
            for (int r = 0; r < 4; ++r) { s1[tb][r] = 0.f; s2[tb][r] = 0.f; }
#pragma unroll
        for (int n = 0; n < 9; ++n)
#pragma unroll
            for (int tb = 0; tb < 2; ++tb)
#pragma unroll
                for (int r = 0; r < 4; ++r) { const float v = gelu_t(y[n][tb][r]); y[n][tb][r] = v; s1[tb][r] += v; s2[tb][r] = __builtin_fmaf(v, v, s2[tb][r]); }
#pragma unroll
        for (int tb = 0; tb < 2; ++tb)
#pragma unroll
            for (int r = 0; r < 4; ++r) {
                const float a = row16_sum(s1[tb][r]), b = row16_sum(s2[tb][r]);
                if ((l & 15) == 0) st64(c, WS_ST + (unsigned)((panel * 16 + jt) * 1024 + (32 * w + 16 * tb + 4 * g + r) * 8), u32x2{__float_as_uint(a), __float_as_uint(b)});
            }
    }
    STAMPV(c.P, 2);
    f32x4 acc[9][2];
    gemm1_main<true>(c, c.P.x, c.P.win, 4608, panel * 128, jt * 144, c.lds0, acc, s0, FL_ST + panel * 16 + jt);
    STAMPV(c.P, 3);
    if (tid < 64) poll_flags(c, tid < 16 ? FL_ST + panel * 16 + tid : ~0u);
    BAR();
    u32x2 pv[8];
#pragma unroll
    for (int i = 0; i < 8; ++i) pv[i] = __builtin_amdgcn_raw_buffer_load_b64(c.ws, (int)(WS_ST + (panel * 16 + 8 * (l >> 5) + i) * 1024 + (32 * w + (l & 31)) * 8), 0, 16);
    SB();
#pragma unroll
    for (int n = 0; n < 9; ++n)
#pragma unroll
        for (int tb = 0; tb < 2; ++tb) { z1p[n][2 * tb] = pk2(gelu_t(acc[n][tb][0]), gelu_t(acc[n][tb][1])); z1p[n][2 * tb + 1] = pk2(gelu_t(acc[n][tb][2]), gelu_t(acc[n][tb][3])); }
    asm volatile("" : "+v"(z1p[0]), "+v"(z1p[1]), "+v"(z1p[2]), "+v"(z1p[3]), "+v"(z1p[4]), "+v"(z1p[5]), "+v"(z1p[6]), "+v"(z1p[7]), "+v"(z1p[8]));
    SB();
    {
        float a = 0.f, b = 0.f;
#pragma unroll
        for (int i = 0; i < 8; ++i) { a += __uint_as_float(pv[i][0]); b += __uint_as_float(pv[i][1]); }
        auto ra = __builtin_amdgcn_permlane32_swap(__float_as_uint(a), __float_as_uint(a), false, false);
        auto rb = __builtin_amdgcn_permlane32_swap(__float_as_uint(b), __float_as_uint(b), false, false);
        a = __uint_as_float(ra[0]) + __uint_as_float(ra[1]); b = __uint_as_float(rb[0]) + __uint_as_float(rb[1]);
        const float m = a * (1.0f / FH), var = __builtin_fmaxf(b * (1.0f / FH) - m * m, 0.f);
        const float rs = 1.0f / __builtin_sqrtf(var + 1e-5f);
        if (l < 32) *(f32x2*)(c.smem + (w * 32 + l) * 8) = f32x2{m, rs};
        asm volatile("s_waitcnt lgkmcnt(0)" ::: "memory");
    }
    f32x2 ms[2][4];
#pragma unroll
    for (int tb = 0; tb < 2; ++tb)
#pragma unroll
        for (int r = 0; r < 4; ++r) ms[tb][r] = *(const f32x2*)(c.smem + (w * 32 + 16 * tb + 4 * g + r) * 8);
    const unsigned dst = WS_Z2 + (unsigned)(((jt * 9) * 64 + panel * 4 + w) * 1024 + l * 16);
#pragma unroll
    for (int n = 0; n < 9; ++n) {
        unsigned pk[2][2];
#pragma unroll
        for (int tb = 0; tb < 2; ++tb) {
            float v[4];
#pragma unroll
            for (int r = 0; r < 4; ++r) v[r] = (y[n][tb][r] - ms[tb][r][0]) * ms[tb][r][1];
            pk[tb][0] = pk2(v[0], v[1]); pk[tb][1] = pk2(v[2], v[3]);
        }
        u32x4 o;
#pragma unroll
        for (int i = 0; i < 2; ++i) {
            auto a = __builtin_amdgcn_permlane32_swap(pk[0][i], pk[1][i], false, false);
            auto b = __builtin_amdgcn_permlane16_swap(a[0], a[1], false, false);
            o[i] = b[0]; o[2 + i] = b[1];
        }
        st128(c, dst + n * 65536, o);
    }
    publish(c, FL_Z + jt * 16 + panel);
    STAMPV(c.P, 11);
    wait_begin(c, tid < 16 ? FL_Z + jt * 16 + tid : tid < 20 ? FL_WP + 4 * panel + (tid - 16) : ~0u);
    wait_end();
}

DI void tile_qk(const Ctx& c, int panel, int which, int hq, int s0) {
    f32x4 acc[9][2];
    gemm1_main<true>(c, c.P.x, c.P.wqkv, 1728, panel * 128, which * 576 + hq * 144, c.lds0, acc, s0);
    const int l = threadIdx.x & 63; const unsigned w = rfl(threadIdx.x >> 6); const int g = l >> 4;
    const float sc = which == 0 ? 0.23570226039551584f * 1.4426950408889634f : 1.0f;
    char* st = c.smem + w * 9216;
#pragma unroll
    for (int n = 0; n < 9; ++n)
#pragma unroll
        for (int pp = 0; pp < 2; ++pp) {
            const int cc = 16 * n + 4 * g + 2 * pp, hl = cc / 18, d = cc - 18 * hl;
#pragma unroll
            for (int tb = 0; tb < 2; ++tb) {
                const int tok = 16 * tb + (l & 15);
                const int off = d < 16 ? hl * 1024 + (tok + 32 * (d >> 3)) * 16 + (d & 7) * 2 : 8192 + hl * 128 + tok * 4;
                *(unsigned*)(st + off) = pk2(acc[n][tb][2 * pp] * sc, acc[n][tb][2 * pp + 1] * sc);
            }
        }
    asm volatile("s_waitcnt lgkmcnt(0)" ::: "memory");
    const unsigned dm = (which == 0 ? WS_QM : WS_KM) + (unsigned)(((hq * 8) * 64 + panel * 4 + w) * 1024 + l * 16);
#pragma unroll
    for (int h = 0; h < 8; ++h) st128(c, dm + h * 65536, *(const u32x4*)(st + h * 1024 + l * 16));
    st128(c, (which == 0 ? WS_QR : WS_KR) + (unsigned)(((hq * 8 + (l >> 3)) * 2048 + panel * 128 + 32 * w) * 4 + (l & 7) * 16), *(const u32x4*)(st + 8192 + l * 16));
}

DI void tile_v(const Ctx& c, int panel, int hq, int s0) {
    f32x4 acc[9][2];
    gemm1_main<false>(c, c.P.x, c.P.wqkv, 1728, panel * 128, 2 * 576 + hq * 144, c.lds0, acc, s0);
    const int l = threadIdx.x & 63; const unsigned w = rfl(threadIdx.x >> 6); const int g = l >> 4;
    char* st = c.smem + w * 10240;
    if (l < 32) {
        const int blk = l >> 1, hh = l & 1;
        *(u32x4*)(st + blk * 640 + (hh * 20 + 18) * 16) = u32x4{0x3f803f80u, 0x3f803f80u, 0x3f803f80u, 0x3f803f80u};
        *(u32x4*)(st + blk * 640 + (hh * 20 + 19) * 16) = u32x4{0, 0, 0, 0};
    }
#pragma unroll
    for (int n = 0; n < 9; ++n) {
        const int cc = 16 * n + (l & 15), hl = cc / 18, d = cc - 18 * hl;
#pragma unroll
        for (int tb = 0; tb < 2; ++tb)
            *(u32x2*)(st + (hl * 2 + tb) * 640 + ((g & 1) * 20 + d) * 16 + (g >> 1) * 8) = u32x2{pk2(acc[n][tb][0], acc[n][tb][1]), pk2(acc[n][tb][2], acc[n][tb][3])};
    }
    asm volatile("s_waitcnt lgkmcnt(0)" ::: "memory");
    const unsigned dv = WS_VT + (unsigned)(((hq * 8) * 128 + panel * 8 + 2 * w) * 640);
#pragma unroll
    for (int i = 0; i < 10; ++i) { const int idx = 64 * i + l, h = idx / 80, o = idx - 80 * h; st128(c, dv + (unsigned)(h * 128 * 640 + o * 16), *(const u32x4*)(st + idx * 16)); }
}

DI void prep_wsp(const Ctx& c, int n) {
    const int tid = threadIdx.x, l = tid & 63; const unsigned w = rfl(tid >> 6); const int g = l >> 4;
    const u32x4 srd = make_srd(c.P.wsp, S_ * S_ * 2);
    const int tb = 2 * n + (int)(w & 1), s0 = (int)(w >> 1) * 16;
    unsigned vsrc = (unsigned)((tb * 16 + (l >> 3)) * (S_ * 2) + (((l & 7) ^ (l >> 3)) << 4));
    const unsigned lw = c.lds0 + w * 16384;
    const unsigned rd0 = lw + (unsigned)((l & 15) * 128) + ((g ^ (l & 7)) << 4), rd1 = lw + (unsigned)((l & 15) * 128) + (((4 + g) ^ (l & 7)) << 4);
    auto batch = [&](int bt) {
#pragma unroll
        for (int i = 0; i < 8; ++i) {
            dma_now(srd, vsrc, (unsigned)((s0 + bt * 8 + i) * 128), lw + i * 2048);
            dma_now(srd, vsrc, (unsigned)((s0 + bt * 8 + i) * 128 + 8 * S_ * 2), lw + i * 2048 + 1024);
        }
    };
    u32x4 f[16];
    batch(0);
    asm volatile("s_waitcnt vmcnt(0)" ::: "memory");
#pragma unroll
    for (int i = 0; i < 8; ++i) { f[2 * i] = __builtin_bit_cast(u32x4, lds_rd128(rd0 + i * 2048)); f[2 * i + 1] = __builtin_bit_cast(u32x4, lds_rd128(rd1 + i * 2048)); }
#pragma unroll
    for (int i = 0; i < 16; ++i) OPAQUE(f[i]);
    batch(1);
#pragma unroll
    for (int i = 0; i < 16; ++i) st128(c, WS_WP + (unsigned)((tb * 64 + 2 * s0 + i) * 1024 + l * 16), f[i]);
    asm volatile("s_waitcnt vmcnt(16)" ::: "memory");
#pragma unroll
    for (int i = 0; i < 8; ++i) {
        st128(c, WS_WP + (unsigned)((tb * 64 + 2 * s0 + 16 + 2 * i) * 1024 + l * 16), __builtin_bit_cast(u32x4, lds_rd128(rd0 + i * 2048)));
        st128(c, WS_WP + (unsigned)((tb * 64 + 2 * s0 + 17 + 2 * i) * 1024 + l * 16), __builtin_bit_cast(u32x4, lds_rd128(rd1 + i * 2048)));
    }
    publish(c, FL_WP + n);
}

DI void prep_wcat(const Ctx& c, int kb, unsigned flag) {
    const int tid = threadIdx.x, l = tid & 63; const unsigned w = rfl(tid >> 6); const int g = l >> 4;
    const u32x4 srd = kb < 18 ? make_srd(c.P.wo, D_ * D_ * 2) : make_srd(c.P.wout, FH * D_ * 2);
#pragma unroll
    for (int i = 0; i < 9; ++i) {
        const int idx = 64 * ((int)w + 4 * i) + l, sg = idx / 72, ch = idx - 72 * sg, gg = sg >> 3, j = sg & 7;
        int row;
        if (kb < 16) { const int head = 2 * kb + (gg >> 1), h = gg & 1; row = 18 * head + (j < 4 ? 4 * h + j : 8 + 4 * h + (j - 4)); }
        else if (kb < 18) row = 18 * (16 * (kb - 16) + 4 * gg + (j >> 1)) + 16 + (j & 1);
        else row = 32 * (kb - 18) + (j < 4 ? 4 * gg + j : 16 + 4 * gg + (j - 4));
        dma_now(srd, (unsigned)(row * 1152 + ch * 16), 0u, c.lds0 + (w + 4 * i) * 1024);
    }
    WAIT_BAR0();
    const int q = (l & 15) >> 2, p = l & 3;
    const unsigned r0 = c.lds0 + (unsigned)((8 * g + q) * 1152 + p * 8);
#pragma unroll
    for (int i = 0; i < 9; ++i) {
        const int db = (int)w + 4 * i;
        st128(c, WS_WC + (unsigned)((db * 90 + kb) * 1024 + l * 16), __builtin_bit_cast(u32x4, cat8(lds_rdtr(r0 + db * 32), lds_rdtr(r0 + 4 * 1152 + db * 32))));
    }
    publish(c, FL_WC + kb);
}

DI void prep_gate(const Ctx& c, int n, unsigned flag) {
    const int tid = threadIdx.x, l = tid & 63; const int w = tid >> 6;
    const int row = n * 32 + w * 8 + (l >> 3);
    u32x4 xv[9], gv[9];
#pragma unroll
    for (int i = 0; i < 9; ++i) { xv[i] = *(const u32x4*)(c.P.x + row * 576 + ((l & 7) + 8 * i) * 8); gv[i] = *(const u32x4*)(c.P.gatew + ((l & 7) + 8 * i) * 8); }
    float a = 0.f;
#pragma unroll
    for (int i = 0; i < 9; ++i)
#pragma unroll
        for (int e = 0; e < 4; ++e) { a = __builtin_fmaf(bflo(xv[i][e]), bflo(gv[i][e]), a); a = __builtin_fmaf(bfhi(xv[i][e]), bfhi(gv[i][e]), a); }
    a += __shfl_xor(a, 1); a += __shfl_xor(a, 2); a += __shfl_xor(a, 4);
    if ((l & 7) == 0) st32(c, WS_GT + row * 4, __float_as_uint(1.0f / (1.0f + __expf(-a))));
    publish(c, FL_WC + 90 + n);
}

DI void warm_l2(const Ctx& c, int x, int rank) {
    const int tid = threadIdx.x; const unsigned w = rfl(tid >> 6);
    const u32x4 sx = make_srd(c.P.x, S_ * D_ * 2), sw = make_srd(c.P.win, D_ * 4608 * 2), sq = make_srd(c.P.wqkv, D_ * 1728 * 2);
    const unsigned dst = c.lds0 + 73728 + w * 256;
    auto touch = [&](u32x4 srd, unsigned off) { asm volatile("s_mov_b32 m0, %0\n\ts_nop 0\n\tbuffer_load_dword %1, %2, 0 offen lds" ::"s"(dst), "v"(off), "s"(srd) : "memory"); };
    const int i = rank * 256 + tid;
    if (w < 2) {
        const int j = rank * 128 + tid;
        { const int row = j / 18, k = j - row * 18; touch(sw, (unsigned)(row * 9216 + (k < 9 ? 0 : 4608) + (x & 3) * 1152 + (k < 9 ? k : k - 9) * 128)); }
        { const int j2 = j + 8192, row = j2 / 18, k = j2 - row * 18; touch(sw, j2 < 10368 ? (unsigned)(row * 9216 + (k < 9 ? 0 : 4608) + (x & 3) * 1152 + (k < 9 ? k : k - 9) * 128) : OOB); }
    } else {
        const int j = rank * 128 + (tid - 128);
        touch(sx, (unsigned)((x >> 2) * 1024 * 1152 + j * 128));
        { const int row = j / 9, k = j - row * 9, ty = k / 3, ln = k - 3 * ty;
          touch(sq, j < 5184 ? (unsigned)(row * 3456 + ty * 1152 + (((x & 3) * 288) & ~127) + ln * 128) : OOB);
        }
        touch(sx, j < 1024 ? (unsigned)((x >> 2) * 1024 * 1152 + (8192 + j) * 128) : OOB);
    }
    (void)i;
}

constexpr int AB_SLOT = 20 * 1024;
struct AttnState {
    f32x16 S[2], O[2]; u32x4 pl[2], ph[2];
    bf16x8 k0, v0, v1; u32x4 k1; u32x4 q0[2], q1[2];
};
#define AT_PRIO 0
#define AT_HI() do { __builtin_amdgcn_s_setprio(AT_PRIO); SB(); } while (0)
#define AT_LO() __builtin_amdgcn_s_setprio(0)
template <int T, class F>
DI void attn_slot(AttnState& s, unsigned aK, unsigned aR, unsigned aV, const f32x16& zero, F&& hook) {
    constexpr int U = T ^ 1;
    AT_HI(); s.S[U] = MFMA32(s.k0, BC8(s.q0[U]), zero); SB(); AT_LO();
    SB();
#pragma unroll
    for (int i = 0; i < 4; ++i) s.S[T][i] = EX2(s.S[T][i]);
    SB();
    AT_HI(); s.S[U] = MFMA32(BC8(s.k1), BC8(s.q1[U]), s.S[U]); SB(); AT_LO();
    SB();
    if (T == 0) { s.k0 = lds_rd128(aK); s.k1[0] = lds_rd32(aR); }
#pragma unroll
    for (int i = 4; i < 8; ++i) s.S[T][i] = EX2(s.S[T][i]);
    s.pl[T][0] = pk2(s.S[T][0], s.S[T][1]); s.pl[T][1] = pk2(s.S[T][2], s.S[T][3]);
    SB();
    AT_HI(); s.O[U] = MFMA32(s.v1, BC8(s.ph[U]), s.O[U]); SB(); AT_LO();
    SB();
    if (T == 0) s.v1 = lds_rd128(aV);
    hook();
    s.pl[T][2] = pk2(s.S[T][4], s.S[T][5]); s.pl[T][3] = pk2(s.S[T][6], s.S[T][7]);
    SB();
#pragma unroll
    for (int i = 8; i < 12; ++i) s.S[T][i] = EX2(s.S[T][i]);
    SB();
    AT_HI(); s.O[T] = MFMA32(s.v0, BC8(s.pl[T]), s.O[T]); SB(); AT_LO();
    SB();
    if (T == 1) s.v0 = lds_rd128(aV);
#pragma unroll
    for (int i = 12; i < 16; ++i) s.S[T][i] = EX2(s.S[T][i]);
    SB();
    s.ph[T][0] = pk2(s.S[T][8], s.S[T][9]); s.ph[T][1] = pk2(s.S[T][10], s.S[T][11]);
    s.ph[T][2] = pk2(s.S[T][12], s.S[T][13]); s.ph[T][3] = pk2(s.S[T][14], s.S[T][15]);
    SB();
}

DI void attn_unit(const Ctx& c, int head, int qb) {
    const int tid = threadIdx.x, l = tid & 63; const unsigned w = rfl(tid >> 6); const int h = l >> 5, r32 = l & 31; const unsigned lds0 = c.lds0;
    {
        const int hq = head >> 3;
        wait_flags(c, tid < 16 ? FL_QKV + (4 + hq) * 16 + tid : tid < 32 ? FL_QKV + (8 + hq) * 16 + (tid - 16) : tid < 34 ? FL_QKV + hq * 16 + 2 * qb + (tid - 32) : ~0u);
    }
    STAMPV(c.P, 2);
    const u32x4 srd = make_srd(c.P.ws + WS_KM, WS_QM - WS_KM);
    unsigned vl = (unsigned)l * 16; OPAQUE(vl);
    unsigned sLds[5], sSrc[5], sStep[5];
#pragma unroll
    for (int i = 0; i < 5; ++i) {
        const unsigned p = w + 4 * i;
        sLds[i] = lds0 + p * 1024;
        sSrc[i] = p < 8 ? (unsigned)(head * 65536) + p * 1024 : p < 18 ? (WS_VT - WS_KM) + (unsigned)(head * 81920) + (p - 8) * 1024
                : p == 18 ? (WS_KR - WS_KM) + (unsigned)(head * 8192) : OOB;
        sStep[i] = p < 8 ? 8192u : (p < 18 ? 10240u : (p == 18 ? 1024u : 0u));
    }
    auto piece = [&](int i, int ch, unsigned boff) { set_m0s(sLds[i], rfl(boff)); const unsigned so = ch < 8 ? sSrc[i] + (unsigned)ch * sStep[i] : OOB; asm volatile("s_nop 0"); dma_go_c(srd, vl, so); };
    auto issue = [&](int ch, unsigned boff) {
#pragma unroll
        for (int i = 0; i < 5; ++i) piece(i, ch, boff); };
    AttnState s;
    const int tok0 = qb * 256 + 64 * (int)w;
    issue(0, 0);
#pragma unroll
    for (int t = 0; t < 2; ++t) {
        s.q0[t] = __builtin_amdgcn_raw_buffer_load_b128(c.ws, (int)(WS_QM + ((unsigned)head * 64 + (tok0 >> 5) + t) * 1024 + l * 16), 0, 16);
        const unsigned qr = __builtin_amdgcn_raw_buffer_load_b32(c.ws, (int)(WS_QR + ((unsigned)head * 2048 + tok0 + 32 * t + r32) * 4), 0, 16);
        s.q1[t] = u32x4{h ? 0u : qr, 0u, 0u, 0u};
    }
    asm volatile("" : "+v"(s.q0[0]), "+v"(s.q0[1]), "+v"(s.q1[0]), "+v"(s.q1[1]));
    unsigned ka = lds0 + (unsigned)l * 16, ra = lds0 + 18432u + (unsigned)r32 * 4, va = lds0 + 8192u + (unsigned)(h * 20 + (r32 < 19 ? r32 : 19)) * 16;
    OPAQUE(ka); OPAQUE(ra); OPAQUE(va);
    const f32x16 zero = {};
    volatile LAS unsigned* flag = (volatile LAS unsigned*)(uintptr_t)(lds0 + 2 * AB_SLOT);

    for (int attempt = 0; attempt < 2; ++attempt) {
        float mx[2] = {-3.0e38f, -3.0e38f};
        {
            const u32x4 qz[2] = {u32x4{s.q1[0][0], 0u, 0u, 0u}, u32x4{s.q1[1][0], 0u, 0u, 0u}};
            const int nch = attempt ? 8 : 1, nblk = attempt ? 8 : 1;
            for (int ch = 0; ch < nch; ++ch) {
                if (attempt) { WAIT_BAR0(); issue(ch, 0); }
                WAIT_BAR0();
                for (int kb = 0; kb < nblk; ++kb) {
                    const bf16x8 k0 = lds_rd128(ka + kb * 1024);
                    const u32x4 k1 = u32x4{lds_rd32(ra + kb * 128), 0u, 0u, 0u};
#pragma unroll
                    for (int t = 0; t < 2; ++t) {
                        f32x16 sc = MFMA32(k0, BC8(s.q0[t]), zero); sc = MFMA32(BC8(k1), BC8(qz[t]), sc);
#pragma unroll
                        for (int r = 0; r < 16; ++r) mx[t] = __builtin_fmaxf(mx[t], sc[r]);
                    }
                }
            }
            if (attempt) { WAIT_BAR0(); issue(0, 0); WAIT_BAR0(); }
        }
#pragma unroll
        for (int t = 0; t < 2; ++t) {
            auto rr = __builtin_amdgcn_permlane32_swap(__float_as_uint(mx[t]), __float_as_uint(mx[t]), false, false);
            const float m = __builtin_fmaxf(__uint_as_float(rr[0]), __uint_as_float(rr[1]));
            const unsigned nb = __float_as_uint(-m), hi = nb & 0xffff0000u;
            const float r1 = -m - __uint_as_float(hi); const unsigned mid = __float_as_uint(r1) & 0xffff0000u;
            const float r2 = r1 - __uint_as_float(mid); const unsigned lo = __float_as_uint(r2) & 0xffff0000u;
            s.q1[t][1] = h ? 0u : ((hi >> 16) | mid); s.q1[t][2] = h ? 0u : (lo >> 16);
        }
        STAMPV(c.P, 3);
        s.O[0] = zero; s.O[1] = zero;
        s.k0 = lds_rd128(ka); s.k1 = u32x4{lds_rd32(ra), 0x3f803f80u, 0x00003f80u, 0u};
        s.v0 = lds_rd128(va); s.v1 = s.v0; s.ph[1] = u32x4{0u, 0u, 0u, 0u};
        s.S[0] = MFMA32(s.k0, BC8(s.q0[0]), zero); s.S[0] = MFMA32(BC8(s.k1), BC8(s.q1[0]), s.S[0]);
        unsigned cur = 0, nxt = AB_SLOT;
        auto nohook = [] {};
        for (int ch = 0; ch < 8; ++ch) {
            const unsigned cK = ka + cur, cR = ra + cur, cV = va + cur, nK = ka + nxt, nR = ra + nxt, nV = va + nxt;
#pragma unroll
            for (int j = 0; j < 7; ++j) {
                attn_slot<0>(s, cK + (j + 1) * 1024, cR + (j + 1) * 128, cV + j * 1280 + 640, zero, nohook);
                attn_slot<1>(s, 0, 0, cV + (j + 1) * 1280, zero, [&] { if (j < 5) piece(j, ch + 1, nxt); });
            }
            asm volatile("s_waitcnt vmcnt(0)\n\ts_barrier" ::: "memory");
            SB();
            attn_slot<0>(s, nK, nR, cV + 7 * 1280 + 640, zero, nohook);
            attn_slot<1>(s, 0, 0, nV, zero, [&] { asm volatile("s_barrier" ::: "memory"); });
            const unsigned t_ = cur; cur = nxt; nxt = t_;
        }
        s.O[1] = MFMA32(s.v1, BC8(s.ph[1]), s.O[1]);
        if (attempt == 1) break;
        bool bad = false;
#pragma unroll
        for (int t = 0; t < 2; ++t) {
            float am = 0.f; bool b = false;
#pragma unroll
            for (int r = 0; r < 11; ++r) { am = __builtin_fmaxf(am, __builtin_fabsf(s.O[t][r])); b = b || (s.O[t][r] != s.O[t][r]); }
            bad = bad || b || !(am < 1.0e30f);
        }
        if (tid == 0) *flag = 0u;
        WAIT_BAR0();
        if (__any(bad)) *flag = 1u;
        WAIT_BAR0();
        if (rfl(*flag) == 0u) break;
    }
    STAMPV(c.P, 4);
#pragma unroll
    for (int t = 0; t < 2; ++t) {
        auto rr = __builtin_amdgcn_permlane32_swap(__float_as_uint(s.O[t][10]), __float_as_uint(s.O[t][10]), false, false);
        const float inv = 1.0f / __uint_as_float(rr[0]);
        const int tok = tok0 + 32 * t + r32, tb = tok >> 4;
        const unsigned base = WS_HC + (unsigned)(tb * 90 * 1024);
        const u32x4 m = {pk2(s.O[t][0] * inv, s.O[t][1] * inv), pk2(s.O[t][2] * inv, s.O[t][3] * inv), pk2(s.O[t][4] * inv, s.O[t][5] * inv), pk2(s.O[t][6] * inv, s.O[t][7] * inv)};
        st128(c, base + (unsigned)((head >> 1) * 1024 + ((tok & 15) + 16 * (2 * (head & 1) + h)) * 16), m);
        if (h == 0) st32(c, base + (unsigned)((16 + (head >> 4)) * 1024 + ((tok & 15) + 16 * ((head & 15) >> 2)) * 16 + (head & 3) * 4), pk2(s.O[t][8] * inv, s.O[t][9] * inv));
    }
    publish(c, FL_O + qb * 32 + head);
}

constexpr int SD_N = 8, SD_SLOT = 9216;
struct SpdRegs { bf16x8 zf[9]; u32x4 t[4][2]; unsigned vZ[3], vz, vT; };
template <int U>
DI void spd_sub(f32x4 (&acc)[9][2], SpdRegs& r, u32x4 srdT, u32x4 srdZ, unsigned sWZ, unsigned w, unsigned sTa, unsigned sTb, unsigned sZ) {
    constexpr int NX = (U + 1) & (SD_N - 1), PF = (U + SD_N - 1) & (SD_N - 1), TU = U & 3, TP = (U + 3) & 3;
    asm volatile("s_waitcnt vmcnt(10)\n\ts_barrier" : "+v"(r.t[TU][0]), "+v"(r.t[TU][1])::"memory");
    SB();
#pragma unroll
    for (int n = 0; n < 9; ++n) {
        if (n == 0) LGKM(4);
        if (n == 5) LGKM(5);
        if (n == 4 || n == 6) set_m0(sWZ, PF * SD_SLOT + (n == 4 ? 0 : 4096));
        mfma16v(acc[n][0], r.zf[n], BC8(r.t[TU][0]));
        if (n == 0) ld_frag<0>(r.t[TP][0], srdT, r.vT, sTa);
        if (n == 2) ld_frag<0>(r.t[TP][1], srdT, r.vT, sTb);
        if (n == 4) dma_go_c(srdZ, r.vZ[0], sZ);
        if (n == 6) dma_go_c(srdZ, r.vZ[1], sZ);
        mfma16v(acc[n][1], r.zf[n], BC8(r.t[TU][1]));
        r.zf[n] = lds_rd128(r.vz + NX * SD_SLOT + n * 1024);
        SB();
    }
    if (w == 0) { set_m0(sWZ, PF * SD_SLOT + 8192); asm volatile("s_nop 0"); dma_go_c(srdZ, r.vZ[2], sZ); }
    SB();
}

DI void spatial_tile(const Ctx& c, int panel, int jt, int u0, const u32x4 (&z1p)[9]) {
    const int tid = threadIdx.x, l = tid & 63; const unsigned w = rfl(tid >> 6); const unsigned lds0 = c.lds0;
    STAMPV(c.P, 4);
    const u32x4 srdT = make_srd(c.P.ws + WS_WP + (size_t)(panel * 8 + 2 * w) * 65536, 2 * 65536), srdZ = make_srd(c.P.ws + WS_Z2 + (size_t)jt * 9 * 65536, 9 * 65536);
    SpdRegs r;
#pragma unroll
    for (int i = 0; i < 3; ++i) { r.vZ[i] = (w + 4 * i) * 65536 + l * 16; OPAQUE(r.vZ[i]); }
    r.vz = lds0 + l * 16; OPAQUE(r.vz);
    r.vT = l * 16; OPAQUE(r.vT);
    const unsigned sWZ = lds0 + w * 1024;
#define SO(v) ((v) < 64 ? (unsigned)(((v) + u0) & 63) * 1024u : OOB)
#define SO2(v) ((v) < 64 ? (unsigned)(((v) + u0) & 63) * 1024u + 65536u : OOB)
    f32x4 acc[9][2];
#pragma unroll
    for (int n = 0; n < 9; ++n) { acc[n][0] = f32x4{0, 0, 0, 0}; acc[n][1] = f32x4{0, 0, 0, 0}; }
    PIN18(acc, "s_nop 1");
#pragma unroll
    for (int u = 0; u < SD_N - 1; ++u) {
        if (u >= 4) { ld_frag<0>(r.t[u - 4][0], srdT, r.vT, SO(u - 4)); ld_frag<0>(r.t[u - 4][1], srdT, r.vT, SO2(u - 4)); }
#pragma unroll
        for (int i = 0; i < 2; ++i) { set_m0(sWZ, u * SD_SLOT + i * 4096); asm volatile("s_nop 0"); dma_go_c(srdZ, r.vZ[i], SO(u)); }
        if (w == 0) { set_m0(sWZ, u * SD_SLOT + 8192); asm volatile("s_nop 0"); dma_go_c(srdZ, r.vZ[2], SO(u)); }
    }
    asm volatile("s_waitcnt vmcnt(18)\n\ts_barrier" ::: "memory");
#pragma unroll
    for (int n = 0; n < 9; ++n) r.zf[n] = lds_rd128(r.vz + n * 1024);
    LGKM(0);
    for (int u = 0; u < 64; u += 8) {
        spd_sub<0>(acc, r, srdT, srdZ, sWZ, w, SO(u + 3), SO2(u + 3), SO(u + 7));
        spd_sub<1>(acc, r, srdT, srdZ, sWZ, w, SO(u + 4), SO2(u + 4), SO(u + 8));
        spd_sub<2>(acc, r, srdT, srdZ, sWZ, w, SO(u + 5), SO2(u + 5), SO(u + 9));
        spd_sub<3>(acc, r, srdT, srdZ, sWZ, w, SO(u + 6), SO2(u + 6), SO(u + 10));
        spd_sub<4>(acc, r, srdT, srdZ, sWZ, w, SO(u + 7), SO2(u + 7), SO(u + 11));
        spd_sub<5>(acc, r, srdT, srdZ, sWZ, w, SO(u + 8), SO2(u + 8), SO(u + 12));
        spd_sub<6>(acc, r, srdT, srdZ, sWZ, w, SO(u + 9), SO2(u + 9), SO(u + 13));
        spd_sub<7>(acc, r, srdT, srdZ, sWZ, w, SO(u + 10), SO2(u + 10), SO(u + 14));
    }
    asm volatile("s_waitcnt vmcnt(0) lgkmcnt(0)" : "+v"(r.t[0][0]), "+v"(r.t[0][1]), "+v"(r.t[1][0]), "+v"(r.t[1][1]), "+v"(r.t[2][0]), "+v"(r.t[2][1]), "+v"(r.t[3][0]), "+v"(r.t[3][1])::"memory");
    PIN18(acc, "s_nop 7\n\ts_nop 1");
    STAMPV(c.P, 5);
    float bs[2];
#pragma unroll
    for (int tb = 0; tb < 2; ++tb) bs[tb] = bf2f(c.P.bsp[panel * 128 + 32 * w + 16 * tb + (l & 15)]);
    unsigned hp[9][2][2];
#pragma unroll
    for (int n = 0; n < 9; ++n)
#pragma unroll
        for (int tb = 0; tb < 2; ++tb) {
            hp[n][tb][0] = pk2(bflo(z1p[n][2 * tb]) * (acc[n][tb][0] + bs[tb]), bfhi(z1p[n][2 * tb]) * (acc[n][tb][1] + bs[tb]));
            hp[n][tb][1] = pk2(bflo(z1p[n][2 * tb + 1]) * (acc[n][tb][2] + bs[tb]), bfhi(z1p[n][2 * tb + 1]) * (acc[n][tb][3] + bs[tb]));
        }
    const int fb0 = jt * 9;
#pragma unroll
    for (int tb = 0; tb < 2; ++tb) {
        const unsigned base = WS_HC + (unsigned)(((panel * 8 + 2 * w + tb) * 90 + 18) * 1024 + l * 16);
        if ((jt & 1) == 0) {
#pragma unroll
            for (int n = 0; n < 8; n += 2) st128(c, base + (unsigned)(((fb0 + n) >> 1) * 1024), u32x4{hp[n][tb][0], hp[n][tb][1], hp[n + 1][tb][0], hp[n + 1][tb][1]});
            st64(c, base + (unsigned)(((fb0 + 8) >> 1) * 1024), u32x2{hp[8][tb][0], hp[8][tb][1]});
        } else {
            st64(c, base + (unsigned)((fb0 >> 1) * 1024 + 8), u32x2{hp[0][tb][0], hp[0][tb][1]});
#pragma unroll
            for (int n = 1; n < 9; n += 2) st128(c, base + (unsigned)(((fb0 + n) >> 1) * 1024), u32x4{hp[n][tb][0], hp[n][tb][1], hp[n + 1][tb][0], hp[n + 1][tb][1]});
        }
    }
}

constexpr int FN_STAGE = 16 * 1024;
struct FnRegs { bf16x8 wf[5]; u32x4 h[4][3]; unsigned vW[4], vrd, vl; };
template <int U, int VM>
DI void fn_stage(f32x4 (&acc)[5], FnRegs& r, u32x4 srdW, u32x4 srdH, unsigned sWW, unsigned sNext) {
    constexpr int PF = (U + 3) & 3;
    asm volatile("s_waitcnt vmcnt(%3)\n\ts_barrier" : "+v"(r.h[U][0]), "+v"(r.h[U][1]), "+v"(r.h[U][2]) : "n"(VM) : "memory");
    SB();
#pragma unroll
    for (int n = 0; n < 5; ++n) r.wf[n] = lds_rd128(r.vrd + U * FN_STAGE + (n * 3) * 1024);
    ld_frag<0>(r.h[PF][0], srdH, r.vl, sNext); ld_frag<1024>(r.h[PF][1], srdH, r.vl, sNext); ld_frag<2048>(r.h[PF][2], srdH, r.vl, sNext);
    SB();
#pragma unroll
    for (int kk = 0; kk < 3; ++kk) {
#pragma unroll
        for (int n = 0; n < 5; ++n) {
            const int i = kk * 5 + n;
            const bool dm = (i % 4 == 1);
            if (dm) set_m0(sWW, PF * FN_STAGE + (i / 4) * 4096);
            mfma16v(acc[n], r.wf[n], BC8(r.h[U][kk]));
            if (dm) dma_go_c(srdW, r.vW[i / 4], sNext);
            if (kk + 1 < 3) r.wf[n] = lds_rd128(r.vrd + U * FN_STAGE + (n * 3 + kk + 1) * 1024);
            SB();
        }
    }
}
DI void fn_pre_w(FnRegs& r, u32x4 srdW, unsigned sWW, int kb0) {
#pragma unroll
    for (int s = 0; s < 3; ++s)
#pragma unroll
        for (int i = 0; i < 4; ++i) { set_m0(sWW, s * FN_STAGE + i * 4096); asm volatile("s_nop 0"); dma_go_c(srdW, r.vW[i], (unsigned)kb0 * 1024u + s * 3072); }
}
template <int NST>
DI void fn_range(f32x4 (&acc)[5], FnRegs& r, u32x4 srdW, u32x4 srdH, unsigned sWW, int kb0) {
    const unsigned o0 = (unsigned)kb0 * 1024u;
#define FO(s) ((s) < NST ? o0 + (unsigned)(s) * 3072u : OOB)
#pragma unroll
    for (int s = 0; s < 3; ++s) { ld_frag<0>(r.h[s][0], srdH, r.vl, o0 + s * 3072); ld_frag<1024>(r.h[s][1], srdH, r.vl, o0 + s * 3072); ld_frag<2048>(r.h[s][2], srdH, r.vl, o0 + s * 3072); }
    asm volatile("s_waitcnt vmcnt(6)" : "+v"(r.h[0][0]), "+v"(r.h[0][1]), "+v"(r.h[0][2])::"memory");
    fn_stage<0, 63>(acc, r, srdW, srdH, sWW, FO(3));
    asm volatile("s_waitcnt vmcnt(10)" : "+v"(r.h[1][0]), "+v"(r.h[1][1]), "+v"(r.h[1][2])::"memory");
    fn_stage<1, 63>(acc, r, srdW, srdH, sWW, FO(4));
    fn_stage<2, 14>(acc, r, srdW, srdH, sWW, FO(5));
    fn_stage<3, 14>(acc, r, srdW, srdH, sWW, FO(6));
    for (int s = 4; s + 4 <= NST; s += 4) {
        fn_stage<0, 14>(acc, r, srdW, srdH, sWW, FO(s + 3)); fn_stage<1, 14>(acc, r, srdW, srdH, sWW, FO(s + 4));
        fn_stage<2, 14>(acc, r, srdW, srdH, sWW, FO(s + 5)); fn_stage<3, 14>(acc, r, srdW, srdH, sWW, FO(s + 6));
    }
    if (NST % 4 == 2) { fn_stage<0, 14>(acc, r, srdW, srdH, sWW, OOB); fn_stage<1, 14>(acc, r, srdW, srdH, sWW, OOB); }
    asm volatile("s_waitcnt vmcnt(0) lgkmcnt(0)\n\ts_barrier" : "+v"(r.h[0][0]), "+v"(r.h[0][1]), "+v"(r.h[0][2]), "+v"(r.h[1][0]), "+v"(r.h[1][1]), "+v"(r.h[1][2]),
                 "+v"(r.h[2][0]), "+v"(r.h[2][1]), "+v"(r.h[2][2]), "+v"(r.h[3][0]), "+v"(r.h[3][1]), "+v"(r.h[3][2])::"memory");
    PIN5(acc, "s_nop 7\n\ts_nop 1");
}

DI void final_tile(const Ctx& c, int tt, int j, unsigned hflag) {
    const int tid = threadIdx.x, l = tid & 63; const unsigned w = rfl(tid >> 6); const int g = l >> 4;
    const int db0 = (9 * j) >> 1;
    const u32x4 srdW = make_srd(c.P.ws + WS_WC + (size_t)db0 * 90 * 1024, 5 * 90 * 1024);
    const u32x4 srdH = make_srd(c.P.ws + WS_HC + (size_t)(tt * 4 + w) * 90 * 1024, 90 * 1024);
    FnRegs r;
    r.vl = (unsigned)l * 16; OPAQUE(r.vl);
#pragma unroll
    for (int i = 0; i < 4; ++i) { const unsigned p = w + 4 * i, db = p / 3, kk = p - db * 3; r.vW[i] = p < 15 ? (db * 90 + kk) * 1024 + l * 16 : OOB; OPAQUE(r.vW[i]); }
    r.vrd = c.lds0 + l * 16; OPAQUE(r.vrd);
    const unsigned sWW = c.lds0 + w * 1024;
    if (tid < 64) { poll_flags(c, FL_WC + tid); poll_flags(c, FL_WC + 64 + tid); poll_flags(c, tid < 26 ? FL_WC + 128 + tid : ~0u); }
    asm volatile("s_waitcnt vmcnt(0) lgkmcnt(0)\n\ts_barrier" ::: "memory");
    raise_flag(c, hflag);
    wait_begin(c, tid < 16 ? FL_H + (tt >> 1) * 16 + tid : ~0u);
    wait_end();
    fn_pre_w(r, srdW, sWW, 18);
    STAMPV(c.P, 6);
    f32x4 accA[5], accB[5];
#pragma unroll
    for (int n = 0; n < 5; ++n) { accA[n] = f32x4{0, 0, 0, 0}; accB[n] = f32x4{0, 0, 0, 0}; }
    PIN5(accA, "s_nop 1"); PIN5(accB, "s_nop 1");
    fn_range<24>(accB, r, srdW, srdH, sWW, 18);
    STAMPV(c.P, 7);
    fn_pre_w(r, srdW, sWW, 0);
    if (tid < 64) poll_flags(c, tid < 32 ? FL_O + (tt >> 2) * 32 + tid : ~0u);
    if (tid < 64) ACQUIRE();
    wait_end();
    STAMPV(c.P, 8);
    fn_range<6>(accA, r, srdW, srdH, sWW, 0);
    const int tok = tt * 64 + 16 * w + (l & 15);
    const float gt = __uint_as_float(__builtin_amdgcn_raw_buffer_load_b32(c.ws, (int)(WS_GT + tok * 4), 0, 16));
#pragma unroll
    for (int n = 0; n < 5; ++n) {
        const int d = (db0 + n) * 16 + 4 * g;
        if (d >= 72 * j && d < 72 * j + 72) {
            float v[4];
#pragma unroll
            for (int rr = 0; rr < 4; ++rr) v[rr] = gt * accA[n][rr] + (1.0f - gt) * accB[n][rr];
            *(u32x2*)(c.P.out + tok * 576 + d) = u32x2{pk2(v[0], v[1]), pk2(v[2], v[3])};
        }
    }
}

constexpr int MEGA_LDS = 73728 + 1024;
#define A_DELAY 0
#define SP_PRIO 2
#define A1_PRIO 1
#define B1_PRIO 1
#define SROT 0
#define SROT2 0
__global__ __launch_bounds__(256, 2) void mega_kernel(Params P) {
    extern __shared__ __attribute__((aligned(16))) char smem[];
    const int b = blockIdx.x, tid = threadIdx.x;
    STAMPV(P, 0);
    const u64 ep = __hip_atomic_load((gu64*)(P.ws + WS_EP), __ATOMIC_RELAXED, __HIP_MEMORY_SCOPE_AGENT);
    const u64 tag = ep + 1ull;
    const Ctx c{P, __builtin_amdgcn_make_buffer_rsrc(P.ws, (short)0, (int)WS_END, 0x00020000), tag, (unsigned)(uintptr_t)smem, smem};
    if (b < 256) {
        __builtin_amdgcn_s_setprio(A1_PRIO);
        const int x = b & 7, i = b >> 3, panel = (x >> 2) * 8 + (i & 7), jt = (x & 3) * 4 + (i >> 3);
        u32x4 z1p[9];
        tile_z(c, panel, jt, ROT ? ((i & 7) + 2 * (i >> 3)) % 9 : 0, z1p);
        __builtin_amdgcn_s_setprio(SP_PRIO);
        spatial_tile(c, panel, jt, ((i & 7) * SROT + (i >> 3) * SROT2) & 63, z1p);
        final_tile(c, 2 * panel + (jt >> 3), jt & 7, FL_H + panel * 16 + jt);
        STAMPV(P, 9);
        if (b == 0) {
            if (tid < 64) {
                for (unsigned spins = 0; spins < SPIN_CAP; ++spins) {
                    bool ok = true;
#pragma unroll
                    for (int k = 0; k < 4; ++k) {
                        ok = ok && __hip_atomic_load(flag_ptr(c, FL_H + 64 * k + tid), __ATOMIC_RELAXED, __HIP_MEMORY_SCOPE_AGENT) == tag;
                        ok = ok && __hip_atomic_load(flag_ptr(c, FL_O + 64 * k + tid), __ATOMIC_RELAXED, __HIP_MEMORY_SCOPE_AGENT) == tag;
                    }
                    if (__all(ok)) break;
                    __builtin_amdgcn_s_sleep(POLL_SLEEP);
                }
                if (tid == 0) __hip_atomic_store((gu64*)(P.ws + WS_EP), tag, __ATOMIC_RELAXED, __HIP_MEMORY_SCOPE_AGENT);
            }
        }
    } else {
        const int a = b - 256, x = a & 7, idx = a >> 3;
        __builtin_amdgcn_s_setprio(B1_PRIO);
        if (idx < 24) {
            const int hq = x & 3, ty = idx % 3, panel = (x >> 2) * 8 + idx / 3;
            const int s0 = ROT ? (idx / 3 + 3 * ty + 1) % 9 : 0;
            if (ty < 2) tile_qk(c, panel, ty, hq, s0); else tile_v(c, panel, hq, s0);
            const unsigned qf = FL_QKV + (ty * 4 + hq) * 16 + panel;
            STAMPV(P, 6);
            publish(c, qf);
            STAMPV(P, 7);
            const int k = idx * 8 + x;
            if (k < 90) prep_wcat(c, k, ~0u);
            else if (k < 154) prep_gate(c, k - 90, ~0u);
        } else {
            prep_wsp(c, (idx - 24) * 8 + x);
        }
        STAMPV(P, 1);
        __builtin_amdgcn_s_setprio(0);
        attn_unit(c, x * 4 + (idx & 3), idx >> 2);
        STAMPV(P, 5);
    }
}

extern "C" void kernel_launch(void* const* d_in, const int* in_sizes, int n_in, void* d_out, int out_size, void* d_ws, size_t ws_size, hipStream_t stream) {
    Params P;
    P.x = (const u16*)d_in[0]; P.wqkv = (const u16*)d_in[1]; P.wo = (const u16*)d_in[2]; P.win = (const u16*)d_in[4]; P.wsp = (const u16*)d_in[5];
    P.bsp = (const u16*)d_in[6]; P.wout = (const u16*)d_in[7]; P.gatew = (const u16*)d_in[8]; P.out = (u16*)d_out; P.ws = (char*)d_ws;
    hipFuncSetAttribute((const void*)mega_kernel, hipFuncAttributeMaxDynamicSharedMemorySize, MEGA_LDS);
    mega_kernel<<<512, 256, MEGA_LDS, stream>>>(P);
}
```

```cpp
#define FILL_ON_QKV 0
#define QKV_PRESLEEP 2
#define POLL_SLEEP 16
#include <hip/hip_runtime.h>
#include <stdint.h>

typedef __bf16 bf16x8 __attribute__((ext_vector_type(8)));
typedef __bf16 bf16x2 __attribute__((ext_vector_type(2)));
typedef short s16x4 __attribute__((ext_vector_type(4)));
typedef float f32x2 __attribute__((ext_vector_type(2)));
typedef float f32x4 __attribute__((ext_vector_type(4)));
typedef float f32x16 __attribute__((ext_vector_type(16)));
typedef unsigned u32x2 __attribute__((ext_vector_type(2)));
typedef unsigned u32x4 __attribute__((ext_vector_type(4)));
typedef unsigned short u16;
typedef unsigned long long u64;
typedef __attribute__((address_space(1))) unsigned long long gu64;
#define LAS __attribute__((address_space(3)))
#define DI __device__ __forceinline__

constexpr int S_ = 2048, D_ = 576, FH = 2304;
constexpr unsigned WS_EP = 0;
constexpr unsigned WS_FL = 256;
constexpr unsigned FL_ST = 0, FL_Z = 256, FL_H = 512, FL_QKV = 768, FL_WP = 960, FL_O = 1024, FL_WC = 1280, FL_N = FL_WC + 154;
constexpr unsigned WS_GT = 16384;
constexpr unsigned WS_ST = WS_GT + 8192;
constexpr unsigned WS_KM = WS_ST + 16 * 16 * 1024;
constexpr unsigned WS_KR = WS_KM + 32 * 64 * 1024;
constexpr unsigned WS_VT = WS_KR + 32 * 2048 * 4;
constexpr unsigned WS_QM = WS_VT + 32 * 128 * 640;
constexpr unsigned WS_QR = WS_QM + 32 * 64 * 1024;
constexpr unsigned WS_Z2 = WS_QR + 32 * 2048 * 4;
constexpr unsigned WS_WP = WS_Z2 + 144 * 64 * 1024;
constexpr unsigned WS_HC = WS_WP + 128 * 64 * 1024;
constexpr unsigned WS_WC = WS_HC + 128 * 90 * 1024;
constexpr unsigned WS_END = WS_WC + 36 * 90 * 1024;
constexpr size_t WS_DBG = 200u << 20;

#define ROT 1
#define PREP_NT 1
#define SC1_LOADS 1
#define PREP_SLEEP 8
#define WARM_Z1 1
struct Params {
    const u16* x; const u16* wqkv; const u16* wo; const u16* win; const u16* wsp; const u16* bsp; const u16* wout; const u16* gatew;
    u16* out; char* ws;
};

#define STAMPV(P_, idx) do {} while (0)

DI float bf2f(u16 v) { return __uint_as_float((unsigned)v << 16); }
DI float bflo(unsigned v) { return __uint_as_float(v << 16); }
DI float bfhi(unsigned v) { return __uint_as_float(v & 0xffff0000u); }
DI unsigned pk2(float lo, float hi) { f32x2 v = {lo, hi}; bf16x2 b = __builtin_convertvector(v, bf16x2); return __builtin_bit_cast(unsigned, b); }
DI unsigned rfl(unsigned v) { return __builtin_amdgcn_readfirstlane(v); }
DI u32x4 make_srd(const void* p, unsigned bytes) {
    uint64_t a = (uint64_t)p;
    u32x4 r; r[0] = rfl((unsigned)a); r[1] = rfl((unsigned)(a >> 32) & 0xffffu); r[2] = rfl(bytes); r[3] = 0x00020000u; return r;
}
#define SB() __builtin_amdgcn_sched_barrier(0)
#define BAR() asm volatile("s_waitcnt lgkmcnt(0)\n\ts_barrier" ::: "memory")
#define WAIT_BAR0() asm volatile("s_waitcnt vmcnt(0) lgkmcnt(0)\n\ts_barrier" ::: "memory")
DI void set_m0(unsigned base, unsigned imm) { asm volatile("s_add_u32 m0, %0, %1" ::"s"(base), "n"(imm) : "scc"); }
DI void set_m0s(unsigned a, unsigned b) { asm volatile("s_add_u32 m0, %0, %1" ::"s"(a), "s"(b) : "scc"); }
DI void dma_go(u32x4 srd, unsigned voff, unsigned soff) { asm volatile("buffer_load_dwordx4 %0, %1, %2 offen lds" ::"v"(voff), "s"(srd), "s"(soff) : "memory"); }
DI void dma_go_c(u32x4 srd, unsigned voff, unsigned soff) { asm volatile("buffer_load_dwordx4 %0, %1, %2 offen sc1 lds" ::"v"(voff), "s"(srd), "s"(soff) : "memory"); }
#define LDSC " sc1"
#define ACQUIRE() do {} while (0)
DI void dma_now(u32x4 srd, unsigned voff, unsigned soff, unsigned ldsaddr) { asm volatile("s_mov_b32 m0, %0\n\ts_nop 0\n\tbuffer_load_dwordx4 %1, %2, %3 offen nt lds" ::"s"(ldsaddr), "v"(voff), "s"(srd), "s"(soff) : "memory"); }
template <int IMM> DI void ld_frag(u32x4& d, u32x4 srd, unsigned voff, unsigned soff) {
    asm volatile("buffer_load_dwordx4 %0, %1, %2, %3 offen offset:%4" LDSC : "=v"(d) : "v"(voff), "s"(srd), "s"(soff), "n"(IMM) : "memory");
}
DI bf16x8 lds_rd128(unsigned a) { return *(const LAS bf16x8*)(uintptr_t)a; }
DI unsigned lds_rd32(unsigned a) { return *(const LAS unsigned*)(uintptr_t)a; }
DI s16x4 lds_rdtr(unsigned a) { return __builtin_amdgcn_ds_read_tr16_b64_v4i16((LAS s16x4*)(uintptr_t)a); }
DI bf16x8 cat8(s16x4 a, s16x4 b) { typedef short s16x8 __attribute__((ext_vector_type(8))); s16x8 v = __builtin_shufflevector(a, b, 0, 1, 2, 3, 4, 5, 6, 7); return __builtin_bit_cast(bf16x8, v); }
DI void mfma16v(f32x4& c, bf16x8 a, bf16x8 b) { asm volatile("v_mfma_f32_16x16x32_bf16 %0, %1, %2, %0" : "+v"(c) : "v"(a), "v"(b)); }
#define PIN18(acc, txt) asm volatile(txt : "+v"(acc[0][0]), "+v"(acc[0][1]), "+v"(acc[1][0]), "+v"(acc[1][1]), "+v"(acc[2][0]), "+v"(acc[2][1]), "+v"(acc[3][0]), "+v"(acc[3][1]), "+v"(acc[4][0]), "+v"(acc[4][1]), \
    "+v"(acc[5][0]), "+v"(acc[5][1]), "+v"(acc[6][0]), "+v"(acc[6][1]), "+v"(acc[7][0]), "+v"(acc[7][1]), "+v"(acc[8][0]), "+v"(acc[8][1]))
#define PIN5(acc, txt) asm volatile(txt : "+v"(acc[0]), "+v"(acc[1]), "+v"(acc[2]), "+v"(acc[3]), "+v"(acc[4]))
#define LGKM(n) __builtin_amdgcn_s_waitcnt(0xC07F | ((n) << 8))
#define OPAQUE(x) asm volatile("" : "+v"(x))
constexpr unsigned OOB = 0x40000000u;
#define EX2(x) __builtin_amdgcn_exp2f(x)
#define BC8(x) __builtin_bit_cast(bf16x8, x)
#define MFMA32(a, b, c) __builtin_amdgcn_mfma_f32_32x32x16_bf16(a, b, c, 0, 0, 0)

DI float gelu_t(float x) {
    const float c0 = -2.0f * 0.7978845608028654f * 1.4426950408889634f, c1 = c0 * 0.044715f;
    const float e = __builtin_amdgcn_exp2f(x * __builtin_fmaf(x * x, c1, c0));
    return x * __builtin_amdgcn_rcpf(1.0f + e);
}
template <int N> DI float dpp_ror_add(float v) { return v + __builtin_bit_cast(float, __builtin_amdgcn_update_dpp(0, __builtin_bit_cast(int, v), 0x120 + N, 0xf, 0xf, false)); }
DI float row16_sum(float v) { v = dpp_ror_add<8>(v); v = dpp_ror_add<4>(v); v = dpp_ror_add<2>(v); v = dpp_ror_add<1>(v); return v; }

struct Ctx { const Params& P; __amdgpu_buffer_rsrc_t ws; u64 tag; unsigned lds0; char* smem; };
#define ST_AUX 16
DI void st128(const Ctx& c, unsigned off, u32x4 v) { __builtin_amdgcn_raw_buffer_store_b128(v, c.ws, (int)off, 0, ST_AUX); }
DI void st64(const Ctx& c, unsigned off, u32x2 v) { __builtin_amdgcn_raw_buffer_store_b64(v, c.ws, (int)off, 0, 16); }
DI void st32(const Ctx& c, unsigned off, unsigned v) { __builtin_amdgcn_raw_buffer_store_b32(v, c.ws, (int)off, 0, 16); }
DI gu64* flag_ptr(const Ctx& c, unsigned idx) { return (gu64*)(c.P.ws + WS_FL) + idx; }
DI void publish(const Ctx& c, unsigned idx) {
    asm volatile("s_waitcnt vmcnt(0) lgkmcnt(0)\n\ts_barrier" ::: "memory");
    if (threadIdx.x == 0) __hip_atomic_store(flag_ptr(c, idx), c.tag, __ATOMIC_RELAXED, __HIP_MEMORY_SCOPE_AGENT);
}
#define SPIN_CAP (1u << 16)
DI void wait_flags(const Ctx& c, unsigned idx) {
    if (threadIdx.x < 64) {
        gu64* p = flag_ptr(c, idx == ~0u ? 0u : idx);
        for (unsigned spins = 0; spins < SPIN_CAP; ++spins) {
            const u64 v = idx == ~0u ? c.tag : __hip_atomic_load(p, __ATOMIC_RELAXED, __HIP_MEMORY_SCOPE_AGENT);
            if (__all(v == c.tag)) break;
            __builtin_amdgcn_s_sleep(POLL_SLEEP);
        }
        ACQUIRE();
        asm volatile("s_waitcnt vmcnt(0)" ::: "memory");
    }
    asm volatile("s_waitcnt lgkmcnt(0)\n\ts_barrier" ::: "memory");
}

DI void poll_flags(const Ctx& c, unsigned idx) {
    gu64* p = flag_ptr(c, idx == ~0u ? 0u : idx);
    for (unsigned spins = 0; spins < SPIN_CAP; ++spins) {
        const u64 v = idx == ~0u ? c.tag : __hip_atomic_load(p, __ATOMIC_RELAXED, __HIP_MEMORY_SCOPE_AGENT);
        if (__all(v == c.tag)) break;
        __builtin_amdgcn_s_sleep(POLL_SLEEP);
    }
}
DI void raise_flag(const Ctx& c, unsigned idx) { if (threadIdx.x == 0) __hip_atomic_store(flag_ptr(c, idx), c.tag, __ATOMIC_RELAXED, __HIP_MEMORY_SCOPE_AGENT); }
DI void wait_begin(const Ctx& c, unsigned idx) { if (threadIdx.x < 64) { poll_flags(c, idx); ACQUIRE(); } }
DI void wait_end() { asm volatile("s_waitcnt vmcnt(0) lgkmcnt(0)\n\ts_barrier" ::: "memory"); }

constexpr int G1_XS = 16384, G1_WS = 9216, G1_WOFF = 2 * G1_XS, G1_LDS = G1_WOFF + 4 * G1_WS;
struct G1Regs { bf16x8 wf[9], xf[2][2]; unsigned vX[4], vW[3], vw0, vw1, vx[2]; };
template <bool TRANS> DI void g1_mm(f32x4& c, bf16x8 wf, bf16x8 xf) { if (TRANS) mfma16v(c, wf, xf); else mfma16v(c, xf, wf); }
DI bf16x8 g1_rdw(unsigned lo, unsigned hi, int off) { return cat8(lds_rdtr(lo + off), lds_rdtr(hi + off)); }

template <bool TRANS, int J>
DI void g1_sub(f32x4 (&acc)[9][2], G1Regs& r, u32x4 srdX, u32x4 srdW, unsigned sWX, unsigned sWW, unsigned w, unsigned sX, unsigned sW) {
    constexpr int N1 = (J + 1) & 3, CUR = J & 1, NXT = CUR ^ 1, PW = (J + 3) & 3;
    constexpr int XS_RD = N1 >> 1, XS_WR = J >> 1;
    if (J & 1) asm volatile("s_waitcnt vmcnt(2) lgkmcnt(8)\n\ts_barrier" ::: "memory");
    else asm volatile("s_waitcnt vmcnt(6) lgkmcnt(8)\n\ts_barrier" ::: "memory");
    LGKM(8);
    SB();
#pragma unroll
    for (int n = 0; n < 9; ++n) {
        if (n == 5) LGKM(12);
        const bool dw = (n == 1 || n == 3), dx = (J & 1) && n >= 4 && n < 8;
        if (dw) set_m0(sWW, PW * G1_WS + (n == 1 ? 0 : 4096));
        if (dx) set_m0(sWX, XS_WR * G1_XS + (n - 4) * 1024);
        g1_mm<TRANS>(acc[n][0], r.wf[n], r.xf[CUR][0]);
        if (dw) dma_go(srdW, r.vW[n == 1 ? 0 : 1], sW);
        if (dx) dma_go(srdX, r.vX[n - 4], sX);
        g1_mm<TRANS>(acc[n][1], r.wf[n], r.xf[CUR][1]);
        if (n < 2) r.xf[NXT][n] = lds_rd128(r.vx[N1 & 1] + XS_RD * G1_XS + n * 2048);
        r.wf[n] = g1_rdw(r.vw0, r.vw1, N1 * G1_WS + n * 32);
        SB();
    }
    if (w == 0) { set_m0(sWW, PW * G1_WS + 8192); asm volatile("s_nop 0"); dma_go(srdW, r.vW[2], sW); }
    SB();
}

template <bool TRANS>
DI void gemm1_main(const Ctx& c, const u16* x, const u16* W, int ldw, int row0, int col0, unsigned lds0, f32x4 (&acc)[9][2], int s0, unsigned deferred_flag = ~0u) {
    const int tid = threadIdx.x, l = tid & 63; const unsigned w = rfl(tid >> 6);
    const u32x4 srdX = make_srd(x, S_ * D_ * 2), srdW = make_srd(W, (unsigned)(D_ * ldw * 2));
    const int g = l >> 4, q = (l & 15) >> 2, p = l & 3;
    G1Regs r;
#pragma unroll
    for (int i = 0; i < 4; ++i) { r.vX[i] = (unsigned)((row0 + 32 * w + 8 * i + (l >> 3)) * (D_ * 2) + (((l & 7) ^ (l >> 3)) << 4)); OPAQUE(r.vX[i]); }
#pragma unroll
    for (int i = 0; i < 3; ++i) { const int pc = (int)w + 4 * i, idx = 64 * pc + l, rho = idx / 18, cc = idx - rho * 18, k = rho ^ (((rho >> 3) & 1) << 2);
        r.vW[i] = pc < 9 ? (unsigned)(k * ldw * 2 + (col0 + 8 * cc) * 2) : OOB; OPAQUE(r.vW[i]); }
    r.vw0 = lds0 + (unsigned)(G1_WOFF + (8 * g + (q ^ (4 * (g & 1)))) * 288 + p * 8); r.vw1 = lds0 + (unsigned)(G1_WOFF + (8 * g + ((q + 4) ^ (4 * (g & 1)))) * 288 + p * 8);
    r.vx[0] = lds0 + (unsigned)((32 * w + (l & 15)) * 128) + ((g ^ (l & 7)) << 4); r.vx[1] = lds0 + (unsigned)((32 * w + (l & 15)) * 128) + (((4 + g) ^ (l & 7)) << 4);
    OPAQUE(r.vw0); OPAQUE(r.vw1); OPAQUE(r.vx[0]); OPAQUE(r.vx[1]);
    const unsigned sWX = lds0 + w * 4096, sWW = lds0 + G1_WOFF + w * 1024, kst = (unsigned)(32 * ldw * 2);
    auto xs = [&](int slot, unsigned so) {
#pragma unroll
        for (int i = 0; i < 4; ++i) { set_m0(sWX, slot * G1_XS + i * 1024); asm volatile("s_nop 0"); dma_go(srdX, r.vX[i], so); } };
    auto wsl = [&](int slot, unsigned so) {
#pragma unroll
        for (int i = 0; i < 2; ++i) { set_m0(sWW, slot * G1_WS + i * 4096); asm volatile("s_nop 0"); dma_go(srdW, r.vW[i], so); }
        if (w == 0) { set_m0(sWW, slot * G1_WS + 8192); asm volatile("s_nop 0"); dma_go(srdW, r.vW[2], so); } };
    auto rot9 = [&](int s) { const int t = s + s0; return (unsigned)(t >= 9 ? t - 9 : t); };
#define GW(v) ((v) < 18 ? (rot9((v) >> 1) * 2 + ((v) & 1)) * kst : OOB)
#define GX(s) ((s) < 9 ? rot9(s) * 128u : OOB)
    xs(0, GX(0)); wsl(0, GW(0)); wsl(1, GW(1)); wsl(2, GW(2)); xs(1, GX(1));
#pragma unroll
    for (int n = 0; n < 9; ++n) { acc[n][0] = f32x4{0, 0, 0, 0}; acc[n][1] = f32x4{0, 0, 0, 0}; }
    PIN18(acc, "s_nop 1");
    asm volatile("s_waitcnt vmcnt(8)\n\ts_barrier" ::: "memory");
    if (deferred_flag != ~0u) { raise_flag(c, deferred_flag); STAMPV(c.P, 12); } else STAMPV(c.P, 10);
#pragma unroll
    for (int tb = 0; tb < 2; ++tb) r.xf[0][tb] = lds_rd128(r.vx[0] + tb * 2048);
#pragma unroll
    for (int n = 0; n < 9; ++n) r.wf[n] = g1_rdw(r.vw0, r.vw1, n * 32);
    LGKM(0);
    for (int u = 0; u < 16; u += 4) {
        g1_sub<TRANS, 0>(acc, r, srdX, srdW, sWX, sWW, w, 0, GW(u + 3));
        g1_sub<TRANS, 1>(acc, r, srdX, srdW, sWX, sWW, w, GX(u / 2 + 2), GW(u + 4));
        g1_sub<TRANS, 2>(acc, r, srdX, srdW, sWX, sWW, w, 0, GW(u + 5));
        g1_sub<TRANS, 3>(acc, r, srdX, srdW, sWX, sWW, w, GX(u / 2 + 3), GW(u + 6));
    }
    g1_sub<TRANS, 0>(acc, r, srdX, srdW, sWX, sWW, w, 0, OOB);
    g1_sub<TRANS, 1>(acc, r, srdX, srdW, sWX, sWW, w, OOB, OOB);
    asm volatile("s_waitcnt vmcnt(0) lgkmcnt(0)\n\ts_barrier" ::: "memory");
    PIN18(acc, "s_nop 7\n\ts_nop 1");
}

DI void tile_z(const Ctx& c, int panel, int jt, int s0, u32x4 (&z1p)[9]) {
    const int tid = threadIdx.x, l = tid & 63; const unsigned w = rfl(tid >> 6); const int g = l >> 4;
    f32x4 y[9][2];
    gemm1_main<false>(c, c.P.x, c.P.win, 4608, panel * 128, FH + jt * 144, c.lds0, y, s0);
    STAMPV(c.P, 1);
    {
        float s1[2][4], s2[2][4];
#pragma unroll
        for (int tb = 0; tb < 2; ++tb)
#pragma unroll
            for (int r = 0; r < 4; ++r) { s1[tb][r] = 0.f; s2[tb][r] = 0.f; }
#pragma unroll
        for (int n = 0; n < 9; ++n)
#pragma unroll
            for (int tb = 0; tb < 2; ++tb)
#pragma unroll
                for (int r = 0; r < 4; ++r) { const float v = gelu_t(y[n][tb][r]); y[n][tb][r] = v; s1[tb][r] += v; s2[tb][r] = __builtin_fmaf(v, v, s2[tb][r]); }
#pragma unroll
        for (int tb = 0; tb < 2; ++tb)
#pragma unroll
            for (int r = 0; r < 4; ++r) {
                const float a = row16_sum(s1[tb][r]), b = row16_sum(s2[tb][r]);
                if ((l & 15) == 0) st64(c, WS_ST + (unsigned)((panel * 16 + jt) * 1024 + (32 * w + 16 * tb + 4 * g + r) * 8), u32x2{__float_as_uint(a), __float_as_uint(b)});
            }
    }
    STAMPV(c.P, 2);
    f32x4 acc[9][2];
    gemm1_main<true>(c, c.P.x, c.P.win, 4608, panel * 128, jt * 144, c.lds0, acc, s0, FL_ST + panel * 16 + jt);
    STAMPV(c.P, 3);
    if (tid < 64) poll_flags(c, tid < 16 ? FL_ST + panel * 16 + tid : ~0u);
    BAR();
    STAMPV(c.P, 13);
    u32x2 pv[8];
#pragma unroll
    for (int i = 0; i < 8; ++i) pv[i] = __builtin_amdgcn_raw_buffer_load_b64(c.ws, (int)(WS_ST + (panel * 16 + 8 * (l >> 5) + i) * 1024 + (32 * w + (l & 31)) * 8), 0, 16);
    SB();
#pragma unroll
    for (int n = 0; n < 9; ++n)
#pragma unroll
        for (int tb = 0; tb < 2; ++tb) { z1p[n][2 * tb] = pk2(gelu_t(acc[n][tb][0]), gelu_t(acc[n][tb][1])); z1p[n][2 * tb + 1] = pk2(gelu_t(acc[n][tb][2]), gelu_t(acc[n][tb][3])); }
    asm volatile("" : "+v"(z1p[0]), "+v"(z1p[1]), "+v"(z1p[2]), "+v"(z1p[3]), "+v"(z1p[4]), "+v"(z1p[5]), "+v"(z1p[6]), "+v"(z1p[7]), "+v"(z1p[8]));
    SB();
    {
        float a = 0.f, b = 0.f;
#pragma unroll
        for (int i = 0; i < 8; ++i) { a += __uint_as_float(pv[i][0]); b += __uint_as_float(pv[i][1]); }
        auto ra = __builtin_amdgcn_permlane32_swap(__float_as_uint(a), __float_as_uint(a), false, false);
        auto rb = __builtin_amdgcn_permlane32_swap(__float_as_uint(b), __float_as_uint(b), false, false);
        a = __uint_as_float(ra[0]) + __uint_as_float(ra[1]); b = __uint_as_float(rb[0]) + __uint_as_float(rb[1]);
        const float m = a * (1.0f / FH), var = __builtin_fmaxf(b * (1.0f / FH) - m * m, 0.f);
        const float rs = 1.0f / __builtin_sqrtf(var + 1e-5f);
        if (l < 32) *(f32x2*)(c.smem + (w * 32 + l) * 8) = f32x2{m, rs};
        asm volatile("s_waitcnt lgkmcnt(0)" ::: "memory");
    }
    f32x2 ms[2][4];
#pragma unroll
    for (int tb = 0; tb < 2; ++tb)
#pragma unroll
        for (int r = 0; r < 4; ++r) ms[tb][r] = *(const f32x2*)(c.smem + (w * 32 + 16 * tb + 4 * g + r) * 8);
    const unsigned dst = WS_Z2 + (unsigned)(((jt * 9) * 64 + panel * 4 + w) * 1024 + l * 16);
#pragma unroll
    for (int n = 0; n < 9; ++n) {
        unsigned pk[2][2];
#pragma unroll
        for (int tb = 0; tb < 2; ++tb) {
            float v[4];
#pragma unroll
            for (int r = 0; r < 4; ++r) v[r] = (y[n][tb][r] - ms[tb][r][0]) * ms[tb][r][1];
            pk[tb][0] = pk2(v[0], v[1]); pk[tb][1] = pk2(v[2], v[3]);
        }
        u32x4 o;
#pragma unroll
        for (int i = 0; i < 2; ++i) {
            auto a = __builtin_amdgcn_permlane32_swap(pk[0][i], pk[1][i], false, false);
            auto b = __builtin_amdgcn_permlane16_swap(a[0], a[1], false, false);
            o[i] = b[0]; o[2 + i] = b[1];
        }
        st128(c, dst + n * 65536, o);
    }
    STAMPV(c.P, 14);
    publish(c, FL_Z + jt * 16 + panel);
    STAMPV(c.P, 11);
    if (tid < 64) { poll_flags(c, FL_WC + tid); poll_flags(c, FL_WC + 64 + tid); poll_flags(c, tid < 26 ? FL_WC + 128 + tid : ~0u); }
    wait_begin(c, tid < 16 ? FL_Z + jt * 16 + tid : tid < 20 ? FL_WP + 4 * panel + (tid - 16) : ~0u);
    wait_end();
}

DI void tile_qk(const Ctx& c, int panel, int which, int hq, int s0) {
    f32x4 acc[9][2];
    gemm1_main<true>(c, c.P.x, c.P.wqkv, 1728, panel * 128, which * 576 + hq * 144, c.lds0, acc, s0);
    STAMPV(c.P, 8);
    const int l = threadIdx.x & 63; const unsigned w = rfl(threadIdx.x >> 6); const int g = l >> 4;
    const float sc = which == 0 ? 0.23570226039551584f * 1.4426950408889634f : 1.0f;
    char* st = c.smem + w * 9216;
#pragma unroll
    for (int n = 0; n < 9; ++n)
#pragma unroll
        for (int pp = 0; pp < 2; ++pp) {
            const int cc = 16 * n + 4 * g + 2 * pp, hl = cc / 18, d = cc - 18 * hl;
#pragma unroll
            for (int tb = 0; tb < 2; ++tb) {
                const int tok = 16 * tb + (l & 15);
                const int off = d < 16 ? hl * 1024 + (tok + 32 * (d >> 3)) * 16 + (d & 7) * 2 : 8192 + hl * 128 + tok * 4;
                *(unsigned*)(st + off) = pk2(acc[n][tb][2 * pp] * sc, acc[n][tb][2 * pp + 1] * sc);
            }
        }
    asm volatile("s_waitcnt lgkmcnt(0)" ::: "memory");
    const unsigned dm = (which == 0 ? WS_QM : WS_KM) + (unsigned)(((hq * 8) * 64 + panel * 4 + w) * 1024 + l * 16);
#pragma unroll
    for (int h = 0; h < 8; ++h) st128(c, dm + h * 65536, *(const u32x4*)(st + h * 1024 + l * 16));
    st128(c, (which == 0 ? WS_QR : WS_KR) + (unsigned)(((hq * 8 + (l >> 3)) * 2048 + panel * 128 + 32 * w) * 4 + (l & 7) * 16), *(const u32x4*)(st + 8192 + l * 16));
}

DI void tile_v(const Ctx& c, int panel, int hq, int s0) {
    f32x4 acc[9][2];
    gemm1_main<false>(c, c.P.x, c.P.wqkv, 1728, panel * 128, 2 * 576 + hq * 144, c.lds0, acc, s0);
    STAMPV(c.P, 8);
    const int l = threadIdx.x & 63; const unsigned w = rfl(threadIdx.x >> 6); const int g = l >> 4;
    char* st = c.smem + w * 10240;
    if (l < 32) {
        const int blk = l >> 1, hh = l & 1;
        *(u32x4*)(st + blk * 640 + (hh * 20 + 18) * 16) = u32x4{0x3f803f80u, 0x3f803f80u, 0x3f803f80u, 0x3f803f80u};
        *(u32x4*)(st + blk * 640 + (hh * 20 + 19) * 16) = u32x4{0, 0, 0, 0};
    }
#pragma unroll
    for (int n = 0; n < 9; ++n) {
        const int cc = 16 * n + (l & 15), hl = cc / 18, d = cc - 18 * hl;
#pragma unroll
        for (int tb = 0; tb < 2; ++tb)
            *(u32x2*)(st + (hl * 2 + tb) * 640 + ((g & 1) * 20 + d) * 16 + (g >> 1) * 8) = u32x2{pk2(acc[n][tb][0], acc[n][tb][1]), pk2(acc[n][tb][2], acc[n][tb][3])};
    }
    asm volatile("s_waitcnt lgkmcnt(0)" ::: "memory");
    const unsigned dv = WS_VT + (unsigned)(((hq * 8) * 128 + panel * 8 + 2 * w) * 640);
#pragma unroll
    for (int i = 0; i < 10; ++i) { const int idx = 64 * i + l, h = idx / 80, o = idx - 80 * h; st128(c, dv + (unsigned)(h * 128 * 640 + o * 16), *(const u32x4*)(st + idx * 16)); }
}

DI void prep_wsp(const Ctx& c, int n) {
    const int tid = threadIdx.x, l = tid & 63; const unsigned w = rfl(tid >> 6); const int g = l >> 4;
    const u32x4 srd = make_srd(c.P.wsp, S_ * S_ * 2);
    const int tb = 2 * n + (int)(w & 1), s0 = (int)(w >> 1) * 16;
    unsigned vsrc = (unsigned)((tb * 16 + (l >> 3)) * (S_ * 2) + (((l & 7) ^ (l >> 3)) << 4));
    const unsigned lw = c.lds0 + w * 16384;
    const unsigned rd0 = lw + (unsigned)((l & 15) * 128) + ((g ^ (l & 7)) << 4), rd1 = lw + (unsigned)((l & 15) * 128) + (((4 + g) ^ (l & 7)) << 4);
    auto batch = [&](int bt) {
#pragma unroll
        for (int i = 0; i < 8; ++i) {
            dma_now(srd, vsrc, (unsigned)((s0 + bt * 8 + i) * 128), lw + i * 2048);
            dma_now(srd, vsrc, (unsigned)((s0 + bt * 8 + i) * 128 + 8 * S_ * 2), lw + i * 2048 + 1024);
        }
    };
    u32x4 f[16];
    batch(0);
    asm volatile("s_waitcnt vmcnt(0)" ::: "memory");
#pragma unroll
    for (int i = 0; i < 8; ++i) { f[2 * i] = __builtin_bit_cast(u32x4, lds_rd128(rd0 + i * 2048)); f[2 * i + 1] = __builtin_bit_cast(u32x4, lds_rd128(rd1 + i * 2048)); }
#pragma unroll
    for (int i = 0; i < 16; ++i) OPAQUE(f[i]);
    batch(1);
#pragma unroll
    for (int i = 0; i < 16; ++i) st128(c, WS_WP + (unsigned)((tb * 64 + 2 * s0 + i) * 1024 + l * 16), f[i]);
    asm volatile("s_waitcnt vmcnt(16)" ::: "memory");
#pragma unroll
    for (int i = 0; i < 8; ++i) {
        st128(c, WS_WP + (unsigned)((tb * 64 + 2 * s0 + 16 + 2 * i) * 1024 + l * 16), __builtin_bit_cast(u32x4, lds_rd128(rd0 + i * 2048)));
        st128(c, WS_WP + (unsigned)((tb * 64 + 2 * s0 + 17 + 2 * i) * 1024 + l * 16), __builtin_bit_cast(u32x4, lds_rd128(rd1 + i * 2048)));
    }
    publish(c, FL_WP + n);
}

DI void prep_wcat(const Ctx& c, int kb, unsigned flag) {
    const int tid = threadIdx.x, l = tid & 63; const unsigned w = rfl(tid >> 6); const int g = l >> 4;
    const u32x4 srd = kb < 18 ? make_srd(c.P.wo, D_ * D_ * 2) : make_srd(c.P.wout, FH * D_ * 2);
#pragma unroll
    for (int i = 0; i < 9; ++i) {
        const int idx = 64 * ((int)w + 4 * i) + l, sg = idx / 72, ch = idx - 72 * sg, gg = sg >> 3, j = sg & 7;
        int row;
        if (kb < 16) { const int head = 2 * kb + (gg >> 1), h = gg & 1; row = 18 * head + (j < 4 ? 4 * h + j : 8 + 4 * h + (j - 4)); }
        else if (kb < 18) row = 18 * (16 * (kb - 16) + 4 * gg + (j >> 1)) + 16 + (j & 1);
        else row = 32 * (kb - 18) + (j < 4 ? 4 * gg + j : 16 + 4 * gg + (j - 4));
        dma_now(srd, (unsigned)(row * 1152 + ch * 16), 0u, c.lds0 + (w + 4 * i) * 1024);
    }
    WAIT_BAR0();
    const int q = (l & 15) >> 2, p = l & 3;
    const unsigned r0 = c.lds0 + (unsigned)((8 * g + q) * 1152 + p * 8);
#pragma unroll
    for (int i = 0; i < 9; ++i) {
        const int db = (int)w + 4 * i;
        st128(c, WS_WC + (unsigned)((db * 90 + kb) * 1024 + l * 16), __builtin_bit_cast(u32x4, cat8(lds_rdtr(r0 + db * 32), lds_rdtr(r0 + 4 * 1152 + db * 32))));
    }
    publish(c, FL_WC + kb);
}

DI void prep_gate(const Ctx& c, int n, unsigned flag) {
    const int tid = threadIdx.x, l = tid & 63; const int w = tid >> 6;
    const int row = n * 32 + w * 8 + (l >> 3);
    u32x4 xv[9], gv[9];
#pragma unroll
    for (int i = 0; i < 9; ++i) { xv[i] = *(const u32x4*)(c.P.x + row * 576 + ((l & 7) + 8 * i) * 8); gv[i] = *(const u32x4*)(c.P.gatew + ((l & 7) + 8 * i) * 8); }
    float a = 0.f;
#pragma unroll
    for (int i = 0; i < 9; ++i)
#pragma unroll
        for (int e = 0; e < 4; ++e) { a = __builtin_fmaf(bflo(xv[i][e]), bflo(gv[i][e]), a); a = __builtin_fmaf(bfhi(xv[i][e]), bfhi(gv[i][e]), a); }
    a += __shfl_xor(a, 1); a += __shfl_xor(a, 2); a += __shfl_xor(a, 4);
    if ((l & 7) == 0) st32(c, WS_GT + row * 4, __float_as_uint(1.0f / (1.0f + __expf(-a))));
    publish(c, FL_WC + 90 + n);
}

DI void warm_l2(const Ctx& c, int x, int rank) {
    const int tid = threadIdx.x; const unsigned w = rfl(tid >> 6);
    const u32x4 sx = make_srd(c.P.x, S_ * D_ * 2), sw = make_srd(c.P.win, D_ * 4608 * 2), sq = make_srd(c.P.wqkv, D_ * 1728 * 2);
    const unsigned dst = c.lds0 + 73728 + w * 256;
    auto touch = [&](u32x4 srd, unsigned off) { asm volatile("s_mov_b32 m0, %0\n\ts_nop 0\n\tbuffer_load_dword %1, %2, 0 offen lds" ::"s"(dst), "v"(off), "s"(srd) : "memory"); };
    const int i = rank * 256 + tid;
    if (w < 2) {
        const int j = rank * 128 + tid;
        { const int row = j / 18, k = j - row * 18; touch(sw, (unsigned)(row * 9216 + (k < 9 ? 0 : 4608) + (x & 3) * 1152 + (k < 9 ? k : k - 9) * 128)); }
        { const int j2 = j + 8192, row = j2 / 18, k = j2 - row * 18; touch(sw, j2 < 10368 ? (unsigned)(row * 9216 + (k < 9 ? 0 : 4608) + (x & 3) * 1152 + (k < 9 ? k : k - 9) * 128) : OOB); }
    } else {
        const int j = rank * 128 + (tid - 128);
        touch(sx, (unsigned)((x >> 2) * 1024 * 1152 + j * 128));
        { const int row = j / 9, k = j - row * 9, ty = k / 3, ln = k - 3 * ty;
          touch(sq, j < 5184 ? (unsigned)(row * 3456 + ty * 1152 + (((x & 3) * 288) & ~127) + ln * 128) : OOB);
        }
        touch(sx, j < 1024 ? (unsigned)((x >> 2) * 1024 * 1152 + (8192 + j) * 128) : OOB);
    }
    (void)i;
}

DI void warm_z1(const Ctx& c, int x, int rank) {
    const int tid = threadIdx.x; const unsigned w = rfl(tid >> 6);
    const u32x4 sw = make_srd(c.P.win, D_ * 4608 * 2);
    const unsigned dst = c.lds0 + 73728 + w * 256;
    if (w < 2) {
        const int j = rank * 81 + tid, row = j / 9, k = j - row * 9;
        const unsigned off = (tid < 81) ? (unsigned)(row * 9216 + (x & 3) * 1152 + k * 128) : OOB;
        asm volatile("s_mov_b32 m0, %0\n\ts_nop 0\n\tbuffer_load_dword %1, %2, 0 offen lds" ::"s"(dst), "v"(off), "s"(sw) : "memory");
    }
}

constexpr int AB_SLOT = 20 * 1024;
struct AttnState {
    f32x16 S[2], O[2]; u32x4 pl[2], ph[2];
    bf16x8 k0, v0, v1; u32x4 k1; u32x4 q0[2], q1[2];
};
#define AT_PRIO 0
#define AT_HI() do {} while (0)
#define AT_LO() do {} while (0)
template <int T, class F>
DI void attn_slot(AttnState& s, unsigned aK, unsigned aR, unsigned aV, const f32x16& zero, F&& hook) {
    constexpr int U = T ^ 1;
    AT_HI(); s.S[U] = MFMA32(s.k0, BC8(s.q0[U]), zero); SB(); AT_LO();
    SB();
#pragma unroll
    for (int i = 0; i < 4; ++i) s.S[T][i] = EX2(s.S[T][i]);
    SB();
    AT_HI(); s.S[U] = MFMA32(BC8(s.k1), BC8(s.q1[U]), s.S[U]); SB(); AT_LO();
    SB();
    if (T == 0) { s.k0 = lds_rd128(aK); s.k1[0] = lds_rd32(aR); }
#pragma unroll
    for (int i = 4; i < 8; ++i) s.S[T][i] = EX2(s.S[T][i]);
    s.pl[T][0] = pk2(s.S[T][0], s.S[T][1]); s.pl[T][1] = pk2(s.S[T][2], s.S[T][3]);
    SB();
    AT_HI(); s.O[U] = MFMA32(s.v1, BC8(s.ph[U]), s.O[U]); SB(); AT_LO();
    SB();
    if (T == 0) s.v1 = lds_rd128(aV);
    hook();
    s.pl[T][2] = pk2(s.S[T][4], s.S[T][5]); s.pl[T][3] = pk2(s.S[T][6], s.S[T][7]);
    SB();
#pragma unroll
    for (int i = 8; i < 12; ++i) s.S[T][i] = EX2(s.S[T][i]);
    SB();
    AT_HI(); s.O[T] = MFMA32(s.v0, BC8(s.pl[T]), s.O[T]); SB(); AT_LO();
    SB();
    if (T == 1) s.v0 = lds_rd128(aV);
#pragma unroll
    for (int i = 12; i < 16; ++i) s.S[T][i] = EX2(s.S[T][i]);
    SB();
    s.ph[T][0] = pk2(s.S[T][8], s.S[T][9]); s.ph[T][1] = pk2(s.S[T][10], s.S[T][11]);
    s.ph[T][2] = pk2(s.S[T][12], s.S[T][13]); s.ph[T][3] = pk2(s.S[T][14], s.S[T][15]);
    SB();
}

DI void attn_unit(const Ctx& c, int head, int qb) {
    const int tid = threadIdx.x, l = tid & 63; const unsigned w = rfl(tid >> 6); const int h = l >> 5, r32 = l & 31; const unsigned lds0 = c.lds0;
    {
        const int hq = head >> 3;
        for (int d = 0; d < QKV_PRESLEEP; ++d) __builtin_amdgcn_s_sleep(32);
        wait_flags(c, tid < 16 ? FL_QKV + (4 + hq) * 16 + tid : tid < 32 ? FL_QKV + (8 + hq) * 16 + (tid - 16) : tid < 34 ? FL_QKV + hq * 16 + 2 * qb + (tid - 32) : ~0u);
    }
    STAMPV(c.P, 2);
    const u32x4 srd = make_srd(c.P.ws + WS_KM, WS_QM - WS_KM);
    unsigned vl = (unsigned)l * 16; OPAQUE(vl);
    unsigned sLds[5], sSrc[5], sStep[5];
#pragma unroll
    for (int i = 0; i < 5; ++i) {
        const unsigned p = w + 4 * i;
        sLds[i] = lds0 + p * 1024;
        sSrc[i] = p < 8 ? (unsigned)(head * 65536) + p * 1024 : p < 18 ? (WS_VT - WS_KM) + (unsigned)(head * 81920) + (p - 8) * 1024
                : p == 18 ? (WS_KR - WS_KM) + (unsigned)(head * 8192) : OOB;
        sStep[i] = p < 8 ? 8192u : (p < 18 ? 10240u : (p == 18 ? 1024u : 0u));
    }
    auto piece = [&](int i, int ch, unsigned boff) { set_m0s(sLds[i], rfl(boff)); const unsigned so = ch < 8 ? sSrc[i] + (unsigned)ch * sStep[i] : OOB; asm volatile("s_nop 0"); dma_go_c(srd, vl, so); };
    auto issue = [&](int ch, unsigned boff) {
#pragma unroll
        for (int i = 0; i < 5; ++i) piece(i, ch, boff); };
    AttnState s;
    const int tok0 = qb * 256 + 64 * (int)w;
    issue(0, 0);
#pragma unroll
    for (int t = 0; t < 2; ++t) {
        s.q0[t] = __builtin_amdgcn_raw_buffer_load_b128(c.ws, (int)(WS_QM + ((unsigned)head * 64 + (tok0 >> 5) + t) * 1024 + l * 16), 0, 16);
        const unsigned qr = __builtin_amdgcn_raw_buffer_load_b32(c.ws, (int)(WS_QR + ((unsigned)head * 2048 + tok0 + 32 * t + r32) * 4), 0, 16);
        s.q1[t] = u32x4{h ? 0u : qr, 0u, 0u, 0u};
    }
    asm volatile("" : "+v"(s.q0[0]), "+v"(s.q0[1]), "+v"(s.q1[0]), "+v"(s.q1[1]));
    unsigned ka = lds0 + (unsigned)l * 16, ra = lds0 + 18432u + (unsigned)r32 * 4, va = lds0 + 8192u + (unsigned)(h * 20 + (r32 < 19 ? r32 : 19)) * 16;
    OPAQUE(ka); OPAQUE(ra); OPAQUE(va);
    const f32x16 zero = {};
    volatile LAS unsigned* flag = (volatile LAS unsigned*)(uintptr_t)(lds0 + 2 * AB_SLOT);

    for (int attempt = 0; attempt < 2; ++attempt) {
        float mx[2] = {-3.0e38f, -3.0e38f};
        {
            const u32x4 qz[2] = {u32x4{s.q1[0][0], 0u, 0u, 0u}, u32x4{s.q1[1][0], 0u, 0u, 0u}};
            const int nch = attempt ? 8 : 1, nblk = attempt ? 8 : 1;
            for (int ch = 0; ch < nch; ++ch) {
                if (attempt) { WAIT_BAR0(); issue(ch, 0); }
                WAIT_BAR0();
                for (int kb = 0; kb < nblk; ++kb) {
                    const bf16x8 k0 = lds_rd128(ka + kb * 1024);
                    const u32x4 k1 = u32x4{lds_rd32(ra + kb * 128), 0u, 0u, 0u};
#pragma unroll
                    for (int t = 0; t < 2; ++t) {
                        f32x16 sc = MFMA32(k0, BC8(s.q0[t]), zero); sc = MFMA32(BC8(k1), BC8(qz[t]), sc);
#pragma unroll
                        for (int r = 0; r < 16; ++r) mx[t] = __builtin_fmaxf(mx[t], sc[r]);
                    }
                }
            }
            if (attempt) { WAIT_BAR0(); issue(0, 0); WAIT_BAR0(); }
        }
#pragma unroll
        for (int t = 0; t < 2; ++t) {
            auto rr = __builtin_amdgcn_permlane32_swap(__float_as_uint(mx[t]), __float_as_uint(mx[t]), false, false);
            const float m = __builtin_fmaxf(__uint_as_float(rr[0]), __uint_as_float(rr[1]));
            const unsigned nb = __float_as_uint(-m), hi = nb & 0xffff0000u;
            const float r1 = -m - __uint_as_float(hi); const unsigned mid = __float_as_uint(r1) & 0xffff0000u;
            const float r2 = r1 - __uint_as_float(mid); const unsigned lo = __float_as_uint(r2) & 0xffff0000u;
            s.q1[t][1] = h ? 0u : ((hi >> 16) | mid); s.q1[t][2] = h ? 0u : (lo >> 16);
        }
        STAMPV(c.P, 3);
        s.O[0] = zero; s.O[1] = zero;
        s.k0 = lds_rd128(ka); s.k1 = u32x4{lds_rd32(ra), 0x3f803f80u, 0x00003f80u, 0u};
        s.v0 = lds_rd128(va); s.v1 = s.v0; s.ph[1] = u32x4{0u, 0u, 0u, 0u};
        s.S[0] = MFMA32(s.k0, BC8(s.q0[0]), zero); s.S[0] = MFMA32(BC8(s.k1), BC8(s.q1[0]), s.S[0]);
        unsigned cur = 0, nxt = AB_SLOT;
        auto nohook = [] {};
        for (int ch = 0; ch < 8; ++ch) {
            const unsigned cK = ka + cur, cR = ra + cur, cV = va + cur, nK = ka + nxt, nR = ra + nxt, nV = va + nxt;
#pragma unroll
            for (int j = 0; j < 7; ++j) {
                attn_slot<0>(s, cK + (j + 1) * 1024, cR + (j + 1) * 128, cV + j * 1280 + 640, zero, nohook);
                attn_slot<1>(s, 0, 0, cV + (j + 1) * 1280, zero, [&] { if (j < 5) piece(j, ch + 1, nxt); });
            }
            asm volatile("s_waitcnt vmcnt(0)\n\ts_barrier" ::: "memory");
            SB();
            attn_slot<0>(s, nK, nR, cV + 7 * 1280 + 640, zero, nohook);
            attn_slot<1>(s, 0, 0, nV, zero, [&] { asm volatile("s_barrier" ::: "memory"); });
            const unsigned t_ = cur; cur = nxt; nxt = t_;
        }
        s.O[1] = MFMA32(s.v1, BC8(s.ph[1]), s.O[1]);
        if (attempt == 1) break;
        bool bad = false;
#pragma unroll
        for (int t = 0; t < 2; ++t) {
            float am = 0.f; bool b = false;
#pragma unroll
            for (int r = 0; r < 11; ++r) { am = __builtin_fmaxf(am, __builtin_fabsf(s.O[t][r])); b = b || (s.O[t][r] != s.O[t][r]); }
            bad = bad || b || !(am < 1.0e30f);
        }
        if (tid == 0) *flag = 0u;
        WAIT_BAR0();
        if (__any(bad)) *flag = 1u;
        WAIT_BAR0();
        if (rfl(*flag) == 0u) break;
    }
    STAMPV(c.P, 4);
#pragma unroll
    for (int t = 0; t < 2; ++t) {
        auto rr = __builtin_amdgcn_permlane32_swap(__float_as_uint(s.O[t][10]), __float_as_uint(s.O[t][10]), false, false);
        const float inv = 1.0f / __uint_as_float(rr[0]);
        const int tok = tok0 + 32 * t + r32, tb = tok >> 4;
        const unsigned base = WS_HC + (unsigned)(tb * 90 * 1024);
        const u32x4 m = {pk2(s.O[t][0] * inv, s.O[t][1] * inv), pk2(s.O[t][2] * inv, s.O[t][3] * inv), pk2(s.O[t][4] * inv, s.O[t][5] * inv), pk2(s.O[t][6] * inv, s.O[t][7] * inv)};
        st128(c, base + (unsigned)((head >> 1) * 1024 + ((tok & 15) + 16 * (2 * (head & 1) + h)) * 16), m);
        if (h == 0) st32(c, base + (unsigned)((16 + (head >> 4)) * 1024 + ((tok & 15) + 16 * ((head & 15) >> 2)) * 16 + (head & 3) * 4), pk2(s.O[t][8] * inv, s.O[t][9] * inv));
    }
    publish(c, FL_O + qb * 32 + head);
}

constexpr int SD_N = 8, SD_SLOT = 9216;
struct SpdRegs { bf16x8 zf[9]; u32x4 t[4][2]; unsigned vZ[3], vz, vT; };
template <int U>
DI void spd_sub(f32x4 (&acc)[9][2], SpdRegs& r, u32x4 srdT, u32x4 srdZ, unsigned sWZ, unsigned w, unsigned sTa, unsigned sTb, unsigned sZ) {
    constexpr int NX = (U + 1) & (SD_N - 1), PF = (U + SD_N - 1) & (SD_N - 1), TU = U & 3, TP = (U + 3) & 3;
    asm volatile("s_waitcnt vmcnt(10)\n\ts_barrier" : "+v"(r.t[TU][0]), "+v"(r.t[TU][1])::"memory");
    SB();
#pragma unroll
    for (int n = 0; n < 9; ++n) {
        if (n == 0) LGKM(4);
        if (n == 5) LGKM(5);
        if (n == 4 || n == 6) set_m0(sWZ, PF * SD_SLOT + (n == 4 ? 0 : 4096));
        mfma16v(acc[n][0], r.zf[n], BC8(r.t[TU][0]));
        if (n == 0) ld_frag<0>(r.t[TP][0], srdT, r.vT, sTa);
        if (n == 2) ld_frag<0>(r.t[TP][1], srdT, r.vT, sTb);
        if (n == 4) dma_go_c(srdZ, r.vZ[0], sZ);
        if (n == 6) dma_go_c(srdZ, r.vZ[1], sZ);
        mfma16v(acc[n][1], r.zf[n], BC8(r.t[TU][1]));
        r.zf[n] = lds_rd128(r.vz + NX * SD_SLOT + n * 1024);
        SB();
    }
    if (w == 0) { set_m0(sWZ, PF * SD_SLOT + 8192); asm volatile("s_nop 0"); dma_go_c(srdZ, r.vZ[2], sZ); }
    SB();
}

DI void spatial_tile(const Ctx& c, int panel, int jt, int u0, const u32x4 (&z1p)[9]) {
    const int tid = threadIdx.x, l = tid & 63; const unsigned w = rfl(tid >> 6); const unsigned lds0 = c.lds0;
    STAMPV(c.P, 4);
    const u32x4 srdT = make_srd(c.P.ws + WS_WP + (size_t)(panel * 8 + 2 * w) * 65536, 2 * 65536), srdZ = make_srd(c.P.ws + WS_Z2 + (size_t)jt * 9 * 65536, 9 * 65536);
    SpdRegs r;
#pragma unroll
    for (int i = 0; i < 3; ++i) { r.vZ[i] = (w + 4 * i) * 65536 + l * 16; OPAQUE(r.vZ[i]); }
    r.vz = lds0 + l * 16; OPAQUE(r.vz);
    r.vT = l * 16; OPAQUE(r.vT);
    const unsigned sWZ = lds0 + w * 1024;
#define SO(v) ((v) < 64 ? (unsigned)(((v) + u0) & 63) * 1024u : OOB)
#define SO2(v) ((v) < 64 ? (unsigned)(((v) + u0) & 63) * 1024u + 65536u : OOB)
    f32x4 acc[9][2];
#pragma unroll
    for (int n = 0; n < 9; ++n) { acc[n][0] = f32x4{0, 0, 0, 0}; acc[n][1] = f32x4{0, 0, 0, 0}; }
    PIN18(acc, "s_nop 1");
#pragma unroll
    for (int u = 0; u < SD_N - 1; ++u) {
        if (u >= 4) { ld_frag<0>(r.t[u - 4][0], srdT, r.vT, SO(u - 4)); ld_frag<0>(r.t[u - 4][1], srdT, r.vT, SO2(u - 4)); }
#pragma unroll
        for (int i = 0; i < 2; ++i) { set_m0(sWZ, u * SD_SLOT + i * 4096); asm volatile("s_nop 0"); dma_go_c(srdZ, r.vZ[i], SO(u)); }
        if (w == 0) { set_m0(sWZ, u * SD_SLOT + 8192); asm volatile("s_nop 0"); dma_go_c(srdZ, r.vZ[2], SO(u)); }
    }
    asm volatile("s_waitcnt vmcnt(18)\n\ts_barrier" ::: "memory");
#pragma unroll
    for (int n = 0; n < 9; ++n) r.zf[n] = lds_rd128(r.vz + n * 1024);
    LGKM(0);
    for (int u = 0; u < 64; u += 8) {
        spd_sub<0>(acc, r, srdT, srdZ, sWZ, w, SO(u + 3), SO2(u + 3), SO(u + 7));
        spd_sub<1>(acc, r, srdT, srdZ, sWZ, w, SO(u + 4), SO2(u + 4), SO(u + 8));
        spd_sub<2>(acc, r, srdT, srdZ, sWZ, w, SO(u + 5), SO2(u + 5), SO(u + 9));
        spd_sub<3>(acc, r, srdT, srdZ, sWZ, w, SO(u + 6), SO2(u + 6), SO(u + 10));
        spd_sub<4>(acc, r, srdT, srdZ, sWZ, w, SO(u + 7), SO2(u + 7), SO(u + 11));
        spd_sub<5>(acc, r, srdT, srdZ, sWZ, w, SO(u + 8), SO2(u + 8), SO(u + 12));
        spd_sub<6>(acc, r, srdT, srdZ, sWZ, w, SO(u + 9), SO2(u + 9), SO(u + 13));
        spd_sub<7>(acc, r, srdT, srdZ, sWZ, w, SO(u + 10), SO2(u + 10), SO(u + 14));
    }
    asm volatile("s_waitcnt vmcnt(0) lgkmcnt(0)" : "+v"(r.t[0][0]), "+v"(r.t[0][1]), "+v"(r.t[1][0]), "+v"(r.t[1][1]), "+v"(r.t[2][0]), "+v"(r.t[2][1]), "+v"(r.t[3][0]), "+v"(r.t[3][1])::"memory");
    PIN18(acc, "s_nop 7\n\ts_nop 1");
    STAMPV(c.P, 5);
    float bs[2];
#pragma unroll
    for (int tb = 0; tb < 2; ++tb) bs[tb] = bf2f(c.P.bsp[panel * 128 + 32 * w + 16 * tb + (l & 15)]);
    unsigned hp[9][2][2];
#pragma unroll
    for (int n = 0; n < 9; ++n)
#pragma unroll
        for (int tb = 0; tb < 2; ++tb) {
            hp[n][tb][0] = pk2(bflo(z1p[n][2 * tb]) * (acc[n][tb][0] + bs[tb]), bfhi(z1p[n][2 * tb]) * (acc[n][tb][1] + bs[tb]));
            hp[n][tb][1] = pk2(bflo(z1p[n][2 * tb + 1]) * (acc[n][tb][2] + bs[tb]), bfhi(z1p[n][2 * tb + 1]) * (acc[n][tb][3] + bs[tb]));
        }
    const int fb0 = jt * 9;
#pragma unroll
    for (int tb = 0; tb < 2; ++tb) {
        const unsigned base = WS_HC + (unsigned)(((panel * 8 + 2 * w + tb) * 90 + 18) * 1024 + l * 16);
        if ((jt & 1) == 0) {
#pragma unroll
            for (int n = 0; n < 8; n += 2) st128(c, base + (unsigned)(((fb0 + n) >> 1) * 1024), u32x4{hp[n][tb][0], hp[n][tb][1], hp[n + 1][tb][0], hp[n + 1][tb][1]});
            st64(c, base + (unsigned)(((fb0 + 8) >> 1) * 1024), u32x2{hp[8][tb][0], hp[8][tb][1]});
        } else {
            st64(c, base + (unsigned)((fb0 >> 1) * 1024 + 8), u32x2{hp[0][tb][0], hp[0][tb][1]});
#pragma unroll
            for (int n = 1; n < 9; n += 2) st128(c, base + (unsigned)(((fb0 + n) >> 1) * 1024), u32x4{hp[n][tb][0], hp[n][tb][1], hp[n + 1][tb][0], hp[n + 1][tb][1]});
        }
    }
    STAMPV(c.P, 15);
}

constexpr int FN_STAGE = 16 * 1024;
struct FnRegs { bf16x8 wf[5]; u32x4 h[4][3]; unsigned vW[4], vrd, vl; };
template <int U, int VM>
DI void fn_stage(f32x4 (&acc)[5], FnRegs& r, u32x4 srdW, u32x4 srdH, unsigned sWW, unsigned sNext) {
    constexpr int PF = (U + 3) & 3;
    asm volatile("s_waitcnt vmcnt(%3)\n\ts_barrier" : "+v"(r.h[U][0]), "+v"(r.h[U][1]), "+v"(r.h[U][2]) : "n"(VM) : "memory");
    SB();
#pragma unroll
    for (int n = 0; n < 5; ++n) r.wf[n] = lds_rd128(r.vrd + U * FN_STAGE + (n * 3) * 1024);
    ld_frag<0>(r.h[PF][0], srdH, r.vl, sNext); ld_frag<1024>(r.h[PF][1], srdH, r.vl, sNext); ld_frag<2048>(r.h[PF][2], srdH, r.vl, sNext);
    SB();
#pragma unroll
    for (int kk = 0; kk < 3; ++kk) {
#pragma unroll
        for (int n = 0; n < 5; ++n) {
            const int i = kk * 5 + n;
            const bool dm = (i % 4 == 1);
            if (dm) set_m0(sWW, PF * FN_STAGE + (i / 4) * 4096);
            mfma16v(acc[n], r.wf[n], BC8(r.h[U][kk]));
            if (dm) dma_go_c(srdW, r.vW[i / 4], sNext);
            if (kk + 1 < 3) r.wf[n] = lds_rd128(r.vrd + U * FN_STAGE + (n * 3 + kk + 1) * 1024);
            SB();
        }
    }
}
DI void fn_pre_w(FnRegs& r, u32x4 srdW, unsigned sWW, int kb0, int rot) {
#pragma unroll
    for (int s = 0; s < 3; ++s)
#pragma unroll
        for (int i = 0; i < 4; ++i) { set_m0(sWW, s * FN_STAGE + i * 4096); asm volatile("s_nop 0"); dma_go_c(srdW, r.vW[i], (unsigned)kb0 * 1024u + (unsigned)(s + rot) * 3072u); }
}
template <int NST>
DI void fn_range(f32x4 (&acc)[5], FnRegs& r, u32x4 srdW, u32x4 srdH, unsigned sWW, int kb0, int rot) {
    const unsigned o0 = (unsigned)kb0 * 1024u;
#define FO(s) ((s) < NST ? o0 + (unsigned)((s) + rot >= NST ? (s) + rot - NST : (s) + rot) * 3072u : OOB)
#pragma unroll
    for (int s = 0; s < 3; ++s) { ld_frag<0>(r.h[s][0], srdH, r.vl, FO(s)); ld_frag<1024>(r.h[s][1], srdH, r.vl, FO(s)); ld_frag<2048>(r.h[s][2], srdH, r.vl, FO(s)); }
    asm volatile("s_waitcnt vmcnt(6)" : "+v"(r.h[0][0]), "+v"(r.h[0][1]), "+v"(r.h[0][2])::"memory");
    fn_stage<0, 63>(acc, r, srdW, srdH, sWW, FO(3));
    asm volatile("s_waitcnt vmcnt(10)" : "+v"(r.h[1][0]), "+v"(r.h[1][1]), "+v"(r.h[1][2])::"memory");
    fn_stage<1, 63>(acc, r, srdW, srdH, sWW, FO(4));
    fn_stage<2, 14>(acc, r, srdW, srdH, sWW, FO(5));
    fn_stage<3, 14>(acc, r, srdW, srdH, sWW, FO(6));
    for (int s = 4; s + 4 <= NST; s += 4) {
        fn_stage<0, 14>(acc, r, srdW, srdH, sWW, FO(s + 3)); fn_stage<1, 14>(acc, r, srdW, srdH, sWW, FO(s + 4));
        fn_stage<2, 14>(acc, r, srdW, srdH, sWW, FO(s + 5)); fn_stage<3, 14>(acc, r, srdW, srdH, sWW, FO(s + 6));
    }
    if (NST % 4 == 2) { fn_stage<0, 14>(acc, r, srdW, srdH, sWW, OOB); fn_stage<1, 14>(acc, r, srdW, srdH, sWW, OOB); }
    asm volatile("s_waitcnt vmcnt(0) lgkmcnt(0)\n\ts_barrier" : "+v"(r.h[0][0]), "+v"(r.h[0][1]), "+v"(r.h[0][2]), "+v"(r.h[1][0]), "+v"(r.h[1][1]), "+v"(r.h[1][2]),
                 "+v"(r.h[2][0]), "+v"(r.h[2][1]), "+v"(r.h[2][2]), "+v"(r.h[3][0]), "+v"(r.h[3][1]), "+v"(r.h[3][2])::"memory");
    PIN5(acc, "s_nop 7\n\ts_nop 1");
}

DI void final_tile(const Ctx& c, int tt, int j, unsigned hflag, int rot) {
    const int tid = threadIdx.x, l = tid & 63; const unsigned w = rfl(tid >> 6); const int g = l >> 4;
    const int db0 = (9 * j) >> 1;
    const u32x4 srdW = make_srd(c.P.ws + WS_WC + (size_t)db0 * 90 * 1024, 5 * 90 * 1024);
    const u32x4 srdH = make_srd(c.P.ws + WS_HC + (size_t)(tt * 4 + w) * 90 * 1024, 90 * 1024);
    FnRegs r;
    r.vl = (unsigned)l * 16; OPAQUE(r.vl);
#pragma unroll
    for (int i = 0; i < 4; ++i) { const unsigned p = w + 4 * i, db = p / 3, kk = p - db * 3; r.vW[i] = p < 15 ? (db * 90 + kk) * 1024 + l * 16 : OOB; OPAQUE(r.vW[i]); }
    r.vrd = c.lds0 + l * 16; OPAQUE(r.vrd);
    const unsigned sWW = c.lds0 + w * 1024;
    asm volatile("s_waitcnt vmcnt(0) lgkmcnt(0)\n\ts_barrier" ::: "memory");
    raise_flag(c, hflag);
    fn_pre_w(r, srdW, sWW, 18, rot);
    wait_begin(c, tid < 16 ? FL_H + (tt >> 1) * 16 + tid : ~0u);
    asm volatile("s_waitcnt lgkmcnt(0)\n\ts_barrier" ::: "memory");
    STAMPV(c.P, 6);
    f32x4 accA[5], accB[5];
#pragma unroll
    for (int n = 0; n < 5; ++n) { accA[n] = f32x4{0, 0, 0, 0}; accB[n] = f32x4{0, 0, 0, 0}; }
    PIN5(accA, "s_nop 1"); PIN5(accB, "s_nop 1");
    fn_range<24>(accB, r, srdW, srdH, sWW, 18, rot);
    STAMPV(c.P, 7);
    fn_pre_w(r, srdW, sWW, 0, 0);
    if (tid < 64) poll_flags(c, tid < 32 ? FL_O + (tt >> 2) * 32 + tid : ~0u);
    if (tid < 64) ACQUIRE();
    wait_end();
    STAMPV(c.P, 8);
    fn_range<6>(accA, r, srdW, srdH, sWW, 0, 0);
    const int tok = tt * 64 + 16 * w + (l & 15);
    const float gt = __uint_as_float(__builtin_amdgcn_raw_buffer_load_b32(c.ws, (int)(WS_GT + tok * 4), 0, 16));
#pragma unroll
    for (int n = 0; n < 5; ++n) {
        const int d = (db0 + n) * 16 + 4 * g;
        if (d >= 72 * j && d < 72 * j + 72) {
            float v[4];
#pragma unroll
            for (int rr = 0; rr < 4; ++rr) v[rr] = gt * accA[n][rr] + (1.0f - gt) * accB[n][rr];
            *(u32x2*)(c.P.out + tok * 576 + d) = u32x2{pk2(v[0], v[1]), pk2(v[2], v[3])};
        }
    }
}

constexpr int MEGA_LDS = 73728 + 1024;
#define FROT 1
#define A_DELAY 0
#define SP_PRIO 2
#define A1_PRIO 1
#define B1_PRIO 1
#define SROT 0
#define SROT2 0
__global__ __launch_bounds__(256, 2) void mega_kernel(Params P) {
    extern __shared__ __attribute__((aligned(16))) char smem[];
    const int b = blockIdx.x, tid = threadIdx.x;
    STAMPV(P, 0);
    const u64 ep = __hip_atomic_load((gu64*)(P.ws + WS_EP), __ATOMIC_RELAXED, __HIP_MEMORY_SCOPE_AGENT);
    const u64 tag = ep + 1ull;
    const Ctx c{P, __builtin_amdgcn_make_buffer_rsrc(P.ws, (short)0, (int)WS_END, 0x00020000), tag, (unsigned)(uintptr_t)smem, smem};
    warm_z1(c, b & 7, b >> 3);
    if (b < 256) {
        __builtin_amdgcn_s_setprio(A1_PRIO);
        const int x = b & 7, i = b >> 3, panel = (x >> 2) * 8 + (i & 7), jt = (x & 3) * 4 + (i >> 3);
        u32x4 z1p[9];
        tile_z(c, panel, jt, ROT ? ((i & 7) + 2 * (i >> 3)) % 9 : 0, z1p);
        __builtin_amdgcn_s_setprio(SP_PRIO);
        spatial_tile(c, panel, jt, ((i & 7) * SROT + (i >> 3) * SROT2) & 63, z1p);
        final_tile(c, 2 * panel + (jt >> 3), jt & 7, FL_H + panel * 16 + jt, FROT ? ((i & 7) * 2 + (i >> 3) * 1) % 21 : 0);
        STAMPV(P, 9);
        if (b == 0) {
            if (tid < 64) {
                for (unsigned spins = 0; spins < SPIN_CAP; ++spins) {
                    bool ok = true;
#pragma unroll
                    for (int k = 0; k < 4; ++k) {
                        ok = ok && __hip_atomic_load(flag_ptr(c, FL_H + 64 * k + tid), __ATOMIC_RELAXED, __HIP_MEMORY_SCOPE_AGENT) == tag;
                        ok = ok && __hip_atomic_load(flag_ptr(c, FL_O + 64 * k + tid), __ATOMIC_RELAXED, __HIP_MEMORY_SCOPE_AGENT) == tag;
                    }
                    if (__all(ok)) break;
                    __builtin_amdgcn_s_sleep(POLL_SLEEP);
                }
                if (tid == 0) __hip_atomic_store((gu64*)(P.ws + WS_EP), tag, __ATOMIC_RELAXED, __HIP_MEMORY_SCOPE_AGENT);
            }
        }
    } else {
        const int a = b - 256, x = a & 7, idx = a >> 3;
        __builtin_amdgcn_s_setprio(B1_PRIO);
        if (idx < 24) {
            const int hq = x & 3, ty = idx % 3, panel = (x >> 2) * 8 + idx / 3;
            const int s0 = ROT ? (idx / 3 + 3 * ty + 1) % 9 : 0;
            if (ty < 2) tile_qk(c, panel, ty, hq, s0); else tile_v(c, panel, hq, s0);
            const unsigned qf = FL_QKV + (ty * 4 + hq) * 16 + panel;
            STAMPV(P, 6);
            publish(c, qf);
            STAMPV(P, 7);

        } else {
            const int n = (idx - 24) * 8 + x;
            prep_wsp(c, n);
            prep_gate(c, n, ~0u);
            for (int kb = n; kb < 90; kb += 64) { BAR(); prep_wcat(c, kb, ~0u); }
        }
        STAMPV(P, 1);
        __builtin_amdgcn_s_setprio(0);
        attn_unit(c, x * 4 + (idx & 3), idx >> 2);
        STAMPV(P, 5);
    }
}

extern "C" void kernel_launch(void* const* d_in, const int* in_sizes, int n_in, void* d_out, int out_size, void* d_ws, size_t ws_size, hipStream_t stream) {
    Params P;
    P.x = (const u16*)d_in[0]; P.wqkv = (const u16*)d_in[1]; P.wo = (const u16*)d_in[2]; P.win = (const u16*)d_in[4]; P.wsp = (const u16*)d_in[5];
    P.bsp = (const u16*)d_in[6]; P.wout = (const u16*)d_in[7]; P.gatew = (const u16*)d_in[8]; P.out = (u16*)d_out; P.ws = (char*)d_ws;
    hipFuncSetAttribute((const void*)mega_kernel, hipFuncAttributeMaxDynamicSharedMemorySize, MEGA_LDS);
    mega_kernel<<<512, 256, MEGA_LDS, stream>>>(P);
}
```

```cpp
#include <hip/hip_runtime.h>
#include <stdint.h>

typedef __bf16 bf16x8 __attribute__((ext_vector_type(8)));
typedef __bf16 bf16x2 __attribute__((ext_vector_type(2)));
typedef short s16x4 __attribute__((ext_vector_type(4)));
typedef float f32x2 __attribute__((ext_vector_type(2)));
typedef float f32x4 __attribute__((ext_vector_type(4)));
typedef float f32x16 __attribute__((ext_vector_type(16)));
typedef unsigned u32x2 __attribute__((ext_vector_type(2)));
typedef unsigned u32x4 __attribute__((ext_vector_type(4)));
typedef unsigned short u16;
typedef unsigned long long u64;
typedef __attribute__((address_space(1))) unsigned long long gu64;
#define LAS __attribute__((address_space(3)))
#define DI __device__ __forceinline__

constexpr int S_ = 2048, D_ = 576, FH = 2304;
constexpr unsigned WS_EP = 0;
constexpr unsigned WS_FL = 256;
constexpr unsigned FL_ST = 0, FL_Z = 256, FL_H = 512, FL_QKV = 768, FL_WP = 960, FL_O = 1024, FL_WC = 1280, FL_N = FL_WC + 154;
constexpr unsigned WS_GT = 16384;
constexpr unsigned WS_ST = WS_GT + 8192;
constexpr unsigned WS_KM = WS_ST + 16 * 16 * 1024;
constexpr unsigned WS_KR = WS_KM + 32 * 64 * 1024;
constexpr unsigned WS_VT = WS_KR + 32 * 2048 * 4;
constexpr unsigned WS_QM = WS_VT + 32 * 128 * 640;
constexpr unsigned WS_QR = WS_QM + 32 * 64 * 1024;
constexpr unsigned WS_Z2 = WS_QR + 32 * 2048 * 4;
constexpr unsigned WS_WP = WS_Z2 + 144 * 64 * 1024;
constexpr unsigned WS_HC = WS_WP + 128 * 64 * 1024;
constexpr unsigned WS_WC = WS_HC + 128 * 90 * 1024;
constexpr unsigned WS_END = WS_WC + 36 * 90 * 1024;
constexpr size_t WS_DBG = 200u << 20;

#define ROT 1
#define PREP_NT 1
#define SC1_LOADS 1
#define PREP_SLEEP 8
#define FILL_ON_QKV 1
#define QKV_PRESLEEP 0
#define WARM_Z1 0
struct Params {
    const u16* x; const u16* wqkv; const u16* wo; const u16* win; const u16* wsp; const u16* bsp; const u16* wout; const u16* gatew;
    u16* out; char* ws;
};

#define STAMPV(P_, idx) do {} while (0)

DI float bf2f(u16 v) { return __uint_as_float((unsigned)v << 16); }
DI float bflo(unsigned v) { return __uint_as_float(v << 16); }
DI float bfhi(unsigned v) { return __uint_as_float(v & 0xffff0000u); }
DI unsigned pk2(float lo, float hi) { f32x2 v = {lo, hi}; bf16x2 b = __builtin_convertvector(v, bf16x2); return __builtin_bit_cast(unsigned, b); }
DI unsigned rfl(unsigned v) { return __builtin_amdgcn_readfirstlane(v); }
DI u32x4 make_srd(const void* p, unsigned bytes) {
    uint64_t a = (uint64_t)p;
    u32x4 r; r[0] = rfl((unsigned)a); r[1] = rfl((unsigned)(a >> 32) & 0xffffu); r[2] = rfl(bytes); r[3] = 0x00020000u; return r;
}
#define SB() __builtin_amdgcn_sched_barrier(0)
#define BAR() asm volatile("s_waitcnt lgkmcnt(0)\n\ts_barrier" ::: "memory")
#define WAIT_BAR0() asm volatile("s_waitcnt vmcnt(0) lgkmcnt(0)\n\ts_barrier" ::: "memory")
DI void set_m0(unsigned base, unsigned imm) { asm volatile("s_add_u32 m0, %0, %1" ::"s"(base), "n"(imm) : "scc"); }
DI void set_m0s(unsigned a, unsigned b) { asm volatile("s_add_u32 m0, %0, %1" ::"s"(a), "s"(b) : "scc"); }
DI void dma_go(u32x4 srd, unsigned voff, unsigned soff) { asm volatile("buffer_load_dwordx4 %0, %1, %2 offen lds" ::"v"(voff), "s"(srd), "s"(soff) : "memory"); }
DI void dma_go_c(u32x4 srd, unsigned voff, unsigned soff) { asm volatile("buffer_load_dwordx4 %0, %1, %2 offen sc1 lds" ::"v"(voff), "s"(srd), "s"(soff) : "memory"); }
#define LDSC " sc1"
#define ACQUIRE() do {} while (0)
DI void dma_now(u32x4 srd, unsigned voff, unsigned soff, unsigned ldsaddr) { asm volatile("s_mov_b32 m0, %0\n\ts_nop 0\n\tbuffer_load_dwordx4 %1, %2, %3 offen nt lds" ::"s"(ldsaddr), "v"(voff), "s"(srd), "s"(soff) : "memory"); }
template <int IMM> DI void ld_frag(u32x4& d, u32x4 srd, unsigned voff, unsigned soff) {
    asm volatile("buffer_load_dwordx4 %0, %1, %2, %3 offen offset:%4" LDSC : "=v"(d) : "v"(voff), "s"(srd), "s"(soff), "n"(IMM) : "memory");
}
DI bf16x8 lds_rd128(unsigned a) { return *(const LAS bf16x8*)(uintptr_t)a; }
DI unsigned lds_rd32(unsigned a) { return *(const LAS unsigned*)(uintptr_t)a; }
DI s16x4 lds_rdtr(unsigned a) { return __builtin_amdgcn_ds_read_tr16_b64_v4i16((LAS s16x4*)(uintptr_t)a); }
DI bf16x8 cat8(s16x4 a, s16x4 b) { typedef short s16x8 __attribute__((ext_vector_type(8))); s16x8 v = __builtin_shufflevector(a, b, 0, 1, 2, 3, 4, 5, 6, 7); return __builtin_bit_cast(bf16x8, v); }
DI void mfma16v(f32x4& c, bf16x8 a, bf16x8 b) { asm volatile("v_mfma_f32_16x16x32_bf16 %0, %1, %2, %0" : "+v"(c) : "v"(a), "v"(b)); }
#define PIN18(acc, txt) asm volatile(txt : "+v"(acc[0][0]), "+v"(acc[0][1]), "+v"(acc[1][0]), "+v"(acc[1][1]), "+v"(acc[2][0]), "+v"(acc[2][1]), "+v"(acc[3][0]), "+v"(acc[3][1]), "+v"(acc[4][0]), "+v"(acc[4][1]), \
    "+v"(acc[5][0]), "+v"(acc[5][1]), "+v"(acc[6][0]), "+v"(acc[6][1]), "+v"(acc[7][0]), "+v"(acc[7][1]), "+v"(acc[8][0]), "+v"(acc[8][1]))
#define PIN5(acc, txt) asm volatile(txt : "+v"(acc[0]), "+v"(acc[1]), "+v"(acc[2]), "+v"(acc[3]), "+v"(acc[4]))
#define LGKM(n) __builtin_amdgcn_s_waitcnt(0xC07F | ((n) << 8))
#define OPAQUE(x) asm volatile("" : "+v"(x))
constexpr unsigned OOB = 0x40000000u;
#define EX2(x) __builtin_amdgcn_exp2f(x)
#define BC8(x) __builtin_bit_cast(bf16x8, x)
#define MFMA32(a, b, c) __builtin_amdgcn_mfma_f32_32x32x16_bf16(a, b, c, 0, 0, 0)

DI float gelu_t(float x) {
    const float c0 = -2.0f * 0.7978845608028654f * 1.4426950408889634f, c1 = c0 * 0.044715f;
    const float e = __builtin_amdgcn_exp2f(x * __builtin_fmaf(x * x, c1, c0));
    return x * __builtin_amdgcn_rcpf(1.0f + e);
}
template <int N> DI float dpp_ror_add(float v) { return v + __builtin_bit_cast(float, __builtin_amdgcn_update_dpp(0, __builtin_bit_cast(int, v), 0x120 + N, 0xf, 0xf, false)); }
DI float row16_sum(float v) { v = dpp_ror_add<8>(v); v = dpp_ror_add<4>(v); v = dpp_ror_add<2>(v); v = dpp_ror_add<1>(v); return v; }

struct Ctx { const Params& P; __amdgpu_buffer_rsrc_t ws; u64 tag; unsigned lds0; char* smem; };
#define ST_AUX 16
DI void st128(const Ctx& c, unsigned off, u32x4 v) { __builtin_amdgcn_raw_buffer_store_b128(v, c.ws, (int)off, 0, ST_AUX); }
DI void st64(const Ctx& c, unsigned off, u32x2 v) { __builtin_amdgcn_raw_buffer_store_b64(v, c.ws, (int)off, 0, 16); }
DI void st32(const Ctx& c, unsigned off, unsigned v) { __builtin_amdgcn_raw_buffer_store_b32(v, c.ws, (int)off, 0, 16); }
DI gu64* flag_ptr(const Ctx& c, unsigned idx) { return (gu64*)(c.P.ws + WS_FL) + idx; }
DI void publish(const Ctx& c, unsigned idx) {
    asm volatile("s_waitcnt vmcnt(0) lgkmcnt(0)\n\ts_barrier" ::: "memory");
    if (threadIdx.x == 0) __hip_atomic_store(flag_ptr(c, idx), c.tag, __ATOMIC_RELAXED, __HIP_MEMORY_SCOPE_AGENT);
}
#define POLL_SLEEP 8
#define SPIN_CAP (1u << 16)
DI void wait_flags(const Ctx& c, unsigned idx) {
    if (threadIdx.x < 64) {
        gu64* p = flag_ptr(c, idx == ~0u ? 0u : idx);
        for (unsigned spins = 0; spins < SPIN_CAP; ++spins) {
            const u64 v = idx == ~0u ? c.tag : __hip_atomic_load(p, __ATOMIC_RELAXED, __HIP_MEMORY_SCOPE_AGENT);
            if (__all(v == c.tag)) break;
            __builtin_amdgcn_s_sleep(POLL_SLEEP);
        }
        ACQUIRE();
        asm volatile("s_waitcnt vmcnt(0)" ::: "memory");
    }
    asm volatile("s_waitcnt lgkmcnt(0)\n\ts_barrier" ::: "memory");
}

DI void poll_flags(const Ctx& c, unsigned idx) {
    gu64* p = flag_ptr(c, idx == ~0u ? 0u : idx);
    for (unsigned spins = 0; spins < SPIN_CAP; ++spins) {
        const u64 v = idx == ~0u ? c.tag : __hip_atomic_load(p, __ATOMIC_RELAXED, __HIP_MEMORY_SCOPE_AGENT);
        if (__all(v == c.tag)) break;
        __builtin_amdgcn_s_sleep(POLL_SLEEP);
    }
}
DI void raise_flag(const Ctx& c, unsigned idx) { if (threadIdx.x == 0) __hip_atomic_store(flag_ptr(c, idx), c.tag, __ATOMIC_RELAXED, __HIP_MEMORY_SCOPE_AGENT); }
DI void wait_begin(const Ctx& c, unsigned idx) { if (threadIdx.x < 64) { poll_flags(c, idx); ACQUIRE(); } }
DI void wait_end() { asm volatile("s_waitcnt vmcnt(0) lgkmcnt(0)\n\ts_barrier" ::: "memory"); }

constexpr int G1_XS = 16384, G1_WS = 9216, G1_WOFF = 2 * G1_XS, G1_LDS = G1_WOFF + 4 * G1_WS;
struct G1Regs { bf16x8 wf[9], xf[2][2]; unsigned vX[4], vW[3], vw0, vw1, vx[2]; };
template <bool TRANS> DI void g1_mm(f32x4& c, bf16x8 wf, bf16x8 xf) { if (TRANS) mfma16v(c, wf, xf); else mfma16v(c, xf, wf); }
DI bf16x8 g1_rdw(unsigned lo, unsigned hi, int off) { return cat8(lds_rdtr(lo + off), lds_rdtr(hi + off)); }

template <bool TRANS, int J>
DI void g1_sub(f32x4 (&acc)[9][2], G1Regs& r, u32x4 srdX, u32x4 srdW, unsigned sWX, unsigned sWW, unsigned w, unsigned sX, unsigned sW) {
    constexpr int N1 = (J + 1) & 3, CUR = J & 1, NXT = CUR ^ 1, PW = (J + 3) & 3;
    constexpr int XS_RD = N1 >> 1, XS_WR = J >> 1;
    if (J & 1) asm volatile("s_waitcnt vmcnt(2) lgkmcnt(8)\n\ts_barrier" ::: "memory");
    else asm volatile("s_waitcnt vmcnt(6) lgkmcnt(8)\n\ts_barrier" ::: "memory");
    LGKM(8);
    SB();
#pragma unroll
    for (int n = 0; n < 9; ++n) {
        if (n == 5) LGKM(12);
        const bool dw = (n == 1 || n == 3), dx = (J & 1) && n >= 4 && n < 8;
        if (dw) set_m0(sWW, PW * G1_WS + (n == 1 ? 0 : 4096));
        if (dx) set_m0(sWX, XS_WR * G1_XS + (n - 4) * 1024);
        g1_mm<TRANS>(acc[n][0], r.wf[n], r.xf[CUR][0]);
        if (dw) dma_go(srdW, r.vW[n == 1 ? 0 : 1], sW);
        if (dx) dma_go(srdX, r.vX[n - 4], sX);
        g1_mm<TRANS>(acc[n][1], r.wf[n], r.xf[CUR][1]);
        if (n < 2) r.xf[NXT][n] = lds_rd128(r.vx[N1 & 1] + XS_RD * G1_XS + n * 2048);
        r.wf[n] = g1_rdw(r.vw0, r.vw1, N1 * G1_WS + n * 32);
        SB();
    }
    if (w == 0) { set_m0(sWW, PW * G1_WS + 8192); asm volatile("s_nop 0"); dma_go(srdW, r.vW[2], sW); }
    SB();
}

template <bool TRANS>
DI void gemm1_main(const Ctx& c, const u16* x, const u16* W, int ldw, int row0, int col0, unsigned lds0, f32x4 (&acc)[9][2], int s0, unsigned deferred_flag = ~0u) {
    const int tid = threadIdx.x, l = tid & 63; const unsigned w = rfl(tid >> 6);
    const u32x4 srdX = make_srd(x, S_ * D_ * 2), srdW = make_srd(W, (unsigned)(D_ * ldw * 2));
    const int g = l >> 4, q = (l & 15) >> 2, p = l & 3;
    G1Regs r;
#pragma unroll
    for (int i = 0; i < 4; ++i) { r.vX[i] = (unsigned)((row0 + 32 * w + 8 * i + (l >> 3)) * (D_ * 2) + (((l & 7) ^ (l >> 3)) << 4)); OPAQUE(r.vX[i]); }
#pragma unroll
    for (int i = 0; i < 3; ++i) { const int pc = (int)w + 4 * i, idx = 64 * pc + l, rho = idx / 18, cc = idx - rho * 18, k = rho ^ (((rho >> 3) & 1) << 2);
        r.vW[i] = pc < 9 ? (unsigned)(k * ldw * 2 + (col0 + 8 * cc) * 2) : OOB; OPAQUE(r.vW[i]); }
    r.vw0 = lds0 + (unsigned)(G1_WOFF + (8 * g + (q ^ (4 * (g & 1)))) * 288 + p * 8); r.vw1 = lds0 + (unsigned)(G1_WOFF + (8 * g + ((q + 4) ^ (4 * (g & 1)))) * 288 + p * 8);
    r.vx[0] = lds0 + (unsigned)((32 * w + (l & 15)) * 128) + ((g ^ (l & 7)) << 4); r.vx[1] = lds0 + (unsigned)((32 * w + (l & 15)) * 128) + (((4 + g) ^ (l & 7)) << 4);
    OPAQUE(r.vw0); OPAQUE(r.vw1); OPAQUE(r.vx[0]); OPAQUE(r.vx[1]);
    const unsigned sWX = lds0 + w * 4096, sWW = lds0 + G1_WOFF + w * 1024, kst = (unsigned)(32 * ldw * 2);
    auto xs = [&](int slot, unsigned so) {
#pragma unroll
        for (int i = 0; i < 4; ++i) { set_m0(sWX, slot * G1_XS + i * 1024); asm volatile("s_nop 0"); dma_go(srdX, r.vX[i], so); } };
    auto wsl = [&](int slot, unsigned so) {
#pragma unroll
        for (int i = 0; i < 2; ++i) { set_m0(sWW, slot * G1_WS + i * 4096); asm volatile("s_nop 0"); dma_go(srdW, r.vW[i], so); }
        if (w == 0) { set_m0(sWW, slot * G1_WS + 8192); asm volatile("s_nop 0"); dma_go(srdW, r.vW[2], so); } };
    auto rot9 = [&](int s) { const int t = s + s0; return (unsigned)(t >= 9 ? t - 9 : t); };
#define GW(v) ((v) < 18 ? (rot9((v) >> 1) * 2 + ((v) & 1)) * kst : OOB)
#define GX(s) ((s) < 9 ? rot9(s) * 128u : OOB)
    xs(0, GX(0)); wsl(0, GW(0)); wsl(1, GW(1)); wsl(2, GW(2)); xs(1, GX(1));
#pragma unroll
    for (int n = 0; n < 9; ++n) { acc[n][0] = f32x4{0, 0, 0, 0}; acc[n][1] = f32x4{0, 0, 0, 0}; }
    PIN18(acc, "s_nop 1");
    asm volatile("s_waitcnt vmcnt(8)\n\ts_barrier" ::: "memory");
    if (deferred_flag != ~0u) { raise_flag(c, deferred_flag); STAMPV(c.P, 12); } else STAMPV(c.P, 10);
#pragma unroll
    for (int tb = 0; tb < 2; ++tb) r.xf[0][tb] = lds_rd128(r.vx[0] + tb * 2048);
#pragma unroll
    for (int n = 0; n < 9; ++n) r.wf[n] = g1_rdw(r.vw0, r.vw1, n * 32);
    LGKM(0);
    for (int u = 0; u < 16; u += 4) {
        g1_sub<TRANS, 0>(acc, r, srdX, srdW, sWX, sWW, w, 0, GW(u + 3));
        g1_sub<TRANS, 1>(acc, r, srdX, srdW, sWX, sWW, w, GX(u / 2 + 2), GW(u + 4));
        g1_sub<TRANS, 2>(acc, r, srdX, srdW, sWX, sWW, w, 0, GW(u + 5));
        g1_sub<TRANS, 3>(acc, r, srdX, srdW, sWX, sWW, w, GX(u / 2 + 3), GW(u + 6));
    }
    g1_sub<TRANS, 0>(acc, r, srdX, srdW, sWX, sWW, w, 0, OOB);
    g1_sub<TRANS, 1>(acc, r, srdX, srdW, sWX, sWW, w, OOB, OOB);
    asm volatile("s_waitcnt vmcnt(0) lgkmcnt(0)\n\ts_barrier" ::: "memory");
    PIN18(acc, "s_nop 7\n\ts_nop 1");
}

DI void tile_z(const Ctx& c, int panel, int jt, int s0, u32x4 (&z1p)[9]) {
    const int tid = threadIdx.x, l = tid & 63; const unsigned w = rfl(tid >> 6); const int g = l >> 4;
    f32x4 y[9][2];
    gemm1_main<false>(c, c.P.x, c.P.win, 4608, panel * 128, FH + jt * 144, c.lds0, y, s0);
    STAMPV(c.P, 1);
    {
        float s1[2][4], s2[2][4];
#pragma unroll
        for (int tb = 0; tb < 2; ++tb)
#pragma unroll
            for (int r = 0; r < 4; ++r) { s1[tb][r] = 0.f; s2[tb][r] = 0.f; }
#pragma unroll
        for (int n = 0; n < 9; ++n)
#pragma unroll
            for (int tb = 0; tb < 2; ++tb)
#pragma unroll
                for (int r = 0; r < 4; ++r) { const float v = gelu_t(y[n][tb][r]); y[n][tb][r] = v; s1[tb][r] += v; s2[tb][r] = __builtin_fmaf(v, v, s2[tb][r]); }
#pragma unroll
        for (int tb = 0; tb < 2; ++tb)
#pragma unroll
            for (int r = 0; r < 4; ++r) {
                const float a = row16_sum(s1[tb][r]), b = row16_sum(s2[tb][r]);
                if ((l & 15) == 0) st64(c, WS_ST + (unsigned)((panel * 16 + jt) * 1024 + (32 * w + 16 * tb + 4 * g + r) * 8), u32x2{__float_as_uint(a), __float_as_uint(b)});
            }
    }
    STAMPV(c.P, 2);
    f32x4 acc[9][2];
    gemm1_main<true>(c, c.P.x, c.P.win, 4608, panel * 128, jt * 144, c.lds0, acc, s0, FL_ST + panel * 16 + jt);
    STAMPV(c.P, 3);
    if (tid < 64) poll_flags(c, tid < 16 ? FL_ST + panel * 16 + tid : ~0u);
    BAR();
    STAMPV(c.P, 13);
    u32x2 pv[8];
#pragma unroll
    for (int i = 0; i < 8; ++i) pv[i] = __builtin_amdgcn_raw_buffer_load_b64(c.ws, (int)(WS_ST + (panel * 16 + 8 * (l >> 5) + i) * 1024 + (32 * w + (l & 31)) * 8), 0, 16);
    SB();
#pragma unroll
    for (int n = 0; n < 9; ++n)
#pragma unroll
        for (int tb = 0; tb < 2; ++tb) { z1p[n][2 * tb] = pk2(gelu_t(acc[n][tb][0]), gelu_t(acc[n][tb][1])); z1p[n][2 * tb + 1] = pk2(gelu_t(acc[n][tb][2]), gelu_t(acc[n][tb][3])); }
    asm volatile("" : "+v"(z1p[0]), "+v"(z1p[1]), "+v"(z1p[2]), "+v"(z1p[3]), "+v"(z1p[4]), "+v"(z1p[5]), "+v"(z1p[6]), "+v"(z1p[7]), "+v"(z1p[8]));
    SB();
    {
        float a = 0.f, b = 0.f;
#pragma unroll
        for (int i = 0; i < 8; ++i) { a += __uint_as_float(pv[i][0]); b += __uint_as_float(pv[i][1]); }
        auto ra = __builtin_amdgcn_permlane32_swap(__float_as_uint(a), __float_as_uint(a), false, false);
        auto rb = __builtin_amdgcn_permlane32_swap(__float_as_uint(b), __float_as_uint(b), false, false);
        a = __uint_as_float(ra[0]) + __uint_as_float(ra[1]); b = __uint_as_float(rb[0]) + __uint_as_float(rb[1]);
        const float m = a * (1.0f / FH), var = __builtin_fmaxf(b * (1.0f / FH) - m * m, 0.f);
        const float rs = 1.0f / __builtin_sqrtf(var + 1e-5f);
        if (l < 32) *(f32x2*)(c.smem + (w * 32 + l) * 8) = f32x2{m, rs};
        asm volatile("s_waitcnt lgkmcnt(0)" ::: "memory");
    }
    f32x2 ms[2][4];
#pragma unroll
    for (int tb = 0; tb < 2; ++tb)
#pragma unroll
        for (int r = 0; r < 4; ++r) ms[tb][r] = *(const f32x2*)(c.smem + (w * 32 + 16 * tb + 4 * g + r) * 8);
    const unsigned dst = WS_Z2 + (unsigned)(((jt * 9) * 64 + panel * 4 + w) * 1024 + l * 16);
#pragma unroll
    for (int n = 0; n < 9; ++n) {
        unsigned pk[2][2];
#pragma unroll
        for (int tb = 0; tb < 2; ++tb) {
            float v[4];
#pragma unroll
            for (int r = 0; r < 4; ++r) v[r] = (y[n][tb][r] - ms[tb][r][0]) * ms[tb][r][1];
            pk[tb][0] = pk2(v[0], v[1]); pk[tb][1] = pk2(v[2], v[3]);
        }
        u32x4 o;
#pragma unroll
        for (int i = 0; i < 2; ++i) {
            auto a = __builtin_amdgcn_permlane32_swap(pk[0][i], pk[1][i], false, false);
            auto b = __builtin_amdgcn_permlane16_swap(a[0], a[1], false, false);
            o[i] = b[0]; o[2 + i] = b[1];
        }
        st128(c, dst + n * 65536, o);
    }
    STAMPV(c.P, 14);
    publish(c, FL_Z + jt * 16 + panel);
    STAMPV(c.P, 11);
    if (tid < 64) { poll_flags(c, FL_WC + tid); poll_flags(c, FL_WC + 64 + tid); poll_flags(c, tid < 26 ? FL_WC + 128 + tid : ~0u); }
    wait_begin(c, tid < 16 ? FL_Z + jt * 16 + tid : tid < 20 ? FL_WP + 4 * panel + (tid - 16) : ~0u);
    wait_end();
}

DI void tile_qk(const Ctx& c, int panel, int which, int hq, int s0) {
    f32x4 acc[9][2];
    gemm1_main<true>(c, c.P.x, c.P.wqkv, 1728, panel * 128, which * 576 + hq * 144, c.lds0, acc, s0);
    STAMPV(c.P, 8);
    const int l = threadIdx.x & 63; const unsigned w = rfl(threadIdx.x >> 6); const int g = l >> 4;
    const float sc = which == 0 ? 0.23570226039551584f * 1.4426950408889634f : 1.0f;
    char* st = c.smem + w * 9216;
#pragma unroll
    for (int n = 0; n < 9; ++n)
#pragma unroll
        for (int pp = 0; pp < 2; ++pp) {
            const int cc = 16 * n + 4 * g + 2 * pp, hl = cc / 18, d = cc - 18 * hl;
#pragma unroll
            for (int tb = 0; tb < 2; ++tb) {
                const int tok = 16 * tb + (l & 15);
                const int off = d < 16 ? hl * 1024 + (tok + 32 * (d >> 3)) * 16 + (d & 7) * 2 : 8192 + hl * 128 + tok * 4;
                *(unsigned*)(st + off) = pk2(acc[n][tb][2 * pp] * sc, acc[n][tb][2 * pp + 1] * sc);
            }
        }
    asm volatile("s_waitcnt lgkmcnt(0)" ::: "memory");
    const unsigned dm = (which == 0 ? WS_QM : WS_KM) + (unsigned)(((hq * 8) * 64 + panel * 4 + w) * 1024 + l * 16);
#pragma unroll
    for (int h = 0; h < 8; ++h) st128(c, dm + h * 65536, *(const u32x4*)(st + h * 1024 + l * 16));
    st128(c, (which == 0 ? WS_QR : WS_KR) + (unsigned)(((hq * 8 + (l >> 3)) * 2048 + panel * 128 + 32 * w) * 4 + (l & 7) * 16), *(const u32x4*)(st + 8192 + l * 16));
}

DI void tile_v(const Ctx& c, int panel, int hq, int s0) {
    f32x4 acc[9][2];
    gemm1_main<false>(c, c.P.x, c.P.wqkv, 1728, panel * 128, 2 * 576 + hq * 144, c.lds0, acc, s0);
    STAMPV(c.P, 8);
    const int l = threadIdx.x & 63; const unsigned w = rfl(threadIdx.x >> 6); const int g = l >> 4;
    char* st = c.smem + w * 10240;
    if (l < 32) {
        const int blk = l >> 1, hh = l & 1;
        *(u32x4*)(st + blk * 640 + (hh * 20 + 18) * 16) = u32x4{0x3f803f80u, 0x3f803f80u, 0x3f803f80u, 0x3f803f80u};
        *(u32x4*)(st + blk * 640 + (hh * 20 + 19) * 16) = u32x4{0, 0, 0, 0};
    }
#pragma unroll
    for (int n = 0; n < 9; ++n) {
        const int cc = 16 * n + (l & 15), hl = cc / 18, d = cc - 18 * hl;
#pragma unroll
        for (int tb = 0; tb < 2; ++tb)
            *(u32x2*)(st + (hl * 2 + tb) * 640 + ((g & 1) * 20 + d) * 16 + (g >> 1) * 8) = u32x2{pk2(acc[n][tb][0], acc[n][tb][1]), pk2(acc[n][tb][2], acc[n][tb][3])};
    }
    asm volatile("s_waitcnt lgkmcnt(0)" ::: "memory");
    const unsigned dv = WS_VT + (unsigned)(((hq * 8) * 128 + panel * 8 + 2 * w) * 640);
#pragma unroll
    for (int i = 0; i < 10; ++i) { const int idx = 64 * i + l, h = idx / 80, o = idx - 80 * h; st128(c, dv + (unsigned)(h * 128 * 640 + o * 16), *(const u32x4*)(st + idx * 16)); }
}

DI void prep_wsp(const Ctx& c, int n) {
    const int tid = threadIdx.x, l = tid & 63; const unsigned w = rfl(tid >> 6); const int g = l >> 4;
    const u32x4 srd = make_srd(c.P.wsp, S_ * S_ * 2);
    const int tb = 2 * n + (int)(w & 1), s0 = (int)(w >> 1) * 16;
    unsigned vsrc = (unsigned)((tb * 16 + (l >> 3)) * (S_ * 2) + (((l & 7) ^ (l >> 3)) << 4));
    const unsigned lw = c.lds0 + w * 16384;
    const unsigned rd0 = lw + (unsigned)((l & 15) * 128) + ((g ^ (l & 7)) << 4), rd1 = lw + (unsigned)((l & 15) * 128) + (((4 + g) ^ (l & 7)) << 4);
    auto batch = [&](int bt) {
#pragma unroll
        for (int i = 0; i < 8; ++i) {
            dma_now(srd, vsrc, (unsigned)((s0 + bt * 8 + i) * 128), lw + i * 2048);
            dma_now(srd, vsrc, (unsigned)((s0 + bt * 8 + i) * 128 + 8 * S_ * 2), lw + i * 2048 + 1024);
        }
    };
    u32x4 f[16];
    batch(0);
    asm volatile("s_waitcnt vmcnt(0)" ::: "memory");
#pragma unroll
    for (int i = 0; i < 8; ++i) { f[2 * i] = __builtin_bit_cast(u32x4, lds_rd128(rd0 + i * 2048)); f[2 * i + 1] = __builtin_bit_cast(u32x4, lds_rd128(rd1 + i * 2048)); }
#pragma unroll
    for (int i = 0; i < 16; ++i) OPAQUE(f[i]);
    batch(1);
#pragma unroll
    for (int i = 0; i < 16; ++i) st128(c, WS_WP + (unsigned)((tb * 64 + 2 * s0 + i) * 1024 + l * 16), f[i]);
    asm volatile("s_waitcnt vmcnt(16)" ::: "memory");
#pragma unroll
    for (int i = 0; i < 8; ++i) {
        st128(c, WS_WP + (unsigned)((tb * 64 + 2 * s0 + 16 + 2 * i) * 1024 + l * 16), __builtin_bit_cast(u32x4, lds_rd128(rd0 + i * 2048)));
        st128(c, WS_WP + (unsigned)((tb * 64 + 2 * s0 + 17 + 2 * i) * 1024 + l * 16), __builtin_bit_cast(u32x4, lds_rd128(rd1 + i * 2048)));
    }
    publish(c, FL_WP + n);
}

DI void prep_wcat(const Ctx& c, int kb, unsigned flag) {
    const int tid = threadIdx.x, l = tid & 63; const unsigned w = rfl(tid >> 6); const int g = l >> 4;
    const u32x4 srd = kb < 18 ? make_srd(c.P.wo, D_ * D_ * 2) : make_srd(c.P.wout, FH * D_ * 2);
#pragma unroll
    for (int i = 0; i < 9; ++i) {
        const int idx = 64 * ((int)w + 4 * i) + l, sg = idx / 72, ch = idx - 72 * sg, gg = sg >> 3, j = sg & 7;
        int row;
        if (kb < 16) { const int head = 2 * kb + (gg >> 1), h = gg & 1; row = 18 * head + (j < 4 ? 4 * h + j : 8 + 4 * h + (j - 4)); }
        else if (kb < 18) row = 18 * (16 * (kb - 16) + 4 * gg + (j >> 1)) + 16 + (j & 1);
        else row = 32 * (kb - 18) + (j < 4 ? 4 * gg + j : 16 + 4 * gg + (j - 4));
        dma_now(srd, (unsigned)(row * 1152 + ch * 16), 0u, c.lds0 + (w + 4 * i) * 1024);
    }
    WAIT_BAR0();
    const int q = (l & 15) >> 2, p = l & 3;
    const unsigned r0 = c.lds0 + (unsigned)((8 * g + q) * 1152 + p * 8);
#pragma unroll
    for (int i = 0; i < 9; ++i) {
        const int db = (int)w + 4 * i;
        st128(c, WS_WC + (unsigned)((db * 90 + kb) * 1024 + l * 16), __builtin_bit_cast(u32x4, cat8(lds_rdtr(r0 + db * 32), lds_rdtr(r0 + 4 * 1152 + db * 32))));
    }
    publish(c, FL_WC + kb);
}

DI void prep_gate(const Ctx& c, int n, unsigned flag) {
    const int tid = threadIdx.x, l = tid & 63; const int w = tid >> 6;
    const int row = n * 32 + w * 8 + (l >> 3);
    u32x4 xv[9], gv[9];
#pragma unroll
    for (int i = 0; i < 9; ++i) { xv[i] = *(const u32x4*)(c.P.x + row * 576 + ((l & 7) + 8 * i) * 8); gv[i] = *(const u32x4*)(c.P.gatew + ((l & 7) + 8 * i) * 8); }
    float a = 0.f;
#pragma unroll
    for (int i = 0; i < 9; ++i)
#pragma unroll
        for (int e = 0; e < 4; ++e) { a = __builtin_fmaf(bflo(xv[i][e]), bflo(gv[i][e]), a); a = __builtin_fmaf(bfhi(xv[i][e]), bfhi(gv[i][e]), a); }
    a += __shfl_xor(a, 1); a += __shfl_xor(a, 2); a += __shfl_xor(a, 4);
    if ((l & 7) == 0) st32(c, WS_GT + row * 4, __float_as_uint(1.0f / (1.0f + __expf(-a))));
    publish(c, FL_WC + 90 + n);
}

DI void warm_l2(const Ctx& c, int x, int rank) {
    const int tid = threadIdx.x; const unsigned w = rfl(tid >> 6);
    const u32x4 sx = make_srd(c.P.x, S_ * D_ * 2), sw = make_srd(c.P.win, D_ * 4608 * 2), sq = make_srd(c.P.wqkv, D_ * 1728 * 2);
    const unsigned dst = c.lds0 + 73728 + w * 256;
    auto touch = [&](u32x4 srd, unsigned off) { asm volatile("s_mov_b32 m0, %0\n\ts_nop 0\n\tbuffer_load_dword %1, %2, 0 offen lds" ::"s"(dst), "v"(off), "s"(srd) : "memory"); };
    const int i = rank * 256 + tid;
    if (w < 2) {
        const int j = rank * 128 + tid;
        { const int row = j / 18, k = j - row * 18; touch(sw, (unsigned)(row * 9216 + (k < 9 ? 0 : 4608) + (x & 3) * 1152 + (k < 9 ? k : k - 9) * 128)); }
        { const int j2 = j + 8192, row = j2 / 18, k = j2 - row * 18; touch(sw, j2 < 10368 ? (unsigned)(row * 9216 + (k < 9 ? 0 : 4608) + (x & 3) * 1152 + (k < 9 ? k : k - 9) * 128) : OOB); }
    } else {
        const int j = rank * 128 + (tid - 128);
        touch(sx, (unsigned)((x >> 2) * 1024 * 1152 + j * 128));
        { const int row = j / 9, k = j - row * 9, ty = k / 3, ln = k - 3 * ty;
          touch(sq, j < 5184 ? (unsigned)(row * 3456 + ty * 1152 + (((x & 3) * 288) & ~127) + ln * 128) : OOB);
        }
        touch(sx, j < 1024 ? (unsigned)((x >> 2) * 1024 * 1152 + (8192 + j) * 128) : OOB);
    }
    (void)i;
}

DI void warm_z1(const Ctx& c, int x, int rank) {
    const int tid = threadIdx.x; const unsigned w = rfl(tid >> 6);
    const u32x4 sw = make_srd(c.P.win, D_ * 4608 * 2);
    const unsigned dst = c.lds0 + 73728 + w * 256;
    if (w < 2) {
        const int j = rank * 81 + tid, row = j / 9, k = j - row * 9;
        const unsigned off = (tid < 81) ? (unsigned)(row * 9216 + (x & 3) * 1152 + k * 128) : OOB;
        asm volatile("s_mov_b32 m0, %0\n\ts_nop 0\n\tbuffer_load_dword %1, %2, 0 offen lds" ::"s"(dst), "v"(off), "s"(sw) : "memory");
    }
}

constexpr int AB_SLOT = 20 * 1024;
struct AttnState {
    f32x16 S[2], O[2]; u32x4 pl[2], ph[2];
    bf16x8 k0, v0, v1; u32x4 k1; u32x4 q0[2], q1[2];
};
#define AT_PRIO 0
#define AT_HI() do {} while (0)
#define AT_LO() do {} while (0)
template <int T, class F>
DI void attn_slot(AttnState& s, unsigned aK, unsigned aR, unsigned aV, const f32x16& zero, F&& hook) {
    constexpr int U = T ^ 1;
    AT_HI(); s.S[U] = MFMA32(s.k0, BC8(s.q0[U]), zero); SB(); AT_LO();
    SB();
#pragma unroll
    for (int i = 0; i < 4; ++i) s.S[T][i] = EX2(s.S[T][i]);
    SB();
    AT_HI(); s.S[U] = MFMA32(BC8(s.k1), BC8(s.q1[U]), s.S[U]); SB(); AT_LO();
    SB();
    if (T == 0) { s.k0 = lds_rd128(aK); s.k1[0] = lds_rd32(aR); }
#pragma unroll
    for (int i = 4; i < 8; ++i) s.S[T][i] = EX2(s.S[T][i]);
    s.pl[T][0] = pk2(s.S[T][0], s.S[T][1]); s.pl[T][1] = pk2(s.S[T][2], s.S[T][3]);
    SB();
    AT_HI(); s.O[U] = MFMA32(s.v1, BC8(s.ph[U]), s.O[U]); SB(); AT_LO();
    SB();
    if (T == 0) s.v1 = lds_rd128(aV);
    hook();
    s.pl[T][2] = pk2(s.S[T][4], s.S[T][5]); s.pl[T][3] = pk2(s.S[T][6], s.S[T][7]);
    SB();
#pragma unroll
    for (int i = 8; i < 12; ++i) s.S[T][i] = EX2(s.S[T][i]);
    SB();
    AT_HI(); s.O[T] = MFMA32(s.v0, BC8(s.pl[T]), s.O[T]); SB(); AT_LO();
    SB();
    if (T == 1) s.v0 = lds_rd128(aV);
#pragma unroll
    for (int i = 12; i < 16; ++i) s.S[T][i] = EX2(s.S[T][i]);
    SB();
    s.ph[T][0] = pk2(s.S[T][8], s.S[T][9]); s.ph[T][1] = pk2(s.S[T][10], s.S[T][11]);
    s.ph[T][2] = pk2(s.S[T][12], s.S[T][13]); s.ph[T][3] = pk2(s.S[T][14], s.S[T][15]);
    SB();
}

DI void attn_unit(const Ctx& c, int head, int qb) {
    const int tid = threadIdx.x, l = tid & 63; const unsigned w = rfl(tid >> 6); const int h = l >> 5, r32 = l & 31; const unsigned lds0 = c.lds0;
    {
        const int hq = head >> 3;
        wait_flags(c, tid < 16 ? FL_QKV + (4 + hq) * 16 + tid : tid < 32 ? FL_QKV + (8 + hq) * 16 + (tid - 16) : tid < 34 ? FL_QKV + hq * 16 + 2 * qb + (tid - 32) : ~0u);
    }
    STAMPV(c.P, 2);
    const u32x4 srd = make_srd(c.P.ws + WS_KM, WS_QM - WS_KM);
    unsigned vl = (unsigned)l * 16; OPAQUE(vl);
    unsigned sLds[5], sSrc[5], sStep[5];
#pragma unroll
    for (int i = 0; i < 5; ++i) {
        const unsigned p = w + 4 * i;
        sLds[i] = lds0 + p * 1024;
        sSrc[i] = p < 8 ? (unsigned)(head * 65536) + p * 1024 : p < 18 ? (WS_VT - WS_KM) + (unsigned)(head * 81920) + (p - 8) * 1024
                : p == 18 ? (WS_KR - WS_KM) + (unsigned)(head * 8192) : OOB;
        sStep[i] = p < 8 ? 8192u : (p < 18 ? 10240u : (p == 18 ? 1024u : 0u));
    }
    auto piece = [&](int i, int ch, unsigned boff) { set_m0s(sLds[i], rfl(boff)); const unsigned so = ch < 8 ? sSrc[i] + (unsigned)ch * sStep[i] : OOB; asm volatile("s_nop 0"); dma_go_c(srd, vl, so); };
    auto issue = [&](int ch, unsigned boff) {
#pragma unroll
        for (int i = 0; i < 5; ++i) piece(i, ch, boff); };
    AttnState s;
    const int tok0 = qb * 256 + 64 * (int)w;
    issue(0, 0);
#pragma unroll
    for (int t = 0; t < 2; ++t) {
        s.q0[t] = __builtin_amdgcn_raw_buffer_load_b128(c.ws, (int)(WS_QM + ((unsigned)head * 64 + (tok0 >> 5) + t) * 1024 + l * 16), 0, 16);
        const unsigned qr = __builtin_amdgcn_raw_buffer_load_b32(c.ws, (int)(WS_QR + ((unsigned)head * 2048 + tok0 + 32 * t + r32) * 4), 0, 16);
        s.q1[t] = u32x4{h ? 0u : qr, 0u, 0u, 0u};
    }
    asm volatile("" : "+v"(s.q0[0]), "+v"(s.q0[1]), "+v"(s.q1[0]), "+v"(s.q1[1]));
    unsigned ka = lds0 + (unsigned)l * 16, ra = lds0 + 18432u + (unsigned)r32 * 4, va = lds0 + 8192u + (unsigned)(h * 20 + (r32 < 19 ? r32 : 19)) * 16;
    OPAQUE(ka); OPAQUE(ra); OPAQUE(va);
    const f32x16 zero = {};
    volatile LAS unsigned* flag = (volatile LAS unsigned*)(uintptr_t)(lds0 + 2 * AB_SLOT);

    for (int attempt = 0; attempt < 2; ++attempt) {
        float mx[2] = {-3.0e38f, -3.0e38f};
        {
            const u32x4 qz[2] = {u32x4{s.q1[0][0], 0u, 0u, 0u}, u32x4{s.q1[1][0], 0u, 0u, 0u}};
            const int nch = attempt ? 8 : 1, nblk = attempt ? 8 : 1;
            for (int ch = 0; ch < nch; ++ch) {
                if (attempt) { WAIT_BAR0(); issue(ch, 0); }
                WAIT_BAR0();
                for (int kb = 0; kb < nblk; ++kb) {
                    const bf16x8 k0 = lds_rd128(ka + kb * 1024);
                    const u32x4 k1 = u32x4{lds_rd32(ra + kb * 128), 0u, 0u, 0u};
#pragma unroll
                    for (int t = 0; t < 2; ++t) {
                        f32x16 sc = MFMA32(k0, BC8(s.q0[t]), zero); sc = MFMA32(BC8(k1), BC8(qz[t]), sc);
#pragma unroll
                        for (int r = 0; r < 16; ++r) mx[t] = __builtin_fmaxf(mx[t], sc[r]);
                    }
                }
            }
            if (attempt) { WAIT_BAR0(); issue(0, 0); WAIT_BAR0(); }
        }
#pragma unroll
        for (int t = 0; t < 2; ++t) {
            auto rr = __builtin_amdgcn_permlane32_swap(__float_as_uint(mx[t]), __float_as_uint(mx[t]), false, false);
            const float m = __builtin_fmaxf(__uint_as_float(rr[0]), __uint_as_float(rr[1]));
            const unsigned nb = __float_as_uint(-m), hi = nb & 0xffff0000u;
            const float r1 = -m - __uint_as_float(hi); const unsigned mid = __float_as_uint(r1) & 0xffff0000u;
            const float r2 = r1 - __uint_as_float(mid); const unsigned lo = __float_as_uint(r2) & 0xffff0000u;
            s.q1[t][1] = h ? 0u : ((hi >> 16) | mid); s.q1[t][2] = h ? 0u : (lo >> 16);
        }
        STAMPV(c.P, 3);
        s.O[0] = zero; s.O[1] = zero;
        s.k0 = lds_rd128(ka); s.k1 = u32x4{lds_rd32(ra), 0x3f803f80u, 0x00003f80u, 0u};
        s.v0 = lds_rd128(va); s.v1 = s.v0; s.ph[1] = u32x4{0u, 0u, 0u, 0u};
        s.S[0] = MFMA32(s.k0, BC8(s.q0[0]), zero); s.S[0] = MFMA32(BC8(s.k1), BC8(s.q1[0]), s.S[0]);
        unsigned cur = 0, nxt = AB_SLOT;
        auto nohook = [] {};
        for (int ch = 0; ch < 8; ++ch) {
            const unsigned cK = ka + cur, cR = ra + cur, cV = va + cur, nK = ka + nxt, nR = ra + nxt, nV = va + nxt;
#pragma unroll
            for (int j = 0; j < 7; ++j) {
                attn_slot<0>(s, cK + (j + 1) * 1024, cR + (j + 1) * 128, cV + j * 1280 + 640, zero, nohook);
                attn_slot<1>(s, 0, 0, cV + (j + 1) * 1280, zero, [&] { if (j < 5) piece(j, ch + 1, nxt); });
            }
            asm volatile("s_waitcnt vmcnt(0)\n\ts_barrier" ::: "memory");
            SB();
            attn_slot<0>(s, nK, nR, cV + 7 * 1280 + 640, zero, nohook);
            attn_slot<1>(s, 0, 0, nV, zero, [&] { asm volatile("s_barrier" ::: "memory"); });
            const unsigned t_ = cur; cur = nxt; nxt = t_;
        }
        s.O[1] = MFMA32(s.v1, BC8(s.ph[1]), s.O[1]);
        if (attempt == 1) break;
        bool bad = false;
#pragma unroll
        for (int t = 0; t < 2; ++t) {
            float am = 0.f; bool b = false;
#pragma unroll
            for (int r = 0; r < 11; ++r) { am = __builtin_fmaxf(am, __builtin_fabsf(s.O[t][r])); b = b || (s.O[t][r] != s.O[t][r]); }
            bad = bad || b || !(am < 1.0e30f);
        }
        if (tid == 0) *flag = 0u;
        WAIT_BAR0();
        if (__any(bad)) *flag = 1u;
        WAIT_BAR0();
        if (rfl(*flag) == 0u) break;
    }
    STAMPV(c.P, 4);
#pragma unroll
    for (int t = 0; t < 2; ++t) {
        auto rr = __builtin_amdgcn_permlane32_swap(__float_as_uint(s.O[t][10]), __float_as_uint(s.O[t][10]), false, false);
        const float inv = 1.0f / __uint_as_float(rr[0]);
        const int tok = tok0 + 32 * t + r32, tb = tok >> 4;
        const unsigned base = WS_HC + (unsigned)(tb * 90 * 1024);
        const u32x4 m = {pk2(s.O[t][0] * inv, s.O[t][1] * inv), pk2(s.O[t][2] * inv, s.O[t][3] * inv), pk2(s.O[t][4] * inv, s.O[t][5] * inv), pk2(s.O[t][6] * inv, s.O[t][7] * inv)};
        st128(c, base + (unsigned)((head >> 1) * 1024 + ((tok & 15) + 16 * (2 * (head & 1) + h)) * 16), m);
        if (h == 0) st32(c, base + (unsigned)((16 + (head >> 4)) * 1024 + ((tok & 15) + 16 * ((head & 15) >> 2)) * 16 + (head & 3) * 4), pk2(s.O[t][8] * inv, s.O[t][9] * inv));
    }
    publish(c, FL_O + qb * 32 + head);
}

constexpr int SD_N = 8, SD_SLOT = 9216;
struct SpdRegs { bf16x8 zf[9]; u32x4 t[4][2]; unsigned vZ[3], vz, vT; };
template <int U>
DI void spd_sub(f32x4 (&acc)[9][2], SpdRegs& r, u32x4 srdT, u32x4 srdZ, unsigned sWZ, unsigned w, unsigned sTa, unsigned sTb, unsigned sZ) {
    constexpr int NX = (U + 1) & (SD_N - 1), PF = (U + SD_N - 1) & (SD_N - 1), TU = U & 3, TP = (U + 3) & 3;
    asm volatile("s_waitcnt vmcnt(10)\n\ts_barrier" : "+v"(r.t[TU][0]), "+v"(r.t[TU][1])::"memory");
    SB();
#pragma unroll
    for (int n = 0; n < 9; ++n) {
        if (n == 0) LGKM(4);
        if (n == 5) LGKM(5);
        if (n == 4 || n == 6) set_m0(sWZ, PF * SD_SLOT + (n == 4 ? 0 : 4096));
        mfma16v(acc[n][0], r.zf[n], BC8(r.t[TU][0]));
        if (n == 0) ld_frag<0>(r.t[TP][0], srdT, r.vT, sTa);
        if (n == 2) ld_frag<0>(r.t[TP][1], srdT, r.vT, sTb);
        if (n == 4) dma_go_c(srdZ, r.vZ[0], sZ);
        if (n == 6) dma_go_c(srdZ, r.vZ[1], sZ);
        mfma16v(acc[n][1], r.zf[n], BC8(r.t[TU][1]));
        r.zf[n] = lds_rd128(r.vz + NX * SD_SLOT + n * 1024);
        SB();
    }
    if (w == 0) { set_m0(sWZ, PF * SD_SLOT + 8192); asm volatile("s_nop 0"); dma_go_c(srdZ, r.vZ[2], sZ); }
    SB();
}

DI void spatial_tile(const Ctx& c, int panel, int jt, int u0, const u32x4 (&z1p)[9]) {
    const int tid = threadIdx.x, l = tid & 63; const unsigned w = rfl(tid >> 6); const unsigned lds0 = c.lds0;
    STAMPV(c.P, 4);
    const u32x4 srdT = make_srd(c.P.ws + WS_WP + (size_t)(panel * 8 + 2 * w) * 65536, 2 * 65536), srdZ = make_srd(c.P.ws + WS_Z2 + (size_t)jt * 9 * 65536, 9 * 65536);
    SpdRegs r;
#pragma unroll
    for (int i = 0; i < 3; ++i) { r.vZ[i] = (w + 4 * i) * 65536 + l * 16; OPAQUE(r.vZ[i]); }
    r.vz = lds0 + l * 16; OPAQUE(r.vz);
    r.vT = l * 16; OPAQUE(r.vT);
    const unsigned sWZ = lds0 + w * 1024;
#define SO(v) ((v) < 64 ? (unsigned)(((v) + u0) & 63) * 1024u : OOB)
#define SO2(v) ((v) < 64 ? (unsigned)(((v) + u0) & 63) * 1024u + 65536u : OOB)
    f32x4 acc[9][2];
#pragma unroll
    for (int n = 0; n < 9; ++n) { acc[n][0] = f32x4{0, 0, 0, 0}; acc[n][1] = f32x4{0, 0, 0, 0}; }
    PIN18(acc, "s_nop 1");
#pragma unroll
    for (int u = 0; u < SD_N - 1; ++u) {
        if (u >= 4) { ld_frag<0>(r.t[u - 4][0], srdT, r.vT, SO(u - 4)); ld_frag<0>(r.t[u - 4][1], srdT, r.vT, SO2(u - 4)); }
#pragma unroll
        for (int i = 0; i < 2; ++i) { set_m0(sWZ, u * SD_SLOT + i * 4096); asm volatile("s_nop 0"); dma_go_c(srdZ, r.vZ[i], SO(u)); }
        if (w == 0) { set_m0(sWZ, u * SD_SLOT + 8192); asm volatile("s_nop 0"); dma_go_c(srdZ, r.vZ[2], SO(u)); }
    }
    asm volatile("s_waitcnt vmcnt(18)\n\ts_barrier" ::: "memory");
#pragma unroll
    for (int n = 0; n < 9; ++n) r.zf[n] = lds_rd128(r.vz + n * 1024);
    LGKM(0);
    for (int u = 0; u < 64; u += 8) {
        spd_sub<0>(acc, r, srdT, srdZ, sWZ, w, SO(u + 3), SO2(u + 3), SO(u + 7));
        spd_sub<1>(acc, r, srdT, srdZ, sWZ, w, SO(u + 4), SO2(u + 4), SO(u + 8));
        spd_sub<2>(acc, r, srdT, srdZ, sWZ, w, SO(u + 5), SO2(u + 5), SO(u + 9));
        spd_sub<3>(acc, r, srdT, srdZ, sWZ, w, SO(u + 6), SO2(u + 6), SO(u + 10));
        spd_sub<4>(acc, r, srdT, srdZ, sWZ, w, SO(u + 7), SO2(u + 7), SO(u + 11));
        spd_sub<5>(acc, r, srdT, srdZ, sWZ, w, SO(u + 8), SO2(u + 8), SO(u + 12));
        spd_sub<6>(acc, r, srdT, srdZ, sWZ, w, SO(u + 9), SO2(u + 9), SO(u + 13));
        spd_sub<7>(acc, r, srdT, srdZ, sWZ, w, SO(u + 10), SO2(u + 10), SO(u + 14));
    }
    asm volatile("s_waitcnt vmcnt(0) lgkmcnt(0)" : "+v"(r.t[0][0]), "+v"(r.t[0][1]), "+v"(r.t[1][0]), "+v"(r.t[1][1]), "+v"(r.t[2][0]), "+v"(r.t[2][1]), "+v"(r.t[3][0]), "+v"(r.t[3][1])::"memory");
    PIN18(acc, "s_nop 7\n\ts_nop 1");
    STAMPV(c.P, 5);
    float bs[2];
#pragma unroll
    for (int tb = 0; tb < 2; ++tb) bs[tb] = bf2f(c.P.bsp[panel * 128 + 32 * w + 16 * tb + (l & 15)]);
    unsigned hp[9][2][2];
#pragma unroll
    for (int n = 0; n < 9; ++n)
#pragma unroll
        for (int tb = 0; tb < 2; ++tb) {
            hp[n][tb][0] = pk2(bflo(z1p[n][2 * tb]) * (acc[n][tb][0] + bs[tb]), bfhi(z1p[n][2 * tb]) * (acc[n][tb][1] + bs[tb]));
            hp[n][tb][1] = pk2(bflo(z1p[n][2 * tb + 1]) * (acc[n][tb][2] + bs[tb]), bfhi(z1p[n][2 * tb + 1]) * (acc[n][tb][3] + bs[tb]));
        }
    const int fb0 = jt * 9;
#pragma unroll
    for (int tb = 0; tb < 2; ++tb) {
        const unsigned base = WS_HC + (unsigned)(((panel * 8 + 2 * w + tb) * 90 + 18) * 1024 + l * 16);
        if ((jt & 1) == 0) {
#pragma unroll
            for (int n = 0; n < 8; n += 2) st128(c, base + (unsigned)(((fb0 + n) >> 1) * 1024), u32x4{hp[n][tb][0], hp[n][tb][1], hp[n + 1][tb][0], hp[n + 1][tb][1]});
            st64(c, base + (unsigned)(((fb0 + 8) >> 1) * 1024), u32x2{hp[8][tb][0], hp[8][tb][1]});
        } else {
            st64(c, base + (unsigned)((fb0 >> 1) * 1024 + 8), u32x2{hp[0][tb][0], hp[0][tb][1]});
#pragma unroll
            for (int n = 1; n < 9; n += 2) st128(c, base + (unsigned)(((fb0 + n) >> 1) * 1024), u32x4{hp[n][tb][0], hp[n][tb][1], hp[n + 1][tb][0], hp[n + 1][tb][1]});
        }
    }
    STAMPV(c.P, 15);
}

constexpr int FN_STAGE = 16 * 1024;
struct FnRegs { bf16x8 wf[5]; u32x4 h[4][3]; unsigned vW[4], vrd, vl; };
template <int U, int VM>
DI void fn_stage(f32x4 (&acc)[5], FnRegs& r, u32x4 srdW, u32x4 srdH, unsigned sWW, unsigned sNext) {
    constexpr int PF = (U + 3) & 3;
    asm volatile("s_waitcnt vmcnt(%3)\n\ts_barrier" : "+v"(r.h[U][0]), "+v"(r.h[U][1]), "+v"(r.h[U][2]) : "n"(VM) : "memory");
    SB();
#pragma unroll
    for (int n = 0; n < 5; ++n) r.wf[n] = lds_rd128(r.vrd + U * FN_STAGE + (n * 3) * 1024);
    ld_frag<0>(r.h[PF][0], srdH, r.vl, sNext); ld_frag<1024>(r.h[PF][1], srdH, r.vl, sNext); ld_frag<2048>(r.h[PF][2], srdH, r.vl, sNext);
    SB();
#pragma unroll
    for (int kk = 0; kk < 3; ++kk) {
#pragma unroll
        for (int n = 0; n < 5; ++n) {
            const int i = kk * 5 + n;
            const bool dm = (i % 4 == 1);
            if (dm) set_m0(sWW, PF * FN_STAGE + (i / 4) * 4096);
            mfma16v(acc[n], r.wf[n], BC8(r.h[U][kk]));
            if (dm) dma_go_c(srdW, r.vW[i / 4], sNext);
            if (kk + 1 < 3) r.wf[n] = lds_rd128(r.vrd + U * FN_STAGE + (n * 3 + kk + 1) * 1024);
            SB();
        }
    }
}
DI void fn_pre_w(FnRegs& r, u32x4 srdW, unsigned sWW, int kb0, int rot) {
#pragma unroll
    for (int s = 0; s < 3; ++s)
#pragma unroll
        for (int i = 0; i < 4; ++i) { set_m0(sWW, s * FN_STAGE + i * 4096); asm volatile("s_nop 0"); dma_go_c(srdW, r.vW[i], (unsigned)kb0 * 1024u + (unsigned)(s + rot) * 3072u); }
}
template <int NST>
DI void fn_range(f32x4 (&acc)[5], FnRegs& r, u32x4 srdW, u32x4 srdH, unsigned sWW, int kb0, int rot) {
    const unsigned o0 = (unsigned)kb0 * 1024u;
#define FO(s) ((s) < NST ? o0 + (unsigned)((s) + rot >= NST ? (s) + rot - NST : (s) + rot) * 3072u : OOB)
#pragma unroll
    for (int s = 0; s < 3; ++s) { ld_frag<0>(r.h[s][0], srdH, r.vl, FO(s)); ld_frag<1024>(r.h[s][1], srdH, r.vl, FO(s)); ld_frag<2048>(r.h[s][2], srdH, r.vl, FO(s)); }
    asm volatile("s_waitcnt vmcnt(6)" : "+v"(r.h[0][0]), "+v"(r.h[0][1]), "+v"(r.h[0][2])::"memory");
    fn_stage<0, 63>(acc, r, srdW, srdH, sWW, FO(3));
    asm volatile("s_waitcnt vmcnt(10)" : "+v"(r.h[1][0]), "+v"(r.h[1][1]), "+v"(r.h[1][2])::"memory");
    fn_stage<1, 63>(acc, r, srdW, srdH, sWW, FO(4));
    fn_stage<2, 14>(acc, r, srdW, srdH, sWW, FO(5));
    fn_stage<3, 14>(acc, r, srdW, srdH, sWW, FO(6));
    for (int s = 4; s + 4 <= NST; s += 4) {
        fn_stage<0, 14>(acc, r, srdW, srdH, sWW, FO(s + 3)); fn_stage<1, 14>(acc, r, srdW, srdH, sWW, FO(s + 4));
        fn_stage<2, 14>(acc, r, srdW, srdH, sWW, FO(s + 5)); fn_stage<3, 14>(acc, r, srdW, srdH, sWW, FO(s + 6));
    }
    if (NST % 4 == 2) { fn_stage<0, 14>(acc, r, srdW, srdH, sWW, OOB); fn_stage<1, 14>(acc, r, srdW, srdH, sWW, OOB); }
    asm volatile("s_waitcnt vmcnt(0) lgkmcnt(0)\n\ts_barrier" : "+v"(r.h[0][0]), "+v"(r.h[0][1]), "+v"(r.h[0][2]), "+v"(r.h[1][0]), "+v"(r.h[1][1]), "+v"(r.h[1][2]),
                 "+v"(r.h[2][0]), "+v"(r.h[2][1]), "+v"(r.h[2][2]), "+v"(r.h[3][0]), "+v"(r.h[3][1]), "+v"(r.h[3][2])::"memory");
    PIN5(acc, "s_nop 7\n\ts_nop 1");
}

DI void final_tile(const Ctx& c, int tt, int j, unsigned hflag, int rot) {
    const int tid = threadIdx.x, l = tid & 63; const unsigned w = rfl(tid >> 6); const int g = l >> 4;
    const int db0 = (9 * j) >> 1;
    const u32x4 srdW = make_srd(c.P.ws + WS_WC + (size_t)db0 * 90 * 1024, 5 * 90 * 1024);
    const u32x4 srdH = make_srd(c.P.ws + WS_HC + (size_t)(tt * 4 + w) * 90 * 1024, 90 * 1024);
    FnRegs r;
    r.vl = (unsigned)l * 16; OPAQUE(r.vl);
#pragma unroll
    for (int i = 0; i < 4; ++i) { const unsigned p = w + 4 * i, db = p / 3, kk = p - db * 3; r.vW[i] = p < 15 ? (db * 90 + kk) * 1024 + l * 16 : OOB; OPAQUE(r.vW[i]); }
    r.vrd = c.lds0 + l * 16; OPAQUE(r.vrd);
    const unsigned sWW = c.lds0 + w * 1024;
    asm volatile("s_waitcnt vmcnt(0) lgkmcnt(0)\n\ts_barrier" ::: "memory");
    raise_flag(c, hflag);
    fn_pre_w(r, srdW, sWW, 18, rot);
    wait_begin(c, tid < 16 ? FL_H + (tt >> 1) * 16 + tid : ~0u);
    asm volatile("s_waitcnt lgkmcnt(0)\n\ts_barrier" ::: "memory");
    STAMPV(c.P, 6);
    f32x4 accA[5], accB[5];
#pragma unroll
    for (int n = 0; n < 5; ++n) { accA[n] = f32x4{0, 0, 0, 0}; accB[n] = f32x4{0, 0, 0, 0}; }
    PIN5(accA, "s_nop 1"); PIN5(accB, "s_nop 1");
    fn_range<24>(accB, r, srdW, srdH, sWW, 18, rot);
    STAMPV(c.P, 7);
    fn_pre_w(r, srdW, sWW, 0, 0);
    if (tid < 64) poll_flags(c, tid < 32 ? FL_O + (tt >> 2) * 32 + tid : ~0u);
    if (tid < 64) ACQUIRE();
    wait_end();
    STAMPV(c.P, 8);
    fn_range<6>(accA, r, srdW, srdH, sWW, 0, 0);
    const int tok = tt * 64 + 16 * w + (l & 15);
    const float gt = __uint_as_float(__builtin_amdgcn_raw_buffer_load_b32(c.ws, (int)(WS_GT + tok * 4), 0, 16));
#pragma unroll
    for (int n = 0; n < 5; ++n) {
        const int d = (db0 + n) * 16 + 4 * g;
        if (d >= 72 * j && d < 72 * j + 72) {
            float v[4];
#pragma unroll
            for (int rr = 0; rr < 4; ++rr) v[rr] = gt * accA[n][rr] + (1.0f - gt) * accB[n][rr];
            *(u32x2*)(c.P.out + tok * 576 + d) = u32x2{pk2(v[0], v[1]), pk2(v[2], v[3])};
        }
    }
}

constexpr int MEGA_LDS = 73728 + 1024;
#define FROT 1
#define A_DELAY 0
#define SP_PRIO 2
#define A1_PRIO 1
#define B1_PRIO 1
#define SROT 0
#define SROT2 0
__global__ __launch_bounds__(256, 2) void mega_kernel(Params P) {
    extern __shared__ __attribute__((aligned(16))) char smem[];
    const int b = blockIdx.x, tid = threadIdx.x;
    STAMPV(P, 0);
    const u64 ep = __hip_atomic_load((gu64*)(P.ws + WS_EP), __ATOMIC_RELAXED, __HIP_MEMORY_SCOPE_AGENT);
    const u64 tag = ep + 1ull;
    const Ctx c{P, __builtin_amdgcn_make_buffer_rsrc(P.ws, (short)0, (int)WS_END, 0x00020000), tag, (unsigned)(uintptr_t)smem, smem};
    if (b < 256) {
        __builtin_amdgcn_s_setprio(A1_PRIO);
        const int x = b & 7, i = b >> 3, panel = (x >> 2) * 8 + (i & 7), jt = (x & 3) * 4 + (i >> 3);
        u32x4 z1p[9];
        tile_z(c, panel, jt, ROT ? ((i & 7) + 2 * (i >> 3)) % 9 : 0, z1p);
        __builtin_amdgcn_s_setprio(SP_PRIO);
        spatial_tile(c, panel, jt, ((i & 7) * SROT + (i >> 3) * SROT2) & 63, z1p);
        final_tile(c, 2 * panel + (jt >> 3), jt & 7, FL_H + panel * 16 + jt, FROT ? ((i & 7) * 2 + (i >> 3) * 1) % 21 : 0);
        STAMPV(P, 9);
        if (b == 0) {
            if (tid < 64) {
                for (unsigned spins = 0; spins < SPIN_CAP; ++spins) {
                    bool ok = true;
#pragma unroll
                    for (int k = 0; k < 4; ++k) {
                        ok = ok && __hip_atomic_load(flag_ptr(c, FL_H + 64 * k + tid), __ATOMIC_RELAXED, __HIP_MEMORY_SCOPE_AGENT) == tag;
                        ok = ok && __hip_atomic_load(flag_ptr(c, FL_O + 64 * k + tid), __ATOMIC_RELAXED, __HIP_MEMORY_SCOPE_AGENT) == tag;
                    }
                    if (__all(ok)) break;
                    __builtin_amdgcn_s_sleep(POLL_SLEEP);
                }
                if (tid == 0) __hip_atomic_store((gu64*)(P.ws + WS_EP), tag, __ATOMIC_RELAXED, __HIP_MEMORY_SCOPE_AGENT);
            }
        }
    } else {
        const int a = b - 256, x = a & 7, idx = a >> 3;
        __builtin_amdgcn_s_setprio(B1_PRIO);
        if (idx < 24) {
            const int hq = x & 3, ty = idx % 3, panel = (x >> 2) * 8 + idx / 3;
            const int s0 = ROT ? (idx / 3 + 3 * ty + 1) % 9 : 0;
            if (ty < 2) tile_qk(c, panel, ty, hq, s0); else tile_v(c, panel, hq, s0);
            const unsigned qf = FL_QKV + (ty * 4 + hq) * 16 + panel;
            STAMPV(P, 6);
            publish(c, qf);
            STAMPV(P, 7);
            { const int k = idx * 8 + x; if (k < 90) prep_wcat(c, k, ~0u); else if (k < 154) prep_gate(c, k - 90, ~0u); }

        } else {
            const int n = (idx - 24) * 8 + x;
            prep_wsp(c, n);
        }
        STAMPV(P, 1);
        __builtin_amdgcn_s_setprio(0);
        attn_unit(c, x * 4 + (idx & 3), idx >> 2);
        STAMPV(P, 5);
    }
}

extern "C" void kernel_launch(void* const* d_in, const int* in_sizes, int n_in, void* d_out, int out_size, void* d_ws, size_t ws_size, hipStream_t stream) {
    Params P;
    P.x = (const u16*)d_in[0]; P.wqkv = (const u16*)d_in[1]; P.wo = (const u16*)d_in[2]; P.win = (const u16*)d_in[4]; P.wsp = (const u16*)d_in[5];
    P.bsp = (const u16*)d_in[6]; P.wout = (const u16*)d_in[7]; P.gatew = (const u16*)d_in[8]; P.out = (u16*)d_out; P.ws = (char*)d_ws;
    hipFuncSetAttribute((const void*)mega_kernel, hipFuncAttributeMaxDynamicSharedMemorySize, MEGA_LDS);
    mega_kernel<<<512, 256, MEGA_LDS, stream>>>(P);
}
```

```cpp
#include <hip/hip_runtime.h>
#include <stdint.h>

typedef __bf16 bf16x8 __attribute__((ext_vector_type(8)));
typedef __bf16 bf16x2 __attribute__((ext_vector_type(2)));
typedef short s16x4 __attribute__((ext_vector_type(4)));
typedef float f32x2 __attribute__((ext_vector_type(2)));
typedef float f32x4 __attribute__((ext_vector_type(4)));
typedef float f32x16 __attribute__((ext_vector_type(16)));
typedef unsigned u32x2 __attribute__((ext_vector_type(2)));
typedef unsigned u32x4 __attribute__((ext_vector_type(4)));
typedef unsigned short u16;
typedef unsigned long long u64;
typedef __attribute__((address_space(1))) unsigned long long gu64;
#define LAS __attribute__((address_space(3)))
#define DI __device__ __forceinline__

constexpr int S_ = 2048, D_ = 576, FH = 2304;
constexpr unsigned WS_EP = 0;
constexpr unsigned WS_FL = 256;
constexpr unsigned FL_ST = 0, FL_Z = 256, FL_H = 512, FL_QKV = 768, FL_WP = 960, FL_O = 1024, FL_WC = 1280, FL_N = FL_WC + 154;
constexpr unsigned WS_GT = 16384;
constexpr unsigned WS_ST = WS_GT + 8192;
constexpr unsigned WS_KM = WS_ST + 16 * 16 * 1024;
constexpr unsigned WS_KR = WS_KM + 32 * 64 * 1024;
constexpr unsigned WS_VT = WS_KR + 32 * 2048 * 4;
constexpr unsigned WS_QM = WS_VT + 32 * 128 * 640;
constexpr unsigned WS_QR = WS_QM + 32 * 64 * 1024;
constexpr unsigned WS_Z2 = WS_QR + 32 * 2048 * 4;
constexpr unsigned WS_WP = WS_Z2 + 144 * 64 * 1024;
constexpr unsigned WS_HC = WS_WP + 128 * 64 * 1024;
constexpr unsigned WS_WC = WS_HC + 128 * 90 * 1024;
constexpr unsigned WS_END = WS_WC + 36 * 90 * 1024;
constexpr size_t WS_DBG = 200u << 20;

#define ROT 1
#define PREP_NT 1
#define SC1_LOADS 1
#define PREP_SLEEP 8
#define FILL_ON_QKV 1
#define QKV_PRESLEEP 2
#define WARM_Z1 0
#define PREP_DELAY 0
struct Params {
    const u16* x; const u16* wqkv; const u16* wo; const u16* win; const u16* wsp; const u16* bsp; const u16* wout; const u16* gatew;
    u16* out; char* ws;
};

#define STAMPV(P_, idx) do {} while (0)

DI float bf2f(u16 v) { return __uint_as_float((unsigned)v << 16); }
DI float bflo(unsigned v) { return __uint_as_float(v << 16); }
DI float bfhi(unsigned v) { return __uint_as_float(v & 0xffff0000u); }
DI unsigned pk2(float lo, float hi) { f32x2 v = {lo, hi}; bf16x2 b = __builtin_convertvector(v, bf16x2); return __builtin_bit_cast(unsigned, b); }
DI unsigned rfl(unsigned v) { return __builtin_amdgcn_readfirstlane(v); }
DI u32x4 make_srd(const void* p, unsigned bytes) {
    uint64_t a = (uint64_t)p;
    u32x4 r; r[0] = rfl((unsigned)a); r[1] = rfl((unsigned)(a >> 32) & 0xffffu); r[2] = rfl(bytes); r[3] = 0x00020000u; return r;
}
#define SB() __builtin_amdgcn_sched_barrier(0)
#define BAR() asm volatile("s_waitcnt lgkmcnt(0)\n\ts_barrier" ::: "memory")
#define WAIT_BAR0() asm volatile("s_waitcnt vmcnt(0) lgkmcnt(0)\n\ts_barrier" ::: "memory")
DI void set_m0(unsigned base, unsigned imm) { asm volatile("s_add_u32 m0, %0, %1" ::"s"(base), "n"(imm) : "scc"); }
DI void set_m0s(unsigned a, unsigned b) { asm volatile("s_add_u32 m0, %0, %1" ::"s"(a), "s"(b) : "scc"); }
DI void dma_go(u32x4 srd, unsigned voff, unsigned soff) { asm volatile("buffer_load_dwordx4 %0, %1, %2 offen lds" ::"v"(voff), "s"(srd), "s"(soff) : "memory"); }
DI void dma_go_c(u32x4 srd, unsigned voff, unsigned soff) { asm volatile("buffer_load_dwordx4 %0, %1, %2 offen sc1 lds" ::"v"(voff), "s"(srd), "s"(soff) : "memory"); }
#define LDSC " sc1"
#define ACQUIRE() do {} while (0)
DI void dma_now(u32x4 srd, unsigned voff, unsigned soff, unsigned ldsaddr) { asm volatile("s_mov_b32 m0, %0\n\ts_nop 0\n\tbuffer_load_dwordx4 %1, %2, %3 offen nt lds" ::"s"(ldsaddr), "v"(voff), "s"(srd), "s"(soff) : "memory"); }
template <int IMM> DI void ld_frag(u32x4& d, u32x4 srd, unsigned voff, unsigned soff) {
    asm volatile("buffer_load_dwordx4 %0, %1, %2, %3 offen offset:%4" LDSC : "=v"(d) : "v"(voff), "s"(srd), "s"(soff), "n"(IMM) : "memory");
}
DI bf16x8 lds_rd128(unsigned a) { return *(const LAS bf16x8*)(uintptr_t)a; }
DI unsigned lds_rd32(unsigned a) { return *(const LAS unsigned*)(uintptr_t)a; }
DI s16x4 lds_rdtr(unsigned a) { return __builtin_amdgcn_ds_read_tr16_b64_v4i16((LAS s16x4*)(uintptr_t)a); }
DI bf16x8 cat8(s16x4 a, s16x4 b) { typedef short s16x8 __attribute__((ext_vector_type(8))); s16x8 v = __builtin_shufflevector(a, b, 0, 1, 2, 3, 4, 5, 6, 7); return __builtin_bit_cast(bf16x8, v); }
DI void mfma16v(f32x4& c, bf16x8 a, bf16x8 b) { asm volatile("v_mfma_f32_16x16x32_bf16 %0, %1, %2, %0" : "+v"(c) : "v"(a), "v"(b)); }
#define PIN18(acc, txt) asm volatile(txt : "+v"(acc[0][0]), "+v"(acc[0][1]), "+v"(acc[1][0]), "+v"(acc[1][1]), "+v"(acc[2][0]), "+v"(acc[2][1]), "+v"(acc[3][0]), "+v"(acc[3][1]), "+v"(acc[4][0]), "+v"(acc[4][1]), \
    "+v"(acc[5][0]), "+v"(acc[5][1]), "+v"(acc[6][0]), "+v"(acc[6][1]), "+v"(acc[7][0]), "+v"(acc[7][1]), "+v"(acc[8][0]), "+v"(acc[8][1]))
#define PIN5(acc, txt) asm volatile(txt : "+v"(acc[0]), "+v"(acc[1]), "+v"(acc[2]), "+v"(acc[3]), "+v"(acc[4]))
#define LGKM(n) __builtin_amdgcn_s_waitcnt(0xC07F | ((n) << 8))
#define OPAQUE(x) asm volatile("" : "+v"(x))
constexpr unsigned OOB = 0x40000000u;
#define EX2(x) __builtin_amdgcn_exp2f(x)
#define BC8(x) __builtin_bit_cast(bf16x8, x)
#define MFMA32(a, b, c) __builtin_amdgcn_mfma_f32_32x32x16_bf16(a, b, c, 0, 0, 0)

DI float gelu_t(float x) {
    const float c0 = -2.0f * 0.7978845608028654f * 1.4426950408889634f, c1 = c0 * 0.044715f;
    const float e = __builtin_amdgcn_exp2f(x * __builtin_fmaf(x * x, c1, c0));
    return x * __builtin_amdgcn_rcpf(1.0f + e);
}
template <int N> DI float dpp_ror_add(float v) { return v + __builtin_bit_cast(float, __builtin_amdgcn_update_dpp(0, __builtin_bit_cast(int, v), 0x120 + N, 0xf, 0xf, false)); }
DI float row16_sum(float v) { v = dpp_ror_add<8>(v); v = dpp_ror_add<4>(v); v = dpp_ror_add<2>(v); v = dpp_ror_add<1>(v); return v; }

struct Ctx { const Params& P; __amdgpu_buffer_rsrc_t ws; u64 tag; unsigned lds0; char* smem; };
#define ST_AUX 16
DI void st128(const Ctx& c, unsigned off, u32x4 v) { __builtin_amdgcn_raw_buffer_store_b128(v, c.ws, (int)off, 0, ST_AUX); }
DI void st64(const Ctx& c, unsigned off, u32x2 v) { __builtin_amdgcn_raw_buffer_store_b64(v, c.ws, (int)off, 0, 16); }
DI void st32(const Ctx& c, unsigned off, unsigned v) { __builtin_amdgcn_raw_buffer_store_b32(v, c.ws, (int)off, 0, 16); }
DI gu64* flag_ptr(const Ctx& c, unsigned idx) { return (gu64*)(c.P.ws + WS_FL) + idx; }
DI void publish(const Ctx& c, unsigned idx) {
    asm volatile("s_waitcnt vmcnt(0) lgkmcnt(0)\n\ts_barrier" ::: "memory");
    if (threadIdx.x == 0) __hip_atomic_store(flag_ptr(c, idx), c.tag, __ATOMIC_RELAXED, __HIP_MEMORY_SCOPE_AGENT);
}
#define POLL_SLEEP 16
#define SPIN_CAP (1u << 16)
DI void wait_flags(const Ctx& c, unsigned idx) {
    if (threadIdx.x < 64) {
        gu64* p = flag_ptr(c, idx == ~0u ? 0u : idx);
        for (unsigned spins = 0; spins < SPIN_CAP; ++spins) {
            const u64 v = idx == ~0u ? c.tag : __hip_atomic_load(p, __ATOMIC_RELAXED, __HIP_MEMORY_SCOPE_AGENT);
            if (__all(v == c.tag)) break;
            __builtin_amdgcn_s_sleep(POLL_SLEEP);
        }
        ACQUIRE();
        asm volatile("s_waitcnt vmcnt(0)" ::: "memory");
    }
    asm volatile("s_waitcnt lgkmcnt(0)\n\ts_barrier" ::: "memory");
}

DI void poll_flags(const Ctx& c, unsigned idx) {
    gu64* p = flag_ptr(c, idx == ~0u ? 0u : idx);
    for (unsigned spins = 0; spins < SPIN_CAP; ++spins) {
        const u64 v = idx == ~0u ? c.tag : __hip_atomic_load(p, __ATOMIC_RELAXED, __HIP_MEMORY_SCOPE_AGENT);
        if (__all(v == c.tag)) break;
        __builtin_amdgcn_s_sleep(POLL_SLEEP);
    }
}
DI void raise_flag(const Ctx& c, unsigned idx) { if (threadIdx.x == 0) __hip_atomic_store(flag_ptr(c, idx), c.tag, __ATOMIC_RELAXED, __HIP_MEMORY_SCOPE_AGENT); }
DI void wait_begin(const Ctx& c, unsigned idx) { if (threadIdx.x < 64) { poll_flags(c, idx); ACQUIRE(); } }
DI void wait_end() { asm volatile("s_waitcnt vmcnt(0) lgkmcnt(0)\n\ts_barrier" ::: "memory"); }

constexpr int G1_XS = 16384, G1_WS = 9216, G1_WOFF = 2 * G1_XS, G1_LDS = G1_WOFF + 4 * G1_WS;
struct G1Regs { bf16x8 wf[9], xf[2][2]; unsigned vX[4], vW[3], vw0, vw1, vx[2]; };
template <bool TRANS> DI void g1_mm(f32x4& c, bf16x8 wf, bf16x8 xf) { if (TRANS) mfma16v(c, wf, xf); else mfma16v(c, xf, wf); }
DI bf16x8 g1_rdw(unsigned lo, unsigned hi, int off) { return cat8(lds_rdtr(lo + off), lds_rdtr(hi + off)); }

template <bool TRANS, int J>
DI void g1_sub(f32x4 (&acc)[9][2], G1Regs& r, u32x4 srdX, u32x4 srdW, unsigned sWX, unsigned sWW, unsigned w, unsigned sX, unsigned sW) {
    constexpr int N1 = (J + 1) & 3, CUR = J & 1, NXT = CUR ^ 1, PW = (J + 3) & 3;
    constexpr int XS_RD = N1 >> 1, XS_WR = J >> 1;
    if (J & 1) asm volatile("s_waitcnt vmcnt(2) lgkmcnt(8)\n\ts_barrier" ::: "memory");
    else asm volatile("s_waitcnt vmcnt(6) lgkmcnt(8)\n\ts_barrier" ::: "memory");
    LGKM(8);
    SB();
#pragma unroll
    for (int n = 0; n < 9; ++n) {
        if (n == 5) LGKM(12);
        const bool dw = (n == 1 || n == 3), dx = (J & 1) && n >= 4 && n < 8;
        if (dw) set_m0(sWW, PW * G1_WS + (n == 1 ? 0 : 4096));
        if (dx) set_m0(sWX, XS_WR * G1_XS + (n - 4) * 1024);
        g1_mm<TRANS>(acc[n][0], r.wf[n], r.xf[CUR][0]);
        if (dw) dma_go(srdW, r.vW[n == 1 ? 0 : 1], sW);
        if (dx) dma_go(srdX, r.vX[n - 4], sX);
        g1_mm<TRANS>(acc[n][1], r.wf[n], r.xf[CUR][1]);
        if (n < 2) r.xf[NXT][n] = lds_rd128(r.vx[N1 & 1] + XS_RD * G1_XS + n * 2048);
        r.wf[n] = g1_rdw(r.vw0, r.vw1, N1 * G1_WS + n * 32);
        SB();
    }
    if (w == 0) { set_m0(sWW, PW * G1_WS + 8192); asm volatile("s_nop 0"); dma_go(srdW, r.vW[2], sW); }
    SB();
}

template <bool TRANS>
DI void gemm1_main(const Ctx& c, const u16* x, const u16* W, int ldw, int row0, int col0, unsigned lds0, f32x4 (&acc)[9][2], int s0, unsigned deferred_flag = ~0u) {
    const int tid = threadIdx.x, l = tid & 63; const unsigned w = rfl(tid >> 6);
    const u32x4 srdX = make_srd(x, S_ * D_ * 2), srdW = make_srd(W, (unsigned)(D_ * ldw * 2));
    const int g = l >> 4, q = (l & 15) >> 2, p = l & 3;
    G1Regs r;
#pragma unroll
    for (int i = 0; i < 4; ++i) { r.vX[i] = (unsigned)((row0 + 32 * w + 8 * i + (l >> 3)) * (D_ * 2) + (((l & 7) ^ (l >> 3)) << 4)); OPAQUE(r.vX[i]); }
#pragma unroll
    for (int i = 0; i < 3; ++i) { const int pc = (int)w + 4 * i, idx = 64 * pc + l, rho = idx / 18, cc = idx - rho * 18, k = rho ^ (((rho >> 3) & 1) << 2);
        r.vW[i] = pc < 9 ? (unsigned)(k * ldw * 2 + (col0 + 8 * cc) * 2) : OOB; OPAQUE(r.vW[i]); }
    r.vw0 = lds0 + (unsigned)(G1_WOFF + (8 * g + (q ^ (4 * (g & 1)))) * 288 + p * 8); r.vw1 = lds0 + (unsigned)(G1_WOFF + (8 * g + ((q + 4) ^ (4 * (g & 1)))) * 288 + p * 8);
    r.vx[0] = lds0 + (unsigned)((32 * w + (l & 15)) * 128) + ((g ^ (l & 7)) << 4); r.vx[1] = lds0 + (unsigned)((32 * w + (l & 15)) * 128) + (((4 + g) ^ (l & 7)) << 4);
    OPAQUE(r.vw0); OPAQUE(r.vw1); OPAQUE(r.vx[0]); OPAQUE(r.vx[1]);
    const unsigned sWX = lds0 + w * 4096, sWW = lds0 + G1_WOFF + w * 1024, kst = (unsigned)(32 * ldw * 2);
    auto xs = [&](int slot, unsigned so) {
#pragma unroll
        for (int i = 0; i < 4; ++i) { set_m0(sWX, slot * G1_XS + i * 1024); asm volatile("s_nop 0"); dma_go(srdX, r.vX[i], so); } };
    auto wsl = [&](int slot, unsigned so) {
#pragma unroll
        for (int i = 0; i < 2; ++i) { set_m0(sWW, slot * G1_WS + i * 4096); asm volatile("s_nop 0"); dma_go(srdW, r.vW[i], so); }
        if (w == 0) { set_m0(sWW, slot * G1_WS + 8192); asm volatile("s_nop 0"); dma_go(srdW, r.vW[2], so); } };
    auto rot9 = [&](int s) { const int t = s + s0; return (unsigned)(t >= 9 ? t - 9 : t); };
#define GW(v) ((v) < 18 ? (rot9((v) >> 1) * 2 + ((v) & 1)) * kst : OOB)
#define GX(s) ((s) < 9 ? rot9(s) * 128u : OOB)
    xs(0, GX(0)); wsl(0, GW(0)); wsl(1, GW(1)); wsl(2, GW(2)); xs(1, GX(1));
#pragma unroll
    for (int n = 0; n < 9; ++n) { acc[n][0] = f32x4{0, 0, 0, 0}; acc[n][1] = f32x4{0, 0, 0, 0}; }
    PIN18(acc, "s_nop 1");
    asm volatile("s_waitcnt vmcnt(8)\n\ts_barrier" ::: "memory");
    if (deferred_flag != ~0u) { raise_flag(c, deferred_flag); STAMPV(c.P, 12); } else STAMPV(c.P, 10);
#pragma unroll
    for (int tb = 0; tb < 2; ++tb) r.xf[0][tb] = lds_rd128(r.vx[0] + tb * 2048);
#pragma unroll
    for (int n = 0; n < 9; ++n) r.wf[n] = g1_rdw(r.vw0, r.vw1, n * 32);
    LGKM(0);
    for (int u = 0; u < 16; u += 4) {
        g1_sub<TRANS, 0>(acc, r, srdX, srdW, sWX, sWW, w, 0, GW(u + 3));
        g1_sub<TRANS, 1>(acc, r, srdX, srdW, sWX, sWW, w, GX(u / 2 + 2), GW(u + 4));
        g1_sub<TRANS, 2>(acc, r, srdX, srdW, sWX, sWW, w, 0, GW(u + 5));
        g1_sub<TRANS, 3>(acc, r, srdX, srdW, sWX, sWW, w, GX(u / 2 + 3), GW(u + 6));
    }
    g1_sub<TRANS, 0>(acc, r, srdX, srdW, sWX, sWW, w, 0, OOB);
    g1_sub<TRANS, 1>(acc, r, srdX, srdW, sWX, sWW, w, OOB, OOB);
    asm volatile("s_waitcnt vmcnt(0) lgkmcnt(0)\n\ts_barrier" ::: "memory");
    PIN18(acc, "s_nop 7\n\ts_nop 1");
}

DI void tile_z(const Ctx& c, int panel, int jt, int s0, u32x4 (&z1p)[9]) {
    const int tid = threadIdx.x, l = tid & 63; const unsigned w = rfl(tid >> 6); const int g = l >> 4;
    f32x4 y[9][2];
    gemm1_main<false>(c, c.P.x, c.P.win, 4608, panel * 128, FH + jt * 144, c.lds0, y, s0);
    STAMPV(c.P, 1);
    {
        float s1[2][4], s2[2][4];
#pragma unroll
        for (int tb = 0; tb < 2; ++tb)
#pragma unroll
            for (int r = 0; r < 4; ++r) { s1[tb][r] = 0.f; s2[tb][r] = 0.f; }
#pragma unroll
        for (int n = 0; n < 9; ++n)
#pragma unroll
            for (int tb = 0; tb < 2; ++tb)
#pragma unroll
                for (int r = 0; r < 4; ++r) { const float v = gelu_t(y[n][tb][r]); y[n][tb][r] = v; s1[tb][r] += v; s2[tb][r] = __builtin_fmaf(v, v, s2[tb][r]); }
#pragma unroll
        for (int tb = 0; tb < 2; ++tb)
#pragma unroll
            for (int r = 0; r < 4; ++r) {
                const float a = row16_sum(s1[tb][r]), b = row16_sum(s2[tb][r]);
                if ((l & 15) == 0) st64(c, WS_ST + (unsigned)((panel * 16 + jt) * 1024 + (32 * w + 16 * tb + 4 * g + r) * 8), u32x2{__float_as_uint(a), __float_as_uint(b)});
            }
    }
    STAMPV(c.P, 2);
    f32x4 acc[9][2];
    gemm1_main<true>(c, c.P.x, c.P.win, 4608, panel * 128, jt * 144, c.lds0, acc, s0, FL_ST + panel * 16 + jt);
    STAMPV(c.P, 3);
    if (tid < 64) poll_flags(c, tid < 16 ? FL_ST + panel * 16 + tid : ~0u);
    BAR();
    STAMPV(c.P, 13);
    u32x2 pv[8];
#pragma unroll
    for (int i = 0; i < 8; ++i) pv[i] = __builtin_amdgcn_raw_buffer_load_b64(c.ws, (int)(WS_ST + (panel * 16 + 8 * (l >> 5) + i) * 1024 + (32 * w + (l & 31)) * 8), 0, 16);
    SB();
#pragma unroll
    for (int n = 0; n < 9; ++n)
#pragma unroll
        for (int tb = 0; tb < 2; ++tb) { z1p[n][2 * tb] = pk2(gelu_t(acc[n][tb][0]), gelu_t(acc[n][tb][1])); z1p[n][2 * tb + 1] = pk2(gelu_t(acc[n][tb][2]), gelu_t(acc[n][tb][3])); }
    asm volatile("" : "+v"(z1p[0]), "+v"(z1p[1]), "+v"(z1p[2]), "+v"(z1p[3]), "+v"(z1p[4]), "+v"(z1p[5]), "+v"(z1p[6]), "+v"(z1p[7]), "+v"(z1p[8]));
    SB();
    {
        float a = 0.f, b = 0.f;
#pragma unroll
        for (int i = 0; i < 8; ++i) { a += __uint_as_float(pv[i][0]); b += __uint_as_float(pv[i][1]); }
        auto ra = __builtin_amdgcn_permlane32_swap(__float_as_uint(a), __float_as_uint(a), false, false);
        auto rb = __builtin_amdgcn_permlane32_swap(__float_as_uint(b), __float_as_uint(b), false, false);
        a = __uint_as_float(ra[0]) + __uint_as_float(ra[1]); b = __uint_as_float(rb[0]) + __uint_as_float(rb[1]);
        const float m = a * (1.0f / FH), var = __builtin_fmaxf(b * (1.0f / FH) - m * m, 0.f);
        const float rs = 1.0f / __builtin_sqrtf(var + 1e-5f);
        if (l < 32) *(f32x2*)(c.smem + (w * 32 + l) * 8) = f32x2{m, rs};
        asm volatile("s_waitcnt lgkmcnt(0)" ::: "memory");
    }
    f32x2 ms[2][4];
#pragma unroll
    for (int tb = 0; tb < 2; ++tb)
#pragma unroll
        for (int r = 0; r < 4; ++r) ms[tb][r] = *(const f32x2*)(c.smem + (w * 32 + 16 * tb + 4 * g + r) * 8);
    const unsigned dst = WS_Z2 + (unsigned)(((jt * 9) * 64 + panel * 4 + w) * 1024 + l * 16);
#pragma unroll
    for (int n = 0; n < 9; ++n) {
        unsigned pk[2][2];
#pragma unroll
        for (int tb = 0; tb < 2; ++tb) {
            float v[4];
#pragma unroll
            for (int r = 0; r < 4; ++r) v[r] = (y[n][tb][r] - ms[tb][r][0]) * ms[tb][r][1];
            pk[tb][0] = pk2(v[0], v[1]); pk[tb][1] = pk2(v[2], v[3]);
        }
        u32x4 o;
#pragma unroll
        for (int i = 0; i < 2; ++i) {
            auto a = __builtin_amdgcn_permlane32_swap(pk[0][i], pk[1][i], false, false);
            auto b = __builtin_amdgcn_permlane16_swap(a[0], a[1], false, false);
            o[i] = b[0]; o[2 + i] = b[1];
        }
        st128(c, dst + n * 65536, o);
    }
    STAMPV(c.P, 14);
    publish(c, FL_Z + jt * 16 + panel);
    STAMPV(c.P, 11);
    if (tid < 64) { poll_flags(c, FL_WC + tid); poll_flags(c, FL_WC + 64 + tid); poll_flags(c, tid < 26 ? FL_WC + 128 + tid : ~0u); }
    wait_begin(c, tid < 16 ? FL_Z + jt * 16 + tid : tid < 20 ? FL_WP + 4 * panel + (tid - 16) : ~0u);
    wait_end();
}

DI void tile_qk(const Ctx& c, int panel, int which, int hq, int s0) {
    f32x4 acc[9][2];
    gemm1_main<true>(c, c.P.x, c.P.wqkv, 1728, panel * 128, which * 576 + hq * 144, c.lds0, acc, s0);
    STAMPV(c.P, 8);
    const int l = threadIdx.x & 63; const unsigned w = rfl(threadIdx.x >> 6); const int g = l >> 4;
    const float sc = which == 0 ? 0.23570226039551584f * 1.4426950408889634f : 1.0f;
    char* st = c.smem + w * 9216;
#pragma unroll
    for (int n = 0; n < 9; ++n)
#pragma unroll
        for (int pp = 0; pp < 2; ++pp) {
            const int cc = 16 * n + 4 * g + 2 * pp, hl = cc / 18, d = cc - 18 * hl;
#pragma unroll
            for (int tb = 0; tb < 2; ++tb) {
                const int tok = 16 * tb + (l & 15);
                const int off = d < 16 ? hl * 1024 + (tok + 32 * (d >> 3)) * 16 + (d & 7) * 2 : 8192 + hl * 128 + tok * 4;
                *(unsigned*)(st + off) = pk2(acc[n][tb][2 * pp] * sc, acc[n][tb][2 * pp + 1] * sc);
            }
        }
    asm volatile("s_waitcnt lgkmcnt(0)" ::: "memory");
    const unsigned dm = (which == 0 ? WS_QM : WS_KM) + (unsigned)(((hq * 8) * 64 + panel * 4 + w) * 1024 + l * 16);
#pragma unroll
    for (int h = 0; h < 8; ++h) st128(c, dm + h * 65536, *(const u32x4*)(st + h * 1024 + l * 16));
    st128(c, (which == 0 ? WS_QR : WS_KR) + (unsigned)(((hq * 8 + (l >> 3)) * 2048 + panel * 128 + 32 * w) * 4 + (l & 7) * 16), *(const u32x4*)(st + 8192 + l * 16));
}

DI void tile_v(const Ctx& c, int panel, int hq, int s0) {
    f32x4 acc[9][2];
    gemm1_main<false>(c, c.P.x, c.P.wqkv, 1728, panel * 128, 2 * 576 + hq * 144, c.lds0, acc, s0);
    STAMPV(c.P, 8);
    const int l = threadIdx.x & 63; const unsigned w = rfl(threadIdx.x >> 6); const int g = l >> 4;
    char* st = c.smem + w * 10240;
    if (l < 32) {
        const int blk = l >> 1, hh = l & 1;
        *(u32x4*)(st + blk * 640 + (hh * 20 + 18) * 16) = u32x4{0x3f803f80u, 0x3f803f80u, 0x3f803f80u, 0x3f803f80u};
        *(u32x4*)(st + blk * 640 + (hh * 20 + 19) * 16) = u32x4{0, 0, 0, 0};
    }
#pragma unroll
    for (int n = 0; n < 9; ++n) {
        const int cc = 16 * n + (l & 15), hl = cc / 18, d = cc - 18 * hl;
#pragma unroll
        for (int tb = 0; tb < 2; ++tb)
            *(u32x2*)(st + (hl * 2 + tb) * 640 + ((g & 1) * 20 + d) * 16 + (g >> 1) * 8) = u32x2{pk2(acc[n][tb][0], acc[n][tb][1]), pk2(acc[n][tb][2], acc[n][tb][3])};
    }
    asm volatile("s_waitcnt lgkmcnt(0)" ::: "memory");
    const unsigned dv = WS_VT + (unsigned)(((hq * 8) * 128 + panel * 8 + 2 * w) * 640);
#pragma unroll
    for (int i = 0; i < 10; ++i) { const int idx = 64 * i + l, h = idx / 80, o = idx - 80 * h; st128(c, dv + (unsigned)(h * 128 * 640 + o * 16), *(const u32x4*)(st + idx * 16)); }
}

DI void prep_wsp(const Ctx& c, int n) {
    const int tid = threadIdx.x, l = tid & 63; const unsigned w = rfl(tid >> 6); const int g = l >> 4;
    const u32x4 srd = make_srd(c.P.wsp, S_ * S_ * 2);
    const int tb = 2 * n + (int)(w & 1), s0 = (int)(w >> 1) * 16;
    unsigned vsrc = (unsigned)((tb * 16 + (l >> 3)) * (S_ * 2) + (((l & 7) ^ (l >> 3)) << 4));
    const unsigned lw = c.lds0 + w * 16384;
    const unsigned rd0 = lw + (unsigned)((l & 15) * 128) + ((g ^ (l & 7)) << 4), rd1 = lw + (unsigned)((l & 15) * 128) + (((4 + g) ^ (l & 7)) << 4);
    auto batch = [&](int bt) {
#pragma unroll
        for (int i = 0; i < 8; ++i) {
            dma_now(srd, vsrc, (unsigned)((s0 + bt * 8 + i) * 128), lw + i * 2048);
            dma_now(srd, vsrc, (unsigned)((s0 + bt * 8 + i) * 128 + 8 * S_ * 2), lw + i * 2048 + 1024);
        }
    };
    u32x4 f[16];
    batch(0);
    asm volatile("s_waitcnt vmcnt(0)" ::: "memory");
#pragma unroll
    for (int i = 0; i < 8; ++i) { f[2 * i] = __builtin_bit_cast(u32x4, lds_rd128(rd0 + i * 2048)); f[2 * i + 1] = __builtin_bit_cast(u32x4, lds_rd128(rd1 + i * 2048)); }
#pragma unroll
    for (int i = 0; i < 16; ++i) OPAQUE(f[i]);
    batch(1);
#pragma unroll
    for (int i = 0; i < 16; ++i) st128(c, WS_WP + (unsigned)((tb * 64 + 2 * s0 + i) * 1024 + l * 16), f[i]);
    asm volatile("s_waitcnt vmcnt(16)" ::: "memory");
#pragma unroll
    for (int i = 0; i < 8; ++i) {
        st128(c, WS_WP + (unsigned)((tb * 64 + 2 * s0 + 16 + 2 * i) * 1024 + l * 16), __builtin_bit_cast(u32x4, lds_rd128(rd0 + i * 2048)));
        st128(c, WS_WP + (unsigned)((tb * 64 + 2 * s0 + 17 + 2 * i) * 1024 + l * 16), __builtin_bit_cast(u32x4, lds_rd128(rd1 + i * 2048)));
    }
    publish(c, FL_WP + n);
}

DI void prep_wcat(const Ctx& c, int kb, unsigned flag) {
    const int tid = threadIdx.x, l = tid & 63; const unsigned w = rfl(tid >> 6); const int g = l >> 4;
    const u32x4 srd = kb < 18 ? make_srd(c.P.wo, D_ * D_ * 2) : make_srd(c.P.wout, FH * D_ * 2);
#pragma unroll
    for (int i = 0; i < 9; ++i) {
        const int idx = 64 * ((int)w + 4 * i) + l, sg = idx / 72, ch = idx - 72 * sg, gg = sg >> 3, j = sg & 7;
        int row;
        if (kb < 16) { const int head = 2 * kb + (gg >> 1), h = gg & 1; row = 18 * head + (j < 4 ? 4 * h + j : 8 + 4 * h + (j - 4)); }
        else if (kb < 18) row = 18 * (16 * (kb - 16) + 4 * gg + (j >> 1)) + 16 + (j & 1);
        else row = 32 * (kb - 18) + (j < 4 ? 4 * gg + j : 16 + 4 * gg + (j - 4));
        dma_now(srd, (unsigned)(row * 1152 + ch * 16), 0u, c.lds0 + (w + 4 * i) * 1024);
    }
    WAIT_BAR0();
    const int q = (l & 15) >> 2, p = l & 3;
    const unsigned r0 = c.lds0 + (unsigned)((8 * g + q) * 1152 + p * 8);
#pragma unroll
    for (int i = 0; i < 9; ++i) {
        const int db = (int)w + 4 * i;
        st128(c, WS_WC + (unsigned)((db * 90 + kb) * 1024 + l * 16), __builtin_bit_cast(u32x4, cat8(lds_rdtr(r0 + db * 32), lds_rdtr(r0 + 4 * 1152 + db * 32))));
    }
    publish(c, FL_WC + kb);
}

DI void prep_gate(const Ctx& c, int n, unsigned flag) {
    const int tid = threadIdx.x, l = tid & 63; const int w = tid >> 6;
    const int row = n * 32 + w * 8 + (l >> 3);
    u32x4 xv[9], gv[9];
#pragma unroll
    for (int i = 0; i < 9; ++i) { xv[i] = *(const u32x4*)(c.P.x + row * 576 + ((l & 7) + 8 * i) * 8); gv[i] = *(const u32x4*)(c.P.gatew + ((l & 7) + 8 * i) * 8); }
    float a = 0.f;
#pragma unroll
    for (int i = 0; i < 9; ++i)
#pragma unroll
        for (int e = 0; e < 4; ++e) { a = __builtin_fmaf(bflo(xv[i][e]), bflo(gv[i][e]), a); a = __builtin_fmaf(bfhi(xv[i][e]), bfhi(gv[i][e]), a); }
    a += __shfl_xor(a, 1); a += __shfl_xor(a, 2); a += __shfl_xor(a, 4);
    if ((l & 7) == 0) st32(c, WS_GT + row * 4, __float_as_uint(1.0f / (1.0f + __expf(-a))));
    publish(c, FL_WC + 90 + n);
}

DI void warm_l2(const Ctx& c, int x, int rank) {
    const int tid = threadIdx.x; const unsigned w = rfl(tid >> 6);
    const u32x4 sx = make_srd(c.P.x, S_ * D_ * 2), sw = make_srd(c.P.win, D_ * 4608 * 2), sq = make_srd(c.P.wqkv, D_ * 1728 * 2);
    const unsigned dst = c.lds0 + 73728 + w * 256;
    auto touch = [&](u32x4 srd, unsigned off) { asm volatile("s_mov_b32 m0, %0\n\ts_nop 0\n\tbuffer_load_dword %1, %2, 0 offen lds" ::"s"(dst), "v"(off), "s"(srd) : "memory"); };
    const int i = rank * 256 + tid;
    if (w < 2) {
        const int j = rank * 128 + tid;
        { const int row = j / 18, k = j - row * 18; touch(sw, (unsigned)(row * 9216 + (k < 9 ? 0 : 4608) + (x & 3) * 1152 + (k < 9 ? k : k - 9) * 128)); }
        { const int j2 = j + 8192, row = j2 / 18, k = j2 - row * 18; touch(sw, j2 < 10368 ? (unsigned)(row * 9216 + (k < 9 ? 0 : 4608) + (x & 3) * 1152 + (k < 9 ? k : k - 9) * 128) : OOB); }
    } else {
        const int j = rank * 128 + (tid - 128);
        touch(sx, (unsigned)((x >> 2) * 1024 * 1152 + j * 128));
        { const int row = j / 9, k = j - row * 9, ty = k / 3, ln = k - 3 * ty;
          touch(sq, j < 5184 ? (unsigned)(row * 3456 + ty * 1152 + (((x & 3) * 288) & ~127) + ln * 128) : OOB);
        }
        touch(sx, j < 1024 ? (unsigned)((x >> 2) * 1024 * 1152 + (8192 + j) * 128) : OOB);
    }
    (void)i;
}

DI void warm_z1(const Ctx& c, int x, int rank) {
    const int tid = threadIdx.x; const unsigned w = rfl(tid >> 6);
    const u32x4 sw = make_srd(c.P.win, D_ * 4608 * 2);
    const unsigned dst = c.lds0 + 73728 + w * 256;
    if (w < 2) {
        const int j = rank * 81 + tid, row = j / 9, k = j - row * 9;
        const unsigned off = (tid < 81) ? (unsigned)(row * 9216 + (x & 3) * 1152 + k * 128) : OOB;
        asm volatile("s_mov_b32 m0, %0\n\ts_nop 0\n\tbuffer_load_dword %1, %2, 0 offen lds" ::"s"(dst), "v"(off), "s"(sw) : "memory");
    }
}

constexpr int AB_SLOT = 20 * 1024;
struct AttnState {
    f32x16 S[2], O[2]; u32x4 pl[2], ph[2];
    bf16x8 k0, v0, v1; u32x4 k1; u32x4 q0[2], q1[2];
};
#define AT_PRIO 0
#define AT_HI() do {} while (0)
#define AT_LO() do {} while (0)
template <int T, class F>
DI void attn_slot(AttnState& s, unsigned aK, unsigned aR, unsigned aV, const f32x16& zero, F&& hook) {
    constexpr int U = T ^ 1;
    AT_HI(); s.S[U] = MFMA32(s.k0, BC8(s.q0[U]), zero); SB(); AT_LO();
    SB();
#pragma unroll
    for (int i = 0; i < 4; ++i) s.S[T][i] = EX2(s.S[T][i]);
    SB();
    AT_HI(); s.S[U] = MFMA32(BC8(s.k1), BC8(s.q1[U]), s.S[U]); SB(); AT_LO();
    SB();
    if (T == 0) { s.k0 = lds_rd128(aK); s.k1[0] = lds_rd32(aR); }
#pragma unroll
    for (int i = 4; i < 8; ++i) s.S[T][i] = EX2(s.S[T][i]);
    s.pl[T][0] = pk2(s.S[T][0], s.S[T][1]); s.pl[T][1] = pk2(s.S[T][2], s.S[T][3]);
    SB();
    AT_HI(); s.O[U] = MFMA32(s.v1, BC8(s.ph[U]), s.O[U]); SB(); AT_LO();
    SB();
    if (T == 0) s.v1 = lds_rd128(aV);
    hook();
    s.pl[T][2] = pk2(s.S[T][4], s.S[T][5]); s.pl[T][3] = pk2(s.S[T][6], s.S[T][7]);
    SB();
#pragma unroll
    for (int i = 8; i < 12; ++i) s.S[T][i] = EX2(s.S[T][i]);
    SB();
    AT_HI(); s.O[T] = MFMA32(s.v0, BC8(s.pl[T]), s.O[T]); SB(); AT_LO();
    SB();
    if (T == 1) s.v0 = lds_rd128(aV);
#pragma unroll
    for (int i = 12; i < 16; ++i) s.S[T][i] = EX2(s.S[T][i]);
    SB();
    s.ph[T][0] = pk2(s.S[T][8], s.S[T][9]); s.ph[T][1] = pk2(s.S[T][10], s.S[T][11]);
    s.ph[T][2] = pk2(s.S[T][12], s.S[T][13]); s.ph[T][3] = pk2(s.S[T][14], s.S[T][15]);
    SB();
}

DI void attn_unit(const Ctx& c, int head, int qb) {
    const int tid = threadIdx.x, l = tid & 63; const unsigned w = rfl(tid >> 6); const int h = l >> 5, r32 = l & 31; const unsigned lds0 = c.lds0;
    {
        const int hq = head >> 3;
        for (int d = 0; d < QKV_PRESLEEP; ++d) __builtin_amdgcn_s_sleep(32);
        wait_flags(c, tid < 16 ? FL_QKV + (4 + hq) * 16 + tid : tid < 32 ? FL_QKV + (8 + hq) * 16 + (tid - 16) : tid < 34 ? FL_QKV + hq * 16 + 2 * qb + (tid - 32) : ~0u);
    }
    STAMPV(c.P, 2);
    const u32x4 srd = make_srd(c.P.ws + WS_KM, WS_QM - WS_KM);
    unsigned vl = (unsigned)l * 16; OPAQUE(vl);
    unsigned sLds[5], sSrc[5], sStep[5];
#pragma unroll
    for (int i = 0; i < 5; ++i) {
        const unsigned p = w + 4 * i;
        sLds[i] = lds0 + p * 1024;
        sSrc[i] = p < 8 ? (unsigned)(head * 65536) + p * 1024 : p < 18 ? (WS_VT - WS_KM) + (unsigned)(head * 81920) + (p - 8) * 1024
                : p == 18 ? (WS_KR - WS_KM) + (unsigned)(head * 8192) : OOB;
        sStep[i] = p < 8 ? 8192u : (p < 18 ? 10240u : (p == 18 ? 1024u : 0u));
    }
    auto piece = [&](int i, int ch, unsigned boff) { set_m0s(sLds[i], rfl(boff)); const unsigned so = ch < 8 ? sSrc[i] + (unsigned)ch * sStep[i] : OOB; asm volatile("s_nop 0"); dma_go_c(srd, vl, so); };
    auto issue = [&](int ch, unsigned boff) {
#pragma unroll
        for (int i = 0; i < 5; ++i) piece(i, ch, boff); };
    AttnState s;
    const int tok0 = qb * 256 + 64 * (int)w;
    issue(0, 0);
#pragma unroll
    for (int t = 0; t < 2; ++t) {
        s.q0[t] = __builtin_amdgcn_raw_buffer_load_b128(c.ws, (int)(WS_QM + ((unsigned)head * 64 + (tok0 >> 5) + t) * 1024 + l * 16), 0, 16);
        const unsigned qr = __builtin_amdgcn_raw_buffer_load_b32(c.ws, (int)(WS_QR + ((unsigned)head * 2048 + tok0 + 32 * t + r32) * 4), 0, 16);
        s.q1[t] = u32x4{h ? 0u : qr, 0u, 0u, 0u};
    }
    asm volatile("" : "+v"(s.q0[0]), "+v"(s.q0[1]), "+v"(s.q1[0]), "+v"(s.q1[1]));
    unsigned ka = lds0 + (unsigned)l * 16, ra = lds0 + 18432u + (unsigned)r32 * 4, va = lds0 + 8192u + (unsigned)(h * 20 + (r32 < 19 ? r32 : 19)) * 16;
    OPAQUE(ka); OPAQUE(ra); OPAQUE(va);
    const f32x16 zero = {};
    volatile LAS unsigned* flag = (volatile LAS unsigned*)(uintptr_t)(lds0 + 2 * AB_SLOT);

    for (int attempt = 0; attempt < 2; ++attempt) {
        float mx[2] = {-3.0e38f, -3.0e38f};
        {
            const u32x4 qz[2] = {u32x4{s.q1[0][0], 0u, 0u, 0u}, u32x4{s.q1[1][0], 0u, 0u, 0u}};
            const int nch = attempt ? 8 : 1, nblk = attempt ? 8 : 1;
            for (int ch = 0; ch < nch; ++ch) {
                if (attempt) { WAIT_BAR0(); issue(ch, 0); }
                WAIT_BAR0();
                for (int kb = 0; kb < nblk; ++kb) {
                    const bf16x8 k0 = lds_rd128(ka + kb * 1024);
                    const u32x4 k1 = u32x4{lds_rd32(ra + kb * 128), 0u, 0u, 0u};
#pragma unroll
                    for (int t = 0; t < 2; ++t) {
                        f32x16 sc = MFMA32(k0, BC8(s.q0[t]), zero); sc = MFMA32(BC8(k1), BC8(qz[t]), sc);
#pragma unroll
                        for (int r = 0; r < 16; ++r) mx[t] = __builtin_fmaxf(mx[t], sc[r]);
                    }
                }
            }
            if (attempt) { WAIT_BAR0(); issue(0, 0); WAIT_BAR0(); }
        }
#pragma unroll
        for (int t = 0; t < 2; ++t) {
            auto rr = __builtin_amdgcn_permlane32_swap(__float_as_uint(mx[t]), __float_as_uint(mx[t]), false, false);
            const float m = __builtin_fmaxf(__uint_as_float(rr[0]), __uint_as_float(rr[1]));
            const unsigned nb = __float_as_uint(-m), hi = nb & 0xffff0000u;
            const float r1 = -m - __uint_as_float(hi); const unsigned mid = __float_as_uint(r1) & 0xffff0000u;
            const float r2 = r1 - __uint_as_float(mid); const unsigned lo = __float_as_uint(r2) & 0xffff0000u;
            s.q1[t][1] = h ? 0u : ((hi >> 16) | mid); s.q1[t][2] = h ? 0u : (lo >> 16);
        }
        STAMPV(c.P, 3);
        s.O[0] = zero; s.O[1] = zero;
        s.k0 = lds_rd128(ka); s.k1 = u32x4{lds_rd32(ra), 0x3f803f80u, 0x00003f80u, 0u};
        s.v0 = lds_rd128(va); s.v1 = s.v0; s.ph[1] = u32x4{0u, 0u, 0u, 0u};
        s.S[0] = MFMA32(s.k0, BC8(s.q0[0]), zero); s.S[0] = MFMA32(BC8(s.k1), BC8(s.q1[0]), s.S[0]);
        unsigned cur = 0, nxt = AB_SLOT;
        auto nohook = [] {};
        for (int ch = 0; ch < 8; ++ch) {
            const unsigned cK = ka + cur, cR = ra + cur, cV = va + cur, nK = ka + nxt, nR = ra + nxt, nV = va + nxt;
#pragma unroll
            for (int j = 0; j < 7; ++j) {
                attn_slot<0>(s, cK + (j + 1) * 1024, cR + (j + 1) * 128, cV + j * 1280 + 640, zero, nohook);
                attn_slot<1>(s, 0, 0, cV + (j + 1) * 1280, zero, [&] { if (j < 5) piece(j, ch + 1, nxt); });
            }
            asm volatile("s_waitcnt vmcnt(0)\n\ts_barrier" ::: "memory");
            SB();
            attn_slot<0>(s, nK, nR, cV + 7 * 1280 + 640, zero, nohook);
            attn_slot<1>(s, 0, 0, nV, zero, [&] { asm volatile("s_barrier" ::: "memory"); });
            const unsigned t_ = cur; cur = nxt; nxt = t_;
        }
        s.O[1] = MFMA32(s.v1, BC8(s.ph[1]), s.O[1]);
        if (attempt == 1) break;
        bool bad = false;
#pragma unroll
        for (int t = 0; t < 2; ++t) {
            float am = 0.f; bool b = false;
#pragma unroll
            for (int r = 0; r < 11; ++r) { am = __builtin_fmaxf(am, __builtin_fabsf(s.O[t][r])); b = b || (s.O[t][r] != s.O[t][r]); }
            bad = bad || b || !(am < 1.0e30f);
        }
        if (tid == 0) *flag = 0u;
        WAIT_BAR0();
        if (__any(bad)) *flag = 1u;
        WAIT_BAR0();
        if (rfl(*flag) == 0u) break;
    }
    STAMPV(c.P, 4);
#pragma unroll
    for (int t = 0; t < 2; ++t) {
        auto rr = __builtin_amdgcn_permlane32_swap(__float_as_uint(s.O[t][10]), __float_as_uint(s.O[t][10]), false, false);
        const float inv = 1.0f / __uint_as_float(rr[0]);
        const int tok = tok0 + 32 * t + r32, tb = tok >> 4;
        const unsigned base = WS_HC + (unsigned)(tb * 90 * 1024);
        const u32x4 m = {pk2(s.O[t][0] * inv, s.O[t][1] * inv), pk2(s.O[t][2] * inv, s.O[t][3] * inv), pk2(s.O[t][4] * inv, s.O[t][5] * inv), pk2(s.O[t][6] * inv, s.O[t][7] * inv)};
        st128(c, base + (unsigned)((head >> 1) * 1024 + ((tok & 15) + 16 * (2 * (head & 1) + h)) * 16), m);
        if (h == 0) st32(c, base + (unsigned)((16 + (head >> 4)) * 1024 + ((tok & 15) + 16 * ((head & 15) >> 2)) * 16 + (head & 3) * 4), pk2(s.O[t][8] * inv, s.O[t][9] * inv));
    }
    publish(c, FL_O + qb * 32 + head);
}

constexpr int SD_N = 8, SD_SLOT = 9216;
struct SpdRegs { bf16x8 zf[9]; u32x4 t[4][2]; unsigned vZ[3], vz, vT; };
template <int U>
DI void spd_sub(f32x4 (&acc)[9][2], SpdRegs& r, u32x4 srdT, u32x4 srdZ, unsigned sWZ, unsigned w, unsigned sTa, unsigned sTb, unsigned sZ) {
    constexpr int NX = (U + 1) & (SD_N - 1), PF = (U + SD_N - 1) & (SD_N - 1), TU = U & 3, TP = (U + 3) & 3;
    asm volatile("s_waitcnt vmcnt(10)\n\ts_barrier" : "+v"(r.t[TU][0]), "+v"(r.t[TU][1])::"memory");
    SB();
#pragma unroll
    for (int n = 0; n < 9; ++n) {
        if (n == 0) LGKM(4);
        if (n == 5) LGKM(5);
        if (n == 4 || n == 6) set_m0(sWZ, PF * SD_SLOT + (n == 4 ? 0 : 4096));
        mfma16v(acc[n][0], r.zf[n], BC8(r.t[TU][0]));
        if (n == 0) ld_frag<0>(r.t[TP][0], srdT, r.vT, sTa);
        if (n == 2) ld_frag<0>(r.t[TP][1], srdT, r.vT, sTb);
        if (n == 4) dma_go_c(srdZ, r.vZ[0], sZ);
        if (n == 6) dma_go_c(srdZ, r.vZ[1], sZ);
        mfma16v(acc[n][1], r.zf[n], BC8(r.t[TU][1]));
        r.zf[n] = lds_rd128(r.vz + NX * SD_SLOT + n * 1024);
        SB();
    }
    if (w == 0) { set_m0(sWZ, PF * SD_SLOT + 8192); asm volatile("s_nop 0"); dma_go_c(srdZ, r.vZ[2], sZ); }
    SB();
}

DI void spatial_tile(const Ctx& c, int panel, int jt, int u0, const u32x4 (&z1p)[9]) {
    const int tid = threadIdx.x, l = tid & 63; const unsigned w = rfl(tid >> 6); const unsigned lds0 = c.lds0;
    STAMPV(c.P, 4);
    const u32x4 srdT = make_srd(c.P.ws + WS_WP + (size_t)(panel * 8 + 2 * w) * 65536, 2 * 65536), srdZ = make_srd(c.P.ws + WS_Z2 + (size_t)jt * 9 * 65536, 9 * 65536);
    SpdRegs r;
#pragma unroll
    for (int i = 0; i < 3; ++i) { r.vZ[i] = (w + 4 * i) * 65536 + l * 16; OPAQUE(r.vZ[i]); }
    r.vz = lds0 + l * 16; OPAQUE(r.vz);
    r.vT = l * 16; OPAQUE(r.vT);
    const unsigned sWZ = lds0 + w * 1024;
#define SO(v) ((v) < 64 ? (unsigned)(((v) + u0) & 63) * 1024u : OOB)
#define SO2(v) ((v) < 64 ? (unsigned)(((v) + u0) & 63) * 1024u + 65536u : OOB)
    f32x4 acc[9][2];
#pragma unroll
    for (int n = 0; n < 9; ++n) { acc[n][0] = f32x4{0, 0, 0, 0}; acc[n][1] = f32x4{0, 0, 0, 0}; }
    PIN18(acc, "s_nop 1");
#pragma unroll
    for (int u = 0; u < SD_N - 1; ++u) {
        if (u >= 4) { ld_frag<0>(r.t[u - 4][0], srdT, r.vT, SO(u - 4)); ld_frag<0>(r.t[u - 4][1], srdT, r.vT, SO2(u - 4)); }
#pragma unroll
        for (int i = 0; i < 2; ++i) { set_m0(sWZ, u * SD_SLOT + i * 4096); asm volatile("s_nop 0"); dma_go_c(srdZ, r.vZ[i], SO(u)); }
        if (w == 0) { set_m0(sWZ, u * SD_SLOT + 8192); asm volatile("s_nop 0"); dma_go_c(srdZ, r.vZ[2], SO(u)); }
    }
    asm volatile("s_waitcnt vmcnt(18)\n\ts_barrier" ::: "memory");
#pragma unroll
    for (int n = 0; n < 9; ++n) r.zf[n] = lds_rd128(r.vz + n * 1024);
    LGKM(0);
    for (int u = 0; u < 64; u += 8) {
        spd_sub<0>(acc, r, srdT, srdZ, sWZ, w, SO(u + 3), SO2(u + 3), SO(u + 7));
        spd_sub<1>(acc, r, srdT, srdZ, sWZ, w, SO(u + 4), SO2(u + 4), SO(u + 8));
        spd_sub<2>(acc, r, srdT, srdZ, sWZ, w, SO(u + 5), SO2(u + 5), SO(u + 9));
        spd_sub<3>(acc, r, srdT, srdZ, sWZ, w, SO(u + 6), SO2(u + 6), SO(u + 10));
        spd_sub<4>(acc, r, srdT, srdZ, sWZ, w, SO(u + 7), SO2(u + 7), SO(u + 11));
        spd_sub<5>(acc, r, srdT, srdZ, sWZ, w, SO(u + 8), SO2(u + 8), SO(u + 12));
        spd_sub<6>(acc, r, srdT, srdZ, sWZ, w, SO(u + 9), SO2(u + 9), SO(u + 13));
        spd_sub<7>(acc, r, srdT, srdZ, sWZ, w, SO(u + 10), SO2(u + 10), SO(u + 14));
    }
    asm volatile("s_waitcnt vmcnt(0) lgkmcnt(0)" : "+v"(r.t[0][0]), "+v"(r.t[0][1]), "+v"(r.t[1][0]), "+v"(r.t[1][1]), "+v"(r.t[2][0]), "+v"(r.t[2][1]), "+v"(r.t[3][0]), "+v"(r.t[3][1])::"memory");
    PIN18(acc, "s_nop 7\n\ts_nop 1");
    STAMPV(c.P, 5);
    float bs[2];
#pragma unroll
    for (int tb = 0; tb < 2; ++tb) bs[tb] = bf2f(c.P.bsp[panel * 128 + 32 * w + 16 * tb + (l & 15)]);
    unsigned hp[9][2][2];
#pragma unroll
    for (int n = 0; n < 9; ++n)
#pragma unroll
        for (int tb = 0; tb < 2; ++tb) {
            hp[n][tb][0] = pk2(bflo(z1p[n][2 * tb]) * (acc[n][tb][0] + bs[tb]), bfhi(z1p[n][2 * tb]) * (acc[n][tb][1] + bs[tb]));
            hp[n][tb][1] = pk2(bflo(z1p[n][2 * tb + 1]) * (acc[n][tb][2] + bs[tb]), bfhi(z1p[n][2 * tb + 1]) * (acc[n][tb][3] + bs[tb]));
        }
    const int fb0 = jt * 9;
#pragma unroll
    for (int tb = 0; tb < 2; ++tb) {
        const unsigned base = WS_HC + (unsigned)(((panel * 8 + 2 * w + tb) * 90 + 18) * 1024 + l * 16);
        if ((jt & 1) == 0) {
#pragma unroll
            for (int n = 0; n < 8; n += 2) st128(c, base + (unsigned)(((fb0 + n) >> 1) * 1024), u32x4{hp[n][tb][0], hp[n][tb][1], hp[n + 1][tb][0], hp[n + 1][tb][1]});
            st64(c, base + (unsigned)(((fb0 + 8) >> 1) * 1024), u32x2{hp[8][tb][0], hp[8][tb][1]});
        } else {
            st64(c, base + (unsigned)((fb0 >> 1) * 1024 + 8), u32x2{hp[0][tb][0], hp[0][tb][1]});
#pragma unroll
            for (int n = 1; n < 9; n += 2) st128(c, base + (unsigned)(((fb0 + n) >> 1) * 1024), u32x4{hp[n][tb][0], hp[n][tb][1], hp[n + 1][tb][0], hp[n + 1][tb][1]});
        }
    }
    STAMPV(c.P, 15);
}

constexpr int FN_STAGE = 16 * 1024;
struct FnRegs { bf16x8 wf[5]; u32x4 h[4][3]; unsigned vW[4], vrd, vl; };
template <int U, int VM>
DI void fn_stage(f32x4 (&acc)[5], FnRegs& r, u32x4 srdW, u32x4 srdH, unsigned sWW, unsigned sNext) {
    constexpr int PF = (U + 3) & 3;
    asm volatile("s_waitcnt vmcnt(%3)\n\ts_barrier" : "+v"(r.h[U][0]), "+v"(r.h[U][1]), "+v"(r.h[U][2]) : "n"(VM) : "memory");
    SB();
#pragma unroll
    for (int n = 0; n < 5; ++n) r.wf[n] = lds_rd128(r.vrd + U * FN_STAGE + (n * 3) * 1024);
    ld_frag<0>(r.h[PF][0], srdH, r.vl, sNext); ld_frag<1024>(r.h[PF][1], srdH, r.vl, sNext); ld_frag<2048>(r.h[PF][2], srdH, r.vl, sNext);
    SB();
#pragma unroll
    for (int kk = 0; kk < 3; ++kk) {
#pragma unroll
        for (int n = 0; n < 5; ++n) {
            const int i = kk * 5 + n;
            const bool dm = (i % 4 == 1);
            if (dm) set_m0(sWW, PF * FN_STAGE + (i / 4) * 4096);
            mfma16v(acc[n], r.wf[n], BC8(r.h[U][kk]));
            if (dm) dma_go_c(srdW, r.vW[i / 4], sNext);
            if (kk + 1 < 3) r.wf[n] = lds_rd128(r.vrd + U * FN_STAGE + (n * 3 + kk + 1) * 1024);
            SB();
        }
    }
}
DI void fn_pre_w(FnRegs& r, u32x4 srdW, unsigned sWW, int kb0, int rot) {
#pragma unroll
    for (int s = 0; s < 3; ++s)
#pragma unroll
        for (int i = 0; i < 4; ++i) { set_m0(sWW, s * FN_STAGE + i * 4096); asm volatile("s_nop 0"); dma_go_c(srdW, r.vW[i], (unsigned)kb0 * 1024u + (unsigned)(s + rot) * 3072u); }
}
template <int NST>
DI void fn_range(f32x4 (&acc)[5], FnRegs& r, u32x4 srdW, u32x4 srdH, unsigned sWW, int kb0, int rot) {
    const unsigned o0 = (unsigned)kb0 * 1024u;
#define FO(s) ((s) < NST ? o0 + (unsigned)((s) + rot >= NST ? (s) + rot - NST : (s) + rot) * 3072u : OOB)
#pragma unroll
    for (int s = 0; s < 3; ++s) { ld_frag<0>(r.h[s][0], srdH, r.vl, FO(s)); ld_frag<1024>(r.h[s][1], srdH, r.vl, FO(s)); ld_frag<2048>(r.h[s][2], srdH, r.vl, FO(s)); }
    asm volatile("s_waitcnt vmcnt(6)" : "+v"(r.h[0][0]), "+v"(r.h[0][1]), "+v"(r.h[0][2])::"memory");
    fn_stage<0, 63>(acc, r, srdW, srdH, sWW, FO(3));
    asm volatile("s_waitcnt vmcnt(10)" : "+v"(r.h[1][0]), "+v"(r.h[1][1]), "+v"(r.h[1][2])::"memory");
    fn_stage<1, 63>(acc, r, srdW, srdH, sWW, FO(4));
    fn_stage<2, 14>(acc, r, srdW, srdH, sWW, FO(5));
    fn_stage<3, 14>(acc, r, srdW, srdH, sWW, FO(6));
    for (int s = 4; s + 4 <= NST; s += 4) {
        fn_stage<0, 14>(acc, r, srdW, srdH, sWW, FO(s + 3)); fn_stage<1, 14>(acc, r, srdW, srdH, sWW, FO(s + 4));
        fn_stage<2, 14>(acc, r, srdW, srdH, sWW, FO(s + 5)); fn_stage<3, 14>(acc, r, srdW, srdH, sWW, FO(s + 6));
    }
    if (NST % 4 == 2) { fn_stage<0, 14>(acc, r, srdW, srdH, sWW, OOB); fn_stage<1, 14>(acc, r, srdW, srdH, sWW, OOB); }
    asm volatile("s_waitcnt vmcnt(0) lgkmcnt(0)\n\ts_barrier" : "+v"(r.h[0][0]), "+v"(r.h[0][1]), "+v"(r.h[0][2]), "+v"(r.h[1][0]), "+v"(r.h[1][1]), "+v"(r.h[1][2]),
                 "+v"(r.h[2][0]), "+v"(r.h[2][1]), "+v"(r.h[2][2]), "+v"(r.h[3][0]), "+v"(r.h[3][1]), "+v"(r.h[3][2])::"memory");
    PIN5(acc, "s_nop 7\n\ts_nop 1");
}

DI void final_tile(const Ctx& c, int tt, int j, unsigned hflag, int rot) {
    const int tid = threadIdx.x, l = tid & 63; const unsigned w = rfl(tid >> 6); const int g = l >> 4;
    const int db0 = (9 * j) >> 1;
    const u32x4 srdW = make_srd(c.P.ws + WS_WC + (size_t)db0 * 90 * 1024, 5 * 90 * 1024);
    const u32x4 srdH = make_srd(c.P.ws + WS_HC + (size_t)(tt * 4 + w) * 90 * 1024, 90 * 1024);
    FnRegs r;
    r.vl = (unsigned)l * 16; OPAQUE(r.vl);
#pragma unroll
    for (int i = 0; i < 4; ++i) { const unsigned p = w + 4 * i, db = p / 3, kk = p - db * 3; r.vW[i] = p < 15 ? (db * 90 + kk) * 1024 + l * 16 : OOB; OPAQUE(r.vW[i]); }
    r.vrd = c.lds0 + l * 16; OPAQUE(r.vrd);
    const unsigned sWW = c.lds0 + w * 1024;
    asm volatile("s_waitcnt vmcnt(0) lgkmcnt(0)\n\ts_barrier" ::: "memory");
    raise_flag(c, hflag);
    fn_pre_w(r, srdW, sWW, 18, rot);
    wait_begin(c, tid < 16 ? FL_H + (tt >> 1) * 16 + tid : ~0u);
    asm volatile("s_waitcnt lgkmcnt(0)\n\ts_barrier" ::: "memory");
    STAMPV(c.P, 6);
    f32x4 accA[5], accB[5];
#pragma unroll
    for (int n = 0; n < 5; ++n) { accA[n] = f32x4{0, 0, 0, 0}; accB[n] = f32x4{0, 0, 0, 0}; }
    PIN5(accA, "s_nop 1"); PIN5(accB, "s_nop 1");
    fn_range<24>(accB, r, srdW, srdH, sWW, 18, rot);
    STAMPV(c.P, 7);
    fn_pre_w(r, srdW, sWW, 0, 0);
    if (tid < 64) poll_flags(c, tid < 32 ? FL_O + (tt >> 2) * 32 + tid : ~0u);
    if (tid < 64) ACQUIRE();
    wait_end();
    STAMPV(c.P, 8);
    fn_range<6>(accA, r, srdW, srdH, sWW, 0, 0);
    const int tok = tt * 64 + 16 * w + (l & 15);
    const float gt = __uint_as_float(__builtin_amdgcn_raw_buffer_load_b32(c.ws, (int)(WS_GT + tok * 4), 0, 16));
#pragma unroll
    for (int n = 0; n < 5; ++n) {
        const int d = (db0 + n) * 16 + 4 * g;
        if (d >= 72 * j && d < 72 * j + 72) {
            float v[4];
#pragma unroll
            for (int rr = 0; rr < 4; ++rr) v[rr] = gt * accA[n][rr] + (1.0f - gt) * accB[n][rr];
            *(u32x2*)(c.P.out + tok * 576 + d) = u32x2{pk2(v[0], v[1]), pk2(v[2], v[3])};
        }
    }
}

constexpr int MEGA_LDS = 73728 + 1024;
#define FROT 1
#define A_DELAY 0
#define SP_PRIO 2
#define A1_PRIO 1
#define B1_PRIO 1
#define SROT 0
#define SROT2 0
__global__ __launch_bounds__(256, 2) void mega_kernel(Params P) {
    extern __shared__ __attribute__((aligned(16))) char smem[];
    const int b = blockIdx.x, tid = threadIdx.x;
    STAMPV(P, 0);
    const u64 ep = __hip_atomic_load((gu64*)(P.ws + WS_EP), __ATOMIC_RELAXED, __HIP_MEMORY_SCOPE_AGENT);
    const u64 tag = ep + 1ull;
    const Ctx c{P, __builtin_amdgcn_make_buffer_rsrc(P.ws, (short)0, (int)WS_END, 0x00020000), tag, (unsigned)(uintptr_t)smem, smem};
    if (b < 256) {
        __builtin_amdgcn_s_setprio(A1_PRIO);
        const int x = b & 7, i = b >> 3, panel = (x >> 2) * 8 + (i & 7), jt = (x & 3) * 4 + (i >> 3);
        u32x4 z1p[9];
        tile_z(c, panel, jt, ROT ? ((i & 7) + 2 * (i >> 3)) % 9 : 0, z1p);
        __builtin_amdgcn_s_setprio(SP_PRIO);
        spatial_tile(c, panel, jt, ((i & 7) * SROT + (i >> 3) * SROT2) & 63, z1p);
        final_tile(c, 2 * panel + (jt >> 3), jt & 7, FL_H + panel * 16 + jt, FROT ? ((i & 7) * 2 + (i >> 3) * 1) % 21 : 0);
        STAMPV(P, 9);
        if (b == 0) {
            if (tid < 64) {
                for (unsigned spins = 0; spins < SPIN_CAP; ++spins) {
                    bool ok = true;
#pragma unroll
                    for (int k = 0; k < 4; ++k) {
                        ok = ok && __hip_atomic_load(flag_ptr(c, FL_H + 64 * k + tid), __ATOMIC_RELAXED, __HIP_MEMORY_SCOPE_AGENT) == tag;
                        ok = ok && __hip_atomic_load(flag_ptr(c, FL_O + 64 * k + tid), __ATOMIC_RELAXED, __HIP_MEMORY_SCOPE_AGENT) == tag;
                    }
                    if (__all(ok)) break;
                    __builtin_amdgcn_s_sleep(POLL_SLEEP);
                }
                if (tid == 0) __hip_atomic_store((gu64*)(P.ws + WS_EP), tag, __ATOMIC_RELAXED, __HIP_MEMORY_SCOPE_AGENT);
            }
        }
    } else {
        const int a = b - 256, x = a & 7, idx = a >> 3;
        __builtin_amdgcn_s_setprio(B1_PRIO);
        if (idx < 24) {
            const int hq = x & 3, ty = idx % 3, panel = (x >> 2) * 8 + idx / 3;
            const int s0 = ROT ? (idx / 3 + 3 * ty + 1) % 9 : 0;
            if (ty < 2) tile_qk(c, panel, ty, hq, s0); else tile_v(c, panel, hq, s0);
            const unsigned qf = FL_QKV + (ty * 4 + hq) * 16 + panel;
            STAMPV(P, 6);
            publish(c, qf);
            STAMPV(P, 7);
            { const int k = idx * 8 + x; if (k < 90) prep_wcat(c, k, ~0u); else if (k < 154) prep_gate(c, k - 90, ~0u); }

        } else {
            const int n = (idx - 24) * 8 + x;
            for (int d = 0; d < PREP_DELAY; ++d) __builtin_amdgcn_s_sleep(32);
            prep_wsp(c, n);
        }
        STAMPV(P, 1);
        __builtin_amdgcn_s_setprio(0);
        attn_unit(c, x * 4 + (idx & 3), idx >> 2);
        STAMPV(P, 5);
    }
}

extern "C" void kernel_launch(void* const* d_in, const int* in_sizes, int n_in, void* d_out, int out_size, void* d_ws, size_t ws_size, hipStream_t stream) {
    Params P;
    P.x = (const u16*)d_in[0]; P.wqkv = (const u16*)d_in[1]; P.wo = (const u16*)d_in[2]; P.win = (const u16*)d_in[4]; P.wsp = (const u16*)d_in[5];
    P.bsp = (const u16*)d_in[6]; P.wout = (const u16*)d_in[7]; P.gatew = (const u16*)d_in[8]; P.out = (u16*)d_out; P.ws = (char*)d_ws;
    hipFuncSetAttribute((const void*)mega_kernel, hipFuncAttributeMaxDynamicSharedMemorySize, MEGA_LDS);
    mega_kernel<<<512, 256, MEGA_LDS, stream>>>(P);
}
```

```cpp
#include <hip/hip_runtime.h>
#include <stdint.h>

typedef __bf16 bf16x8 __attribute__((ext_vector_type(8)));
typedef __bf16 bf16x2 __attribute__((ext_vector_type(2)));
typedef short s16x4 __attribute__((ext_vector_type(4)));
typedef float f32x2 __attribute__((ext_vector_type(2)));
typedef float f32x4 __attribute__((ext_vector_type(4)));
typedef float f32x16 __attribute__((ext_vector_type(16)));
typedef unsigned u32x2 __attribute__((ext_vector_type(2)));
typedef unsigned u32x4 __attribute__((ext_vector_type(4)));
typedef unsigned short u16;
typedef unsigned long long u64;
typedef __attribute__((address_space(1))) unsigned long long gu64;
#define LAS __attribute__((address_space(3)))
#define DI __device__ __forceinline__

constexpr int S_ = 2048, D_ = 576, FH = 2304;
constexpr unsigned WS_EP = 0;
constexpr unsigned WS_FL = 256;
constexpr unsigned FL_ST = 0, FL_Z = 256, FL_H = 512, FL_QKV = 768, FL_WP = 960, FL_O = 1024, FL_WC = 1280, FL_N = FL_WC + 154;
constexpr unsigned WS_GT = 16384;
constexpr unsigned WS_ST = WS_GT + 8192;
constexpr unsigned WS_KM = WS_ST + 16 * 16 * 1024;
constexpr unsigned WS_KR = WS_KM + 32 * 64 * 1024;
constexpr unsigned WS_VT = WS_KR + 32 * 2048 * 4;
constexpr unsigned WS_QM = WS_VT + 32 * 128 * 640;
constexpr unsigned WS_QR = WS_QM + 32 * 64 * 1024;
constexpr unsigned WS_Z2 = WS_QR + 32 * 2048 * 4;
constexpr unsigned WS_WP = WS_Z2 + 144 * 64 * 1024;
constexpr unsigned WS_HC = WS_WP + 128 * 64 * 1024;
constexpr unsigned WS_WC = WS_HC + 128 * 90 * 1024;
constexpr unsigned WS_END = WS_WC + 36 * 90 * 1024;
constexpr size_t WS_DBG = 200u << 20;

#define ROT 1
#define PREP_NT 1
#define SC1_LOADS 1
#define PREP_SLEEP 8
#define FILL_ON_QKV 1
#define QKV_PRESLEEP 2
#define WARM_Z1 0
#define PREP_DELAY 0
struct Params {
    const u16* x; const u16* wqkv; const u16* wo; const u16* win; const u16* wsp; const u16* bsp; const u16* wout; const u16* gatew;
    u16* out; char* ws;
};

#define STAMPV(P_, idx) do {} while (0)

DI float bf2f(u16 v) { return __uint_as_float((unsigned)v << 16); }
DI float bflo(unsigned v) { return __uint_as_float(v << 16); }
DI float bfhi(unsigned v) { return __uint_as_float(v & 0xffff0000u); }
DI unsigned pk2(float lo, float hi) { f32x2 v = {lo, hi}; bf16x2 b = __builtin_convertvector(v, bf16x2); return __builtin_bit_cast(unsigned, b); }
DI unsigned rfl(unsigned v) { return __builtin_amdgcn_readfirstlane(v); }
DI u32x4 make_srd(const void* p, unsigned bytes) {
    uint64_t a = (uint64_t)p;
    u32x4 r; r[0] = rfl((unsigned)a); r[1] = rfl((unsigned)(a >> 32) & 0xffffu); r[2] = rfl(bytes); r[3] = 0x00020000u; return r;
}
#define SB() __builtin_amdgcn_sched_barrier(0)
#define BAR() asm volatile("s_waitcnt lgkmcnt(0)\n\ts_barrier" ::: "memory")
#define WAIT_BAR0() asm volatile("s_waitcnt vmcnt(0) lgkmcnt(0)\n\ts_barrier" ::: "memory")
DI void set_m0(unsigned base, unsigned imm) { asm volatile("s_add_u32 m0, %0, %1" ::"s"(base), "n"(imm) : "scc"); }
DI void set_m0s(unsigned a, unsigned b) { asm volatile("s_add_u32 m0, %0, %1" ::"s"(a), "s"(b) : "scc"); }
DI void dma_go(u32x4 srd, unsigned voff, unsigned soff) { asm volatile("buffer_load_dwordx4 %0, %1, %2 offen lds" ::"v"(voff), "s"(srd), "s"(soff) : "memory"); }
DI void dma_go_c(u32x4 srd, unsigned voff, unsigned soff) { asm volatile("buffer_load_dwordx4 %0, %1, %2 offen sc1 lds" ::"v"(voff), "s"(srd), "s"(soff) : "memory"); }
#define LDSC " sc1"
#define ACQUIRE() do {} while (0)
DI void dma_now(u32x4 srd, unsigned voff, unsigned soff, unsigned ldsaddr) { asm volatile("s_mov_b32 m0, %0\n\ts_nop 0\n\tbuffer_load_dwordx4 %1, %2, %3 offen nt lds" ::"s"(ldsaddr), "v"(voff), "s"(srd), "s"(soff) : "memory"); }
template <int IMM> DI void ld_frag(u32x4& d, u32x4 srd, unsigned voff, unsigned soff) {
    asm volatile("buffer_load_dwordx4 %0, %1, %2, %3 offen offset:%4" LDSC : "=v"(d) : "v"(voff), "s"(srd), "s"(soff), "n"(IMM) : "memory");
}
DI bf16x8 lds_rd128(unsigned a) { return *(const LAS bf16x8*)(uintptr_t)a; }
DI unsigned lds_rd32(unsigned a) { return *(const LAS unsigned*)(uintptr_t)a; }
DI s16x4 lds_rdtr(unsigned a) { return __builtin_amdgcn_ds_read_tr16_b64_v4i16((LAS s16x4*)(uintptr_t)a); }
DI bf16x8 cat8(s16x4 a, s16x4 b) { typedef short s16x8 __attribute__((ext_vector_type(8))); s16x8 v = __builtin_shufflevector(a, b, 0, 1, 2, 3, 4, 5, 6, 7); return __builtin_bit_cast(bf16x8, v); }
DI void mfma16v(f32x4& c, bf16x8 a, bf16x8 b) { asm volatile("v_mfma_f32_16x16x32_bf16 %0, %1, %2, %0" : "+v"(c) : "v"(a), "v"(b)); }
#define PIN18(acc, txt) asm volatile(txt : "+v"(acc[0][0]), "+v"(acc[0][1]), "+v"(acc[1][0]), "+v"(acc[1][1]), "+v"(acc[2][0]), "+v"(acc[2][1]), "+v"(acc[3][0]), "+v"(acc[3][1]), "+v"(acc[4][0]), "+v"(acc[4][1]), \
    "+v"(acc[5][0]), "+v"(acc[5][1]), "+v"(acc[6][0]), "+v"(acc[6][1]), "+v"(acc[7][0]), "+v"(acc[7][1]), "+v"(acc[8][0]), "+v"(acc[8][1]))
#define PIN5(acc, txt) asm volatile(txt : "+v"(acc[0]), "+v"(acc[1]), "+v"(acc[2]), "+v"(acc[3]), "+v"(acc[4]))
#define LGKM(n) __builtin_amdgcn_s_waitcnt(0xC07F | ((n) << 8))
#define OPAQUE(x) asm volatile("" : "+v"(x))
constexpr unsigned OOB = 0x40000000u;
#define EX2(x) __builtin_amdgcn_exp2f(x)
#define BC8(x) __builtin_bit_cast(bf16x8, x)
#define MFMA32(a, b, c) __builtin_amdgcn_mfma_f32_32x32x16_bf16(a, b, c, 0, 0, 0)

DI float gelu_t(float x) {
    const float c0 = -2.0f * 0.7978845608028654f * 1.4426950408889634f, c1 = c0 * 0.044715f;
    const float e = __builtin_amdgcn_exp2f(x * __builtin_fmaf(x * x, c1, c0));
    return x * __builtin_amdgcn_rcpf(1.0f + e);
}
template <int N> DI float dpp_ror_add(float v) { return v + __builtin_bit_cast(float, __builtin_amdgcn_update_dpp(0, __builtin_bit_cast(int, v), 0x120 + N, 0xf, 0xf, false)); }
DI float row16_sum(float v) { v = dpp_ror_add<8>(v); v = dpp_ror_add<4>(v); v = dpp_ror_add<2>(v); v = dpp_ror_add<1>(v); return v; }

struct Ctx { const Params& P; __amdgpu_buffer_rsrc_t ws; u64 tag; unsigned lds0; char* smem; };
#define ST_AUX 16
DI void st128(const Ctx& c, unsigned off, u32x4 v) { __builtin_amdgcn_raw_buffer_store_b128(v, c.ws, (int)off, 0, ST_AUX); }
DI void st64(const Ctx& c, unsigned off, u32x2 v) { __builtin_amdgcn_raw_buffer_store_b64(v, c.ws, (int)off, 0, 16); }
DI void st32(const Ctx& c, unsigned off, unsigned v) { __builtin_amdgcn_raw_buffer_store_b32(v, c.ws, (int)off, 0, 16); }
DI gu64* flag_ptr(const Ctx& c, unsigned idx) { return (gu64*)(c.P.ws + WS_FL) + idx; }
DI void publish(const Ctx& c, unsigned idx) {
    asm volatile("s_waitcnt vmcnt(0) lgkmcnt(0)\n\ts_barrier" ::: "memory");
    if (threadIdx.x == 0) __hip_atomic_store(flag_ptr(c, idx), c.tag, __ATOMIC_RELAXED, __HIP_MEMORY_SCOPE_AGENT);
}
#define POLL_SLEEP 16
#define SPIN_CAP (1u << 16)
DI void wait_flags(const Ctx& c, unsigned idx) {
    if (threadIdx.x < 64) {
        gu64* p = flag_ptr(c, idx == ~0u ? 0u : idx);
        for (unsigned spins = 0; spins < SPIN_CAP; ++spins) {
            const u64 v = idx == ~0u ? c.tag : __hip_atomic_load(p, __ATOMIC_RELAXED, __HIP_MEMORY_SCOPE_AGENT);
            if (__all(v == c.tag)) break;
            __builtin_amdgcn_s_sleep(POLL_SLEEP);
        }
        ACQUIRE();
        asm volatile("s_waitcnt vmcnt(0)" ::: "memory");
    }
    asm volatile("s_waitcnt lgkmcnt(0)\n\ts_barrier" ::: "memory");
}

DI void poll_flags(const Ctx& c, unsigned idx) {
    gu64* p = flag_ptr(c, idx == ~0u ? 0u : idx);
    for (unsigned spins = 0; spins < SPIN_CAP; ++spins) {
        const u64 v = idx == ~0u ? c.tag : __hip_atomic_load(p, __ATOMIC_RELAXED, __HIP_MEMORY_SCOPE_AGENT);
        if (__all(v == c.tag)) break;
        __builtin_amdgcn_s_sleep(POLL_SLEEP);
    }
}
DI void raise_flag(const Ctx& c, unsigned idx) { if (threadIdx.x == 0) __hip_atomic_store(flag_ptr(c, idx), c.tag, __ATOMIC_RELAXED, __HIP_MEMORY_SCOPE_AGENT); }
DI void wait_begin(const Ctx& c, unsigned idx) { if (threadIdx.x < 64) { poll_flags(c, idx); ACQUIRE(); } }
DI void wait_end() { asm volatile("s_waitcnt vmcnt(0) lgkmcnt(0)\n\ts_barrier" ::: "memory"); }

constexpr int G1_XS = 16384, G1_WS = 9216, G1_WOFF = 2 * G1_XS, G1_LDS = G1_WOFF + 4 * G1_WS;
struct G1Regs { bf16x8 wf[9], xf[2][2]; unsigned vX[4], vW[3], vw0, vw1, vx[2]; };
template <bool TRANS> DI void g1_mm(f32x4& c, bf16x8 wf, bf16x8 xf) { if (TRANS) mfma16v(c, wf, xf); else mfma16v(c, xf, wf); }
DI bf16x8 g1_rdw(unsigned lo, unsigned hi, int off) { return cat8(lds_rdtr(lo + off), lds_rdtr(hi + off)); }

template <bool TRANS, int J>
DI void g1_sub(f32x4 (&acc)[9][2], G1Regs& r, u32x4 srdX, u32x4 srdW, unsigned sWX, unsigned sWW, unsigned w, unsigned sX, unsigned sW) {
    constexpr int N1 = (J + 1) & 3, CUR = J & 1, NXT = CUR ^ 1, PW = (J + 3) & 3;
    constexpr int XS_RD = N1 >> 1, XS_WR = J >> 1;
    if (J & 1) asm volatile("s_waitcnt vmcnt(2) lgkmcnt(8)\n\ts_barrier" ::: "memory");
    else asm volatile("s_waitcnt vmcnt(6) lgkmcnt(8)\n\ts_barrier" ::: "memory");
    LGKM(8);
    SB();
#pragma unroll
    for (int n = 0; n < 9; ++n) {
        if (n == 5) LGKM(12);
        const bool dw = (n == 1 || n == 3), dx = (J & 1) && n >= 4 && n < 8;
        if (dw) set_m0(sWW, PW * G1_WS + (n == 1 ? 0 : 4096));
        if (dx) set_m0(sWX, XS_WR * G1_XS + (n - 4) * 1024);
        g1_mm<TRANS>(acc[n][0], r.wf[n], r.xf[CUR][0]);
        if (dw) dma_go(srdW, r.vW[n == 1 ? 0 : 1], sW);
        if (dx) dma_go(srdX, r.vX[n - 4], sX);
        g1_mm<TRANS>(acc[n][1], r.wf[n], r.xf[CUR][1]);
        if (n < 2) r.xf[NXT][n] = lds_rd128(r.vx[N1 & 1] + XS_RD * G1_XS + n * 2048);
        r.wf[n] = g1_rdw(r.vw0, r.vw1, N1 * G1_WS + n * 32);
        SB();
    }
    if (w == 0) { set_m0(sWW, PW * G1_WS + 8192); asm volatile("s_nop 0"); dma_go(srdW, r.vW[2], sW); }
    SB();
}

template <bool TRANS>
DI void gemm1_main(const Ctx& c, const u16* x, const u16* W, int ldw, int row0, int col0, unsigned lds0, f32x4 (&acc)[9][2], int s0, unsigned deferred_flag = ~0u) {
    const int tid = threadIdx.x, l = tid & 63; const unsigned w = rfl(tid >> 6);
    const u32x4 srdX = make_srd(x, S_ * D_ * 2), srdW = make_srd(W, (unsigned)(D_ * ldw * 2));
    const int g = l >> 4, q = (l & 15) >> 2, p = l & 3;
    G1Regs r;
#pragma unroll
    for (int i = 0; i < 4; ++i) { r.vX[i] = (unsigned)((row0 + 32 * w + 8 * i + (l >> 3)) * (D_ * 2) + (((l & 7) ^ (l >> 3)) << 4)); OPAQUE(r.vX[i]); }
#pragma unroll
    for (int i = 0; i < 3; ++i) { const int pc = (int)w + 4 * i, idx = 64 * pc + l, rho = idx / 18, cc = idx - rho * 18, k = rho ^ (((rho >> 3) & 1) << 2);
        r.vW[i] = pc < 9 ? (unsigned)(k * ldw * 2 + (col0 + 8 * cc) * 2) : OOB; OPAQUE(r.vW[i]); }
    r.vw0 = lds0 + (unsigned)(G1_WOFF + (8 * g + (q ^ (4 * (g & 1)))) * 288 + p * 8); r.vw1 = lds0 + (unsigned)(G1_WOFF + (8 * g + ((q + 4) ^ (4 * (g & 1)))) * 288 + p * 8);
    r.vx[0] = lds0 + (unsigned)((32 * w + (l & 15)) * 128) + ((g ^ (l & 7)) << 4); r.vx[1] = lds0 + (unsigned)((32 * w + (l & 15)) * 128) + (((4 + g) ^ (l & 7)) << 4);
    OPAQUE(r.vw0); OPAQUE(r.vw1); OPAQUE(r.vx[0]); OPAQUE(r.vx[1]);
    const unsigned sWX = lds0 + w * 4096, sWW = lds0 + G1_WOFF + w * 1024, kst = (unsigned)(32 * ldw * 2);
    auto xs = [&](int slot, unsigned so) {
#pragma unroll
        for (int i = 0; i < 4; ++i) { set_m0(sWX, slot * G1_XS + i * 1024); asm volatile("s_nop 0"); dma_go(srdX, r.vX[i], so); } };
    auto wsl = [&](int slot, unsigned so) {
#pragma unroll
        for (int i = 0; i < 2; ++i) { set_m0(sWW, slot * G1_WS + i * 4096); asm volatile("s_nop 0"); dma_go(srdW, r.vW[i], so); }
        if (w == 0) { set_m0(sWW, slot * G1_WS + 8192); asm volatile("s_nop 0"); dma_go(srdW, r.vW[2], so); } };
    auto rot9 = [&](int s) { const int t = s + s0; return (unsigned)(t >= 9 ? t - 9 : t); };
#define GW(v) ((v) < 18 ? (rot9((v) >> 1) * 2 + ((v) & 1)) * kst : OOB)
#define GX(s) ((s) < 9 ? rot9(s) * 128u : OOB)
    xs(0, GX(0)); wsl(0, GW(0)); wsl(1, GW(1)); wsl(2, GW(2)); xs(1, GX(1));
#pragma unroll
    for (int n = 0; n < 9; ++n) { acc[n][0] = f32x4{0, 0, 0, 0}; acc[n][1] = f32x4{0, 0, 0, 0}; }
    PIN18(acc, "s_nop 1");
    asm volatile("s_waitcnt vmcnt(8)\n\ts_barrier" ::: "memory");
    if (deferred_flag != ~0u) { raise_flag(c, deferred_flag); STAMPV(c.P, 12); } else STAMPV(c.P, 10);
#pragma unroll
    for (int tb = 0; tb < 2; ++tb) r.xf[0][tb] = lds_rd128(r.vx[0] + tb * 2048);
#pragma unroll
    for (int n = 0; n < 9; ++n) r.wf[n] = g1_rdw(r.vw0, r.vw1, n * 32);
    LGKM(0);
    for (int u = 0; u < 16; u += 4) {
        g1_sub<TRANS, 0>(acc, r, srdX, srdW, sWX, sWW, w, 0, GW(u + 3));
        g1_sub<TRANS, 1>(acc, r, srdX, srdW, sWX, sWW, w, GX(u / 2 + 2), GW(u + 4));
        g1_sub<TRANS, 2>(acc, r, srdX, srdW, sWX, sWW, w, 0, GW(u + 5));
        g1_sub<TRANS, 3>(acc, r, srdX, srdW, sWX, sWW, w, GX(u / 2 + 3), GW(u + 6));
    }
    g1_sub<TRANS, 0>(acc, r, srdX, srdW, sWX, sWW, w, 0, OOB);
    g1_sub<TRANS, 1>(acc, r, srdX, srdW, sWX, sWW, w, OOB, OOB);
    asm volatile("s_waitcnt vmcnt(0) lgkmcnt(0)\n\ts_barrier" ::: "memory");
    PIN18(acc, "s_nop 7\n\ts_nop 1");
}

DI void tile_z(const Ctx& c, int panel, int jt, int s0, u32x4 (&z1p)[9]) {
    const int tid = threadIdx.x, l = tid & 63; const unsigned w = rfl(tid >> 6); const int g = l >> 4;
    f32x4 y[9][2];
    gemm1_main<false>(c, c.P.x, c.P.win, 4608, panel * 128, FH + jt * 144, c.lds0, y, s0);
    STAMPV(c.P, 1);
    {
        float s1[2][4], s2[2][4];
#pragma unroll
        for (int tb = 0; tb < 2; ++tb)
#pragma unroll
            for (int r = 0; r < 4; ++r) { s1[tb][r] = 0.f; s2[tb][r] = 0.f; }
#pragma unroll
        for (int n = 0; n < 9; ++n)
#pragma unroll
            for (int tb = 0; tb < 2; ++tb)
#pragma unroll
                for (int r = 0; r < 4; ++r) { const float v = gelu_t(y[n][tb][r]); y[n][tb][r] = v; s1[tb][r] += v; s2[tb][r] = __builtin_fmaf(v, v, s2[tb][r]); }
#pragma unroll
        for (int tb = 0; tb < 2; ++tb)
#pragma unroll
            for (int r = 0; r < 4; ++r) {
                const float a = row16_sum(s1[tb][r]), b = row16_sum(s2[tb][r]);
                if ((l & 15) == 0) st64(c, WS_ST + (unsigned)((panel * 16 + jt) * 1024 + (32 * w + 16 * tb + 4 * g + r) * 8), u32x2{__float_as_uint(a), __float_as_uint(b)});
            }
    }
    STAMPV(c.P, 2);
    f32x4 acc[9][2];
    gemm1_main<true>(c, c.P.x, c.P.win, 4608, panel * 128, jt * 144, c.lds0, acc, s0, FL_ST + panel * 16 + jt);
    STAMPV(c.P, 3);
    if (tid < 64) poll_flags(c, tid < 16 ? FL_ST + panel * 16 + tid : ~0u);
    BAR();
    STAMPV(c.P, 13);
    u32x2 pv[8];
#pragma unroll
    for (int i = 0; i < 8; ++i) pv[i] = __builtin_amdgcn_raw_buffer_load_b64(c.ws, (int)(WS_ST + (panel * 16 + 8 * (l >> 5) + i) * 1024 + (32 * w + (l & 31)) * 8), 0, 16);
    SB();
#pragma unroll
    for (int n = 0; n < 9; ++n)
#pragma unroll
        for (int tb = 0; tb < 2; ++tb) { z1p[n][2 * tb] = pk2(gelu_t(acc[n][tb][0]), gelu_t(acc[n][tb][1])); z1p[n][2 * tb + 1] = pk2(gelu_t(acc[n][tb][2]), gelu_t(acc[n][tb][3])); }
    asm volatile("" : "+v"(z1p[0]), "+v"(z1p[1]), "+v"(z1p[2]), "+v"(z1p[3]), "+v"(z1p[4]), "+v"(z1p[5]), "+v"(z1p[6]), "+v"(z1p[7]), "+v"(z1p[8]));
    SB();
    {
        float a = 0.f, b = 0.f;
#pragma unroll
        for (int i = 0; i < 8; ++i) { a += __uint_as_float(pv[i][0]); b += __uint_as_float(pv[i][1]); }
        auto ra = __builtin_amdgcn_permlane32_swap(__float_as_uint(a), __float_as_uint(a), false, false);
        auto rb = __builtin_amdgcn_permlane32_swap(__float_as_uint(b), __float_as_uint(b), false, false);
        a = __uint_as_float(ra[0]) + __uint_as_float(ra[1]); b = __uint_as_float(rb[0]) + __uint_as_float(rb[1]);
        const float m = a * (1.0f / FH), var = __builtin_fmaxf(b * (1.0f / FH) - m * m, 0.f);
        const float rs = 1.0f / __builtin_sqrtf(var + 1e-5f);
        if (l < 32) *(f32x2*)(c.smem + (w * 32 + l) * 8) = f32x2{m, rs};
        asm volatile("s_waitcnt lgkmcnt(0)" ::: "memory");
    }
    f32x2 ms[2][4];
#pragma unroll
    for (int tb = 0; tb < 2; ++tb)
#pragma unroll
        for (int r = 0; r < 4; ++r) ms[tb][r] = *(const f32x2*)(c.smem + (w * 32 + 16 * tb + 4 * g + r) * 8);
    const unsigned dst = WS_Z2 + (unsigned)(((jt * 9) * 64 + panel * 4 + w) * 1024 + l * 16);
#pragma unroll
    for (int n = 0; n < 9; ++n) {
        unsigned pk[2][2];
#pragma unroll
        for (int tb = 0; tb < 2; ++tb) {
            float v[4];
#pragma unroll
            for (int r = 0; r < 4; ++r) v[r] = (y[n][tb][r] - ms[tb][r][0]) * ms[tb][r][1];
            pk[tb][0] = pk2(v[0], v[1]); pk[tb][1] = pk2(v[2], v[3]);
        }
        u32x4 o;
#pragma unroll
        for (int i = 0; i < 2; ++i) {
            auto a = __builtin_amdgcn_permlane32_swap(pk[0][i], pk[1][i], false, false);
            auto b = __builtin_amdgcn_permlane16_swap(a[0], a[1], false, false);
            o[i] = b[0]; o[2 + i] = b[1];
        }
        st128(c, dst + n * 65536, o);
    }
    STAMPV(c.P, 14);
    publish(c, FL_Z + jt * 16 + panel);
    STAMPV(c.P, 11);
    if (tid < 64) { poll_flags(c, FL_WC + tid); poll_flags(c, FL_WC + 64 + tid); poll_flags(c, tid < 26 ? FL_WC + 128 + tid : ~0u); }
    wait_begin(c, tid < 16 ? FL_Z + jt * 16 + tid : tid < 20 ? FL_WP + 4 * panel + (tid - 16) : ~0u);
    wait_end();
}

DI void tile_qk(const Ctx& c, int panel, int which, int hq, int s0) {
    f32x4 acc[9][2];
    gemm1_main<true>(c, c.P.x, c.P.wqkv, 1728, panel * 128, which * 576 + hq * 144, c.lds0, acc, s0);
    STAMPV(c.P, 8);
    const int l = threadIdx.x & 63; const unsigned w = rfl(threadIdx.x >> 6); const int g = l >> 4;
    const float sc = which == 0 ? 0.23570226039551584f * 1.4426950408889634f : 1.0f;
    char* st = c.smem + w * 9216;
#pragma unroll
    for (int n = 0; n < 9; ++n)
#pragma unroll
        for (int pp = 0; pp < 2; ++pp) {
            const int cc = 16 * n + 4 * g + 2 * pp, hl = cc / 18, d = cc - 18 * hl;
#pragma unroll
            for (int tb = 0; tb < 2; ++tb) {
                const int tok = 16 * tb + (l & 15);
                const int off = d < 16 ? hl * 1024 + (tok + 32 * (d >> 3)) * 16 + (d & 7) * 2 : 8192 + hl * 128 + tok * 4;
                *(unsigned*)(st + off) = pk2(acc[n][tb][2 * pp] * sc, acc[n][tb][2 * pp + 1] * sc);
            }
        }
    asm volatile("s_waitcnt lgkmcnt(0)" ::: "memory");
    const unsigned dm = (which == 0 ? WS_QM : WS_KM) + (unsigned)(((hq * 8) * 64 + panel * 4 + w) * 1024 + l * 16);
#pragma unroll
    for (int h = 0; h < 8; ++h) st128(c, dm + h * 65536, *(const u32x4*)(st + h * 1024 + l * 16));
    st128(c, (which == 0 ? WS_QR : WS_KR) + (unsigned)(((hq * 8 + (l >> 3)) * 2048 + panel * 128 + 32 * w) * 4 + (l & 7) * 16), *(const u32x4*)(st + 8192 + l * 16));
}

DI void tile_v(const Ctx& c, int panel, int hq, int s0) {
    f32x4 acc[9][2];
    gemm1_main<false>(c, c.P.x, c.P.wqkv, 1728, panel * 128, 2 * 576 + hq * 144, c.lds0, acc, s0);
    STAMPV(c.P, 8);
    const int l = threadIdx.x & 63; const unsigned w = rfl(threadIdx.x >> 6); const int g = l >> 4;
    char* st = c.smem + w * 10240;
    if (l < 32) {
        const int blk = l >> 1, hh = l & 1;
        *(u32x4*)(st + blk * 640 + (hh * 20 + 18) * 16) = u32x4{0x3f803f80u, 0x3f803f80u, 0x3f803f80u, 0x3f803f80u};
        *(u32x4*)(st + blk * 640 + (hh * 20 + 19) * 16) = u32x4{0, 0, 0, 0};
    }
#pragma unroll
    for (int n = 0; n < 9; ++n) {
        const int cc = 16 * n + (l & 15), hl = cc / 18, d = cc - 18 * hl;
#pragma unroll
        for (int tb = 0; tb < 2; ++tb)
            *(u32x2*)(st + (hl * 2 + tb) * 640 + ((g & 1) * 20 + d) * 16 + (g >> 1) * 8) = u32x2{pk2(acc[n][tb][0], acc[n][tb][1]), pk2(acc[n][tb][2], acc[n][tb][3])};
    }
    asm volatile("s_waitcnt lgkmcnt(0)" ::: "memory");
    const unsigned dv = WS_VT + (unsigned)(((hq * 8) * 128 + panel * 8 + 2 * w) * 640);
#pragma unroll
    for (int i = 0; i < 10; ++i) { const int idx = 64 * i + l, h = idx / 80, o = idx - 80 * h; st128(c, dv + (unsigned)(h * 128 * 640 + o * 16), *(const u32x4*)(st + idx * 16)); }
}

DI void prep_wsp(const Ctx& c, int n) {
    const int tid = threadIdx.x, l = tid & 63; const unsigned w = rfl(tid >> 6); const int g = l >> 4;
    const u32x4 srd = make_srd(c.P.wsp, S_ * S_ * 2);
    const int tb = 2 * n + (int)(w & 1), s0 = (int)(w >> 1) * 16;
    unsigned vsrc = (unsigned)((tb * 16 + (l >> 3)) * (S_ * 2) + (((l & 7) ^ (l >> 3)) << 4));
    const unsigned lw = c.lds0 + w * 16384;
    const unsigned rd0 = lw + (unsigned)((l & 15) * 128) + ((g ^ (l & 7)) << 4), rd1 = lw + (unsigned)((l & 15) * 128) + (((4 + g) ^ (l & 7)) << 4);
    auto batch = [&](int bt) {
#pragma unroll
        for (int i = 0; i < 8; ++i) {
            dma_now(srd, vsrc, (unsigned)((s0 + bt * 8 + i) * 128), lw + i * 2048);
            dma_now(srd, vsrc, (unsigned)((s0 + bt * 8 + i) * 128 + 8 * S_ * 2), lw + i * 2048 + 1024);
        }
    };
    u32x4 f[16];
    batch(0);
    asm volatile("s_waitcnt vmcnt(0)" ::: "memory");
#pragma unroll
    for (int i = 0; i < 8; ++i) { f[2 * i] = __builtin_bit_cast(u32x4, lds_rd128(rd0 + i * 2048)); f[2 * i + 1] = __builtin_bit_cast(u32x4, lds_rd128(rd1 + i * 2048)); }
#pragma unroll
    for (int i = 0; i < 16; ++i) OPAQUE(f[i]);
    batch(1);
#pragma unroll
    for (int i = 0; i < 16; ++i) st128(c, WS_WP + (unsigned)((tb * 64 + 2 * s0 + i) * 1024 + l * 16), f[i]);
    asm volatile("s_waitcnt vmcnt(16)" ::: "memory");
#pragma unroll
    for (int i = 0; i < 8; ++i) {
        st128(c, WS_WP + (unsigned)((tb * 64 + 2 * s0 + 16 + 2 * i) * 1024 + l * 16), __builtin_bit_cast(u32x4, lds_rd128(rd0 + i * 2048)));
        st128(c, WS_WP + (unsigned)((tb * 64 + 2 * s0 + 17 + 2 * i) * 1024 + l * 16), __builtin_bit_cast(u32x4, lds_rd128(rd1 + i * 2048)));
    }
    publish(c, FL_WP + n);
}

DI void prep_wcat(const Ctx& c, int kb, unsigned flag) {
    const int tid = threadIdx.x, l = tid & 63; const unsigned w = rfl(tid >> 6); const int g = l >> 4;
    const u32x4 srd = kb < 18 ? make_srd(c.P.wo, D_ * D_ * 2) : make_srd(c.P.wout, FH * D_ * 2);
#pragma unroll
    for (int i = 0; i < 9; ++i) {
        const int idx = 64 * ((int)w + 4 * i) + l, sg = idx / 72, ch = idx - 72 * sg, gg = sg >> 3, j = sg & 7;
        int row;
        if (kb < 16) { const int head = 2 * kb + (gg >> 1), h = gg & 1; row = 18 * head + (j < 4 ? 4 * h + j : 8 + 4 * h + (j - 4)); }
        else if (kb < 18) row = 18 * (16 * (kb - 16) + 4 * gg + (j >> 1)) + 16 + (j & 1);
        else row = 32 * (kb - 18) + (j < 4 ? 4 * gg + j : 16 + 4 * gg + (j - 4));
        dma_now(srd, (unsigned)(row * 1152 + ch * 16), 0u, c.lds0 + (w + 4 * i) * 1024);
    }
    WAIT_BAR0();
    const int q = (l & 15) >> 2, p = l & 3;
    const unsigned r0 = c.lds0 + (unsigned)((8 * g + q) * 1152 + p * 8);
#pragma unroll
    for (int i = 0; i < 9; ++i) {
        const int db = (int)w + 4 * i;
        st128(c, WS_WC + (unsigned)((db * 90 + kb) * 1024 + l * 16), __builtin_bit_cast(u32x4, cat8(lds_rdtr(r0 + db * 32), lds_rdtr(r0 + 4 * 1152 + db * 32))));
    }
    publish(c, FL_WC + kb);
}

DI void prep_gate(const Ctx& c, int n, unsigned flag) {
    const int tid = threadIdx.x, l = tid & 63; const int w = tid >> 6;
    const int row = n * 32 + w * 8 + (l >> 3);
    u32x4 xv[9], gv[9];
#pragma unroll
    for (int i = 0; i < 9; ++i) { xv[i] = *(const u32x4*)(c.P.x + row * 576 + ((l & 7) + 8 * i) * 8); gv[i] = *(const u32x4*)(c.P.gatew + ((l & 7) + 8 * i) * 8); }
    float a = 0.f;
#pragma unroll
    for (int i = 0; i < 9; ++i)
#pragma unroll
        for (int e = 0; e < 4; ++e) { a = __builtin_fmaf(bflo(xv[i][e]), bflo(gv[i][e]), a); a = __builtin_fmaf(bfhi(xv[i][e]), bfhi(gv[i][e]), a); }
    a += __shfl_xor(a, 1); a += __shfl_xor(a, 2); a += __shfl_xor(a, 4);
    if ((l & 7) == 0) st32(c, WS_GT + row * 4, __float_as_uint(1.0f / (1.0f + __expf(-a))));
    publish(c, FL_WC + 90 + n);
}

DI void warm_l2(const Ctx& c, int x, int rank) {
    const int tid = threadIdx.x; const unsigned w = rfl(tid >> 6);
    const u32x4 sx = make_srd(c.P.x, S_ * D_ * 2), sw = make_srd(c.P.win, D_ * 4608 * 2), sq = make_srd(c.P.wqkv, D_ * 1728 * 2);
    const unsigned dst = c.lds0 + 73728 + w * 256;
    auto touch = [&](u32x4 srd, unsigned off) { asm volatile("s_mov_b32 m0, %0\n\ts_nop 0\n\tbuffer_load_dword %1, %2, 0 offen lds" ::"s"(dst), "v"(off), "s"(srd) : "memory"); };
    const int i = rank * 256 + tid;
    if (w < 2) {
        const int j = rank * 128 + tid;
        { const int row = j / 18, k = j - row * 18; touch(sw, (unsigned)(row * 9216 + (k < 9 ? 0 : 4608) + (x & 3) * 1152 + (k < 9 ? k : k - 9) * 128)); }
        { const int j2 = j + 8192, row = j2 / 18, k = j2 - row * 18; touch(sw, j2 < 10368 ? (unsigned)(row * 9216 + (k < 9 ? 0 : 4608) + (x & 3) * 1152 + (k < 9 ? k : k - 9) * 128) : OOB); }
    } else {
        const int j = rank * 128 + (tid - 128);
        touch(sx, (unsigned)((x >> 2) * 1024 * 1152 + j * 128));
        { const int row = j / 9, k = j - row * 9, ty = k / 3, ln = k - 3 * ty;
          touch(sq, j < 5184 ? (unsigned)(row * 3456 + ty * 1152 + (((x & 3) * 288) & ~127) + ln * 128) : OOB);
        }
        touch(sx, j < 1024 ? (unsigned)((x >> 2) * 1024 * 1152 + (8192 + j) * 128) : OOB);
    }
    (void)i;
}

DI void warm_z1(const Ctx& c, int x, int rank) {
    const int tid = threadIdx.x; const unsigned w = rfl(tid >> 6);
    const u32x4 sw = make_srd(c.P.win, D_ * 4608 * 2);
    const unsigned dst = c.lds0 + 73728 + w * 256;
    if (w < 2) {
        const int j = rank * 81 + tid, row = j / 9, k = j - row * 9;
        const unsigned off = (tid < 81) ? (unsigned)(row * 9216 + (x & 3) * 1152 + k * 128) : OOB;
        asm volatile("s_mov_b32 m0, %0\n\ts_nop 0\n\tbuffer_load_dword %1, %2, 0 offen lds" ::"s"(dst), "v"(off), "s"(sw) : "memory");
    }
}

constexpr int AB_SLOT = 20 * 1024;
struct AttnState {
    f32x16 S[2], O[2]; u32x4 pl[2], ph[2];
    bf16x8 k0, v0, v1; u32x4 k1; u32x4 q0[2], q1[2];
};
#define AT_PRIO 0
#define AT_HI() do {} while (0)
#define AT_LO() do {} while (0)
template <int T, class F>
DI void attn_slot(AttnState& s, unsigned aK, unsigned aR, unsigned aV, const f32x16& zero, F&& hook) {
    constexpr int U = T ^ 1;
    AT_HI(); s.S[U] = MFMA32(s.k0, BC8(s.q0[U]), zero); SB(); AT_LO();
    SB();
#pragma unroll
    for (int i = 0; i < 4; ++i) s.S[T][i] = EX2(s.S[T][i]);
    SB();
    AT_HI(); s.S[U] = MFMA32(BC8(s.k1), BC8(s.q1[U]), s.S[U]); SB(); AT_LO();
    SB();
    if (T == 0) { s.k0 = lds_rd128(aK); s.k1[0] = lds_rd32(aR); }
#pragma unroll
    for (int i = 4; i < 8; ++i) s.S[T][i] = EX2(s.S[T][i]);
    s.pl[T][0] = pk2(s.S[T][0], s.S[T][1]); s.pl[T][1] = pk2(s.S[T][2], s.S[T][3]);
    SB();
    AT_HI(); s.O[U] = MFMA32(s.v1, BC8(s.ph[U]), s.O[U]); SB(); AT_LO();
    SB();
    if (T == 0) s.v1 = lds_rd128(aV);
    hook();
    s.pl[T][2] = pk2(s.S[T][4], s.S[T][5]); s.pl[T][3] = pk2(s.S[T][6], s.S[T][7]);
    SB();
#pragma unroll
    for (int i = 8; i < 12; ++i) s.S[T][i] = EX2(s.S[T][i]);
    SB();
    AT_HI(); s.O[T] = MFMA32(s.v0, BC8(s.pl[T]), s.O[T]); SB(); AT_LO();
    SB();
    if (T == 1) s.v0 = lds_rd128(aV);
#pragma unroll
    for (int i = 12; i < 16; ++i) s.S[T][i] = EX2(s.S[T][i]);
    SB();
    s.ph[T][0] = pk2(s.S[T][8], s.S[T][9]); s.ph[T][1] = pk2(s.S[T][10], s.S[T][11]);
    s.ph[T][2] = pk2(s.S[T][12], s.S[T][13]); s.ph[T][3] = pk2(s.S[T][14], s.S[T][15]);
    SB();
}

DI void attn_unit(const Ctx& c, int head, int qb) {
    const int tid = threadIdx.x, l = tid & 63; const unsigned w = rfl(tid >> 6); const int h = l >> 5, r32 = l & 31; const unsigned lds0 = c.lds0;
    {
        const int hq = head >> 3;
        for (int d = 0; d < QKV_PRESLEEP; ++d) __builtin_amdgcn_s_sleep(32);
        wait_flags(c, tid < 16 ? FL_QKV + (4 + hq) * 16 + tid : tid < 32 ? FL_QKV + (8 + hq) * 16 + (tid - 16) : tid < 34 ? FL_QKV + hq * 16 + 2 * qb + (tid - 32) : ~0u);
    }
    STAMPV(c.P, 2);
    const u32x4 srd = make_srd(c.P.ws + WS_KM, WS_QM - WS_KM);
    unsigned vl = (unsigned)l * 16; OPAQUE(vl);
    unsigned sLds[5], sSrc[5], sStep[5];
#pragma unroll
    for (int i = 0; i < 5; ++i) {
        const unsigned p = w + 4 * i;
        sLds[i] = lds0 + p * 1024;
        sSrc[i] = p < 8 ? (unsigned)(head * 65536) + p * 1024 : p < 18 ? (WS_VT - WS_KM) + (unsigned)(head * 81920) + (p - 8) * 1024
                : p == 18 ? (WS_KR - WS_KM) + (unsigned)(head * 8192) : OOB;
        sStep[i] = p < 8 ? 8192u : (p < 18 ? 10240u : (p == 18 ? 1024u : 0u));
    }
    auto piece = [&](int i, int ch, unsigned boff) { set_m0s(sLds[i], rfl(boff)); const unsigned so = ch < 8 ? sSrc[i] + (unsigned)ch * sStep[i] : OOB; asm volatile("s_nop 0"); dma_go_c(srd, vl, so); };
    auto issue = [&](int ch, unsigned boff) {
#pragma unroll
        for (int i = 0; i < 5; ++i) piece(i, ch, boff); };
    AttnState s;
    const int tok0 = qb * 256 + 64 * (int)w;
    issue(0, 0);
#pragma unroll
    for (int t = 0; t < 2; ++t) {
        s.q0[t] = __builtin_amdgcn_raw_buffer_load_b128(c.ws, (int)(WS_QM + ((unsigned)head * 64 + (tok0 >> 5) + t) * 1024 + l * 16), 0, 16);
        const unsigned qr = __builtin_amdgcn_raw_buffer_load_b32(c.ws, (int)(WS_QR + ((unsigned)head * 2048 + tok0 + 32 * t + r32) * 4), 0, 16);
        s.q1[t] = u32x4{h ? 0u : qr, 0u, 0u, 0u};
    }
    asm volatile("" : "+v"(s.q0[0]), "+v"(s.q0[1]), "+v"(s.q1[0]), "+v"(s.q1[1]));
    unsigned ka = lds0 + (unsigned)l * 16, ra = lds0 + 18432u + (unsigned)r32 * 4, va = lds0 + 8192u + (unsigned)(h * 20 + (r32 < 19 ? r32 : 19)) * 16;
    OPAQUE(ka); OPAQUE(ra); OPAQUE(va);
    const f32x16 zero = {};
    volatile LAS unsigned* flag = (volatile LAS unsigned*)(uintptr_t)(lds0 + 2 * AB_SLOT);

    for (int attempt = 0; attempt < 2; ++attempt) {
        float mx[2] = {-3.0e38f, -3.0e38f};
        {
            const u32x4 qz[2] = {u32x4{s.q1[0][0], 0u, 0u, 0u}, u32x4{s.q1[1][0], 0u, 0u, 0u}};
            const int nch = attempt ? 8 : 1, nblk = attempt ? 8 : 1;
            for (int ch = 0; ch < nch; ++ch) {
                if (attempt) { WAIT_BAR0(); issue(ch, 0); }
                WAIT_BAR0();
                for (int kb = 0; kb < nblk; ++kb) {
                    const bf16x8 k0 = lds_rd128(ka + kb * 1024);
                    const u32x4 k1 = u32x4{lds_rd32(ra + kb * 128), 0u, 0u, 0u};
#pragma unroll
                    for (int t = 0; t < 2; ++t) {
                        f32x16 sc = MFMA32(k0, BC8(s.q0[t]), zero); sc = MFMA32(BC8(k1), BC8(qz[t]), sc);
#pragma unroll
                        for (int r = 0; r < 16; ++r) mx[t] = __builtin_fmaxf(mx[t], sc[r]);
                    }
                }
            }
            if (attempt) { WAIT_BAR0(); issue(0, 0); WAIT_BAR0(); }
        }
#pragma unroll
        for (int t = 0; t < 2; ++t) {
            auto rr = __builtin_amdgcn_permlane32_swap(__float_as_uint(mx[t]), __float_as_uint(mx[t]), false, false);
            const float m = __builtin_fmaxf(__uint_as_float(rr[0]), __uint_as_float(rr[1]));
            const unsigned nb = __float_as_uint(-m), hi = nb & 0xffff0000u;
            const float r1 = -m - __uint_as_float(hi); const unsigned mid = __float_as_uint(r1) & 0xffff0000u;
            const float r2 = r1 - __uint_as_float(mid); const unsigned lo = __float_as_uint(r2) & 0xffff0000u;
            s.q1[t][1] = h ? 0u : ((hi >> 16) | mid); s.q1[t][2] = h ? 0u : (lo >> 16);
        }
        STAMPV(c.P, 3);
        s.O[0] = zero; s.O[1] = zero;
        s.k0 = lds_rd128(ka); s.k1 = u32x4{lds_rd32(ra), 0x3f803f80u, 0x00003f80u, 0u};
        s.v0 = lds_rd128(va); s.v1 = s.v0; s.ph[1] = u32x4{0u, 0u, 0u, 0u};
        s.S[0] = MFMA32(s.k0, BC8(s.q0[0]), zero); s.S[0] = MFMA32(BC8(s.k1), BC8(s.q1[0]), s.S[0]);
        unsigned cur = 0, nxt = AB_SLOT;
        auto nohook = [] {};
        for (int ch = 0; ch < 8; ++ch) {
            const unsigned cK = ka + cur, cR = ra + cur, cV = va + cur, nK = ka + nxt, nR = ra + nxt, nV = va + nxt;
#pragma unroll
            for (int j = 0; j < 7; ++j) {
                attn_slot<0>(s, cK + (j + 1) * 1024, cR + (j + 1) * 128, cV + j * 1280 + 640, zero, nohook);
                attn_slot<1>(s, 0, 0, cV + (j + 1) * 1280, zero, [&] { if (j < 5) piece(j, ch + 1, nxt); });
            }
            asm volatile("s_waitcnt vmcnt(0)\n\ts_barrier" ::: "memory");
            SB();
            attn_slot<0>(s, nK, nR, cV + 7 * 1280 + 640, zero, nohook);
            attn_slot<1>(s, 0, 0, nV, zero, [&] { asm volatile("s_barrier" ::: "memory"); });
            const unsigned t_ = cur; cur = nxt; nxt = t_;
        }
        s.O[1] = MFMA32(s.v1, BC8(s.ph[1]), s.O[1]);
        if (attempt == 1) break;
        bool bad = false;
#pragma unroll
        for (int t = 0; t < 2; ++t) {
            float am = 0.f; bool b = false;
#pragma unroll
            for (int r = 0; r < 11; ++r) { am = __builtin_fmaxf(am, __builtin_fabsf(s.O[t][r])); b = b || (s.O[t][r] != s.O[t][r]); }
            bad = bad || b || !(am < 1.0e30f);
        }
        if (tid == 0) *flag = 0u;
        WAIT_BAR0();
        if (__any(bad)) *flag = 1u;
        WAIT_BAR0();
        if (rfl(*flag) == 0u) break;
    }
    STAMPV(c.P, 4);
#pragma unroll
    for (int t = 0; t < 2; ++t) {
        auto rr = __builtin_amdgcn_permlane32_swap(__float_as_uint(s.O[t][10]), __float_as_uint(s.O[t][10]), false, false);
        const float inv = 1.0f / __uint_as_float(rr[0]);
        const int tok = tok0 + 32 * t + r32, tb = tok >> 4;
        const unsigned base = WS_HC + (unsigned)(tb * 90 * 1024);
        const u32x4 m = {pk2(s.O[t][0] * inv, s.O[t][1] * inv), pk2(s.O[t][2] * inv, s.O[t][3] * inv), pk2(s.O[t][4] * inv, s.O[t][5] * inv), pk2(s.O[t][6] * inv, s.O[t][7] * inv)};
        st128(c, base + (unsigned)((head >> 1) * 1024 + ((tok & 15) + 16 * (2 * (head & 1) + h)) * 16), m);
        if (h == 0) st32(c, base + (unsigned)((16 + (head >> 4)) * 1024 + ((tok & 15) + 16 * ((head & 15) >> 2)) * 16 + (head & 3) * 4), pk2(s.O[t][8] * inv, s.O[t][9] * inv));
    }
    publish(c, FL_O + qb * 32 + head);
}

constexpr int SD_N = 8, SD_SLOT = 9216;
struct SpdRegs { bf16x8 zf[9]; u32x4 t[4][2]; unsigned vZ[3], vz, vT; };
template <int U>
DI void spd_sub(f32x4 (&acc)[9][2], SpdRegs& r, u32x4 srdT, u32x4 srdZ, unsigned sWZ, unsigned w, unsigned sTa, unsigned sTb, unsigned sZ) {
    constexpr int NX = (U + 1) & (SD_N - 1), PF = (U + SD_N - 1) & (SD_N - 1), TU = U & 3, TP = (U + 3) & 3;
    asm volatile("s_waitcnt vmcnt(10)\n\ts_barrier" : "+v"(r.t[TU][0]), "+v"(r.t[TU][1])::"memory");
    SB();
#pragma unroll
    for (int n = 0; n < 9; ++n) {
        if (n == 0) LGKM(4);
        if (n == 5) LGKM(5);
        if (n == 4 || n == 6) set_m0(sWZ, PF * SD_SLOT + (n == 4 ? 0 : 4096));
        mfma16v(acc[n][0], r.zf[n], BC8(r.t[TU][0]));
        if (n == 0) ld_frag<0>(r.t[TP][0], srdT, r.vT, sTa);
        if (n == 2) ld_frag<0>(r.t[TP][1], srdT, r.vT, sTb);
        if (n == 4) dma_go_c(srdZ, r.vZ[0], sZ);
        if (n == 6) dma_go_c(srdZ, r.vZ[1], sZ);
        mfma16v(acc[n][1], r.zf[n], BC8(r.t[TU][1]));
        r.zf[n] = lds_rd128(r.vz + NX * SD_SLOT + n * 1024);
        SB();
    }
    if (w == 0) { set_m0(sWZ, PF * SD_SLOT + 8192); asm volatile("s_nop 0"); dma_go_c(srdZ, r.vZ[2], sZ); }
    SB();
}

DI void spatial_tile(const Ctx& c, int panel, int jt, int u0, const u32x4 (&z1p)[9]) {
    const int tid = threadIdx.x, l = tid & 63; const unsigned w = rfl(tid >> 6); const unsigned lds0 = c.lds0;
    STAMPV(c.P, 4);
    const u32x4 srdT = make_srd(c.P.ws + WS_WP + (size_t)(panel * 8 + 2 * w) * 65536, 2 * 65536), srdZ = make_srd(c.P.ws + WS_Z2 + (size_t)jt * 9 * 65536, 9 * 65536);
    SpdRegs r;
#pragma unroll
    for (int i = 0; i < 3; ++i) { r.vZ[i] = (w + 4 * i) * 65536 + l * 16; OPAQUE(r.vZ[i]); }
    r.vz = lds0 + l * 16; OPAQUE(r.vz);
    r.vT = l * 16; OPAQUE(r.vT);
    const unsigned sWZ = lds0 + w * 1024;
#define SO(v) ((v) < 64 ? (unsigned)(((v) + u0) & 63) * 1024u : OOB)
#define SO2(v) ((v) < 64 ? (unsigned)(((v) + u0) & 63) * 1024u + 65536u : OOB)
    f32x4 acc[9][2];
#pragma unroll
    for (int n = 0; n < 9; ++n) { acc[n][0] = f32x4{0, 0, 0, 0}; acc[n][1] = f32x4{0, 0, 0, 0}; }
    PIN18(acc, "s_nop 1");
#pragma unroll
    for (int u = 0; u < SD_N - 1; ++u) {
        if (u >= 4) { ld_frag<0>(r.t[u - 4][0], srdT, r.vT, SO(u - 4)); ld_frag<0>(r.t[u - 4][1], srdT, r.vT, SO2(u - 4)); }
#pragma unroll
        for (int i = 0; i < 2; ++i) { set_m0(sWZ, u * SD_SLOT + i * 4096); asm volatile("s_nop 0"); dma_go_c(srdZ, r.vZ[i], SO(u)); }
        if (w == 0) { set_m0(sWZ, u * SD_SLOT + 8192); asm volatile("s_nop 0"); dma_go_c(srdZ, r.vZ[2], SO(u)); }
    }
    asm volatile("s_waitcnt vmcnt(18)\n\ts_barrier" ::: "memory");
#pragma unroll
    for (int n = 0; n < 9; ++n) r.zf[n] = lds_rd128(r.vz + n * 1024);
    LGKM(0);
    for (int u = 0; u < 64; u += 8) {
        spd_sub<0>(acc, r, srdT, srdZ, sWZ, w, SO(u + 3), SO2(u + 3), SO(u + 7));
        spd_sub<1>(acc, r, srdT, srdZ, sWZ, w, SO(u + 4), SO2(u + 4), SO(u + 8));
        spd_sub<2>(acc, r, srdT, srdZ, sWZ, w, SO(u + 5), SO2(u + 5), SO(u + 9));
        spd_sub<3>(acc, r, srdT, srdZ, sWZ, w, SO(u + 6), SO2(u + 6), SO(u + 10));
        spd_sub<4>(acc, r, srdT, srdZ, sWZ, w, SO(u + 7), SO2(u + 7), SO(u + 11));
        spd_sub<5>(acc, r, srdT, srdZ, sWZ, w, SO(u + 8), SO2(u + 8), SO(u + 12));
        spd_sub<6>(acc, r, srdT, srdZ, sWZ, w, SO(u + 9), SO2(u + 9), SO(u + 13));
        spd_sub<7>(acc, r, srdT, srdZ, sWZ, w, SO(u + 10), SO2(u + 10), SO(u + 14));
    }
    asm volatile("s_waitcnt vmcnt(0) lgkmcnt(0)" : "+v"(r.t[0][0]), "+v"(r.t[0][1]), "+v"(r.t[1][0]), "+v"(r.t[1][1]), "+v"(r.t[2][0]), "+v"(r.t[2][1]), "+v"(r.t[3][0]), "+v"(r.t[3][1])::"memory");
    PIN18(acc, "s_nop 7\n\ts_nop 1");
    STAMPV(c.P, 5);
    float bs[2];
#pragma unroll
    for (int tb = 0; tb < 2; ++tb) bs[tb] = bf2f(c.P.bsp[panel * 128 + 32 * w + 16 * tb + (l & 15)]);
    unsigned hp[9][2][2];
#pragma unroll
    for (int n = 0; n < 9; ++n)
#pragma unroll
        for (int tb = 0; tb < 2; ++tb) {
            hp[n][tb][0] = pk2(bflo(z1p[n][2 * tb]) * (acc[n][tb][0] + bs[tb]), bfhi(z1p[n][2 * tb]) * (acc[n][tb][1] + bs[tb]));
            hp[n][tb][1] = pk2(bflo(z1p[n][2 * tb + 1]) * (acc[n][tb][2] + bs[tb]), bfhi(z1p[n][2 * tb + 1]) * (acc[n][tb][3] + bs[tb]));
        }
    const int fb0 = jt * 9;
#pragma unroll
    for (int tb = 0; tb < 2; ++tb) {
        const unsigned base = WS_HC + (unsigned)(((panel * 8 + 2 * w + tb) * 90 + 18) * 1024 + l * 16);
        if ((jt & 1) == 0) {
#pragma unroll
            for (int n = 0; n < 8; n += 2) st128(c, base + (unsigned)(((fb0 + n) >> 1) * 1024), u32x4{hp[n][tb][0], hp[n][tb][1], hp[n + 1][tb][0], hp[n + 1][tb][1]});
            st64(c, base + (unsigned)(((fb0 + 8) >> 1) * 1024), u32x2{hp[8][tb][0], hp[8][tb][1]});
        } else {
            st64(c, base + (unsigned)((fb0 >> 1) * 1024 + 8), u32x2{hp[0][tb][0], hp[0][tb][1]});
#pragma unroll
            for (int n = 1; n < 9; n += 2) st128(c, base + (unsigned)(((fb0 + n) >> 1) * 1024), u32x4{hp[n][tb][0], hp[n][tb][1], hp[n + 1][tb][0], hp[n + 1][tb][1]});
        }
    }
    STAMPV(c.P, 15);
}

constexpr int FN_STAGE = 16 * 1024;
struct FnRegs { bf16x8 wf[5]; u32x4 h[4][3]; unsigned vW[4], vrd, vl; };
template <int U, int VM>
DI void fn_stage(f32x4 (&acc)[5], FnRegs& r, u32x4 srdW, u32x4 srdH, unsigned sWW, unsigned sNext) {
    constexpr int PF = (U + 3) & 3;
    asm volatile("s_waitcnt vmcnt(%3)\n\ts_barrier" : "+v"(r.h[U][0]), "+v"(r.h[U][1]), "+v"(r.h[U][2]) : "n"(VM) : "memory");
    SB();
#pragma unroll
    for (int n = 0; n < 5; ++n) r.wf[n] = lds_rd128(r.vrd + U * FN_STAGE + (n * 3) * 1024);
    ld_frag<0>(r.h[PF][0], srdH, r.vl, sNext); ld_frag<1024>(r.h[PF][1], srdH, r.vl, sNext); ld_frag<2048>(r.h[PF][2], srdH, r.vl, sNext);
    SB();
#pragma unroll
    for (int kk = 0; kk < 3; ++kk) {
#pragma unroll
        for (int n = 0; n < 5; ++n) {
            const int i = kk * 5 + n;
            const bool dm = (i % 4 == 1);
            if (dm) set_m0(sWW, PF * FN_STAGE + (i / 4) * 4096);
            mfma16v(acc[n], r.wf[n], BC8(r.h[U][kk]));
            if (dm) dma_go_c(srdW, r.vW[i / 4], sNext);
            if (kk + 1 < 3) r.wf[n] = lds_rd128(r.vrd + U * FN_STAGE + (n * 3 + kk + 1) * 1024);
            SB();
        }
    }
}
DI void final_tile(const Ctx& c, int tt, int j, unsigned hflag, int rot) {
    const int tid = threadIdx.x, l = tid & 63; const unsigned w = rfl(tid >> 6); const int g = l >> 4;
    const int db0 = (9 * j) >> 1;
    const u32x4 srdW = make_srd(c.P.ws + WS_WC + (size_t)db0 * 90 * 1024, 5 * 90 * 1024);
    const u32x4 srdH = make_srd(c.P.ws + WS_HC + (size_t)(tt * 4 + w) * 90 * 1024, 90 * 1024);
    FnRegs r;
    r.vl = (unsigned)l * 16; OPAQUE(r.vl);
#pragma unroll
    for (int i = 0; i < 4; ++i) { const unsigned p = w + 4 * i, db = p / 3, kk = p - db * 3; r.vW[i] = p < 15 ? (db * 90 + kk) * 1024 + l * 16 : OOB; OPAQUE(r.vW[i]); }
    r.vrd = c.lds0 + l * 16; OPAQUE(r.vrd);
    const unsigned sWW = c.lds0 + w * 1024;
#define FO(s) ((s) < 24 ? (18u + 3u * (unsigned)((s) + rot >= 24 ? (s) + rot - 24 : (s) + rot)) * 1024u : (s) < 30 ? (unsigned)((s) - 24) * 3072u : OOB)
    asm volatile("s_waitcnt vmcnt(0) lgkmcnt(0)\n\ts_barrier" ::: "memory");
    raise_flag(c, hflag);
#pragma unroll
    for (int s = 0; s < 3; ++s)
#pragma unroll
        for (int i = 0; i < 4; ++i) { set_m0(sWW, s * FN_STAGE + i * 4096); asm volatile("s_nop 0"); dma_go_c(srdW, r.vW[i], FO(s)); }
    wait_begin(c, tid < 16 ? FL_H + (tt >> 1) * 16 + tid : ~0u);
    asm volatile("s_waitcnt lgkmcnt(0)\n\ts_barrier" ::: "memory");
    STAMPV(c.P, 6);
    f32x4 accA[5], accB[5];
#pragma unroll
    for (int n = 0; n < 5; ++n) { accA[n] = f32x4{0, 0, 0, 0}; accB[n] = f32x4{0, 0, 0, 0}; }
    PIN5(accA, "s_nop 1"); PIN5(accB, "s_nop 1");
#pragma unroll
    for (int s = 0; s < 3; ++s) { ld_frag<0>(r.h[s][0], srdH, r.vl, FO(s)); ld_frag<1024>(r.h[s][1], srdH, r.vl, FO(s)); ld_frag<2048>(r.h[s][2], srdH, r.vl, FO(s)); }
    asm volatile("s_waitcnt vmcnt(6)" : "+v"(r.h[0][0]), "+v"(r.h[0][1]), "+v"(r.h[0][2])::"memory");
    fn_stage<0, 63>(accB, r, srdW, srdH, sWW, FO(3));
    asm volatile("s_waitcnt vmcnt(10)" : "+v"(r.h[1][0]), "+v"(r.h[1][1]), "+v"(r.h[1][2])::"memory");
    fn_stage<1, 63>(accB, r, srdW, srdH, sWW, FO(4));
    fn_stage<2, 14>(accB, r, srdW, srdH, sWW, FO(5));
    fn_stage<3, 14>(accB, r, srdW, srdH, sWW, FO(6));
    for (int s = 4; s < 20; s += 4) {
        fn_stage<0, 14>(accB, r, srdW, srdH, sWW, FO(s + 3)); fn_stage<1, 14>(accB, r, srdW, srdH, sWW, FO(s + 4));
        fn_stage<2, 14>(accB, r, srdW, srdH, sWW, FO(s + 5)); fn_stage<3, 14>(accB, r, srdW, srdH, sWW, FO(s + 6));
    }
    fn_stage<0, 14>(accB, r, srdW, srdH, sWW, FO(23));
    STAMPV(c.P, 7);
    if (tid < 64) { poll_flags(c, tid < 32 ? FL_O + (tt >> 2) * 32 + tid : ~0u); ACQUIRE(); }
    STAMPV(c.P, 8);
    fn_stage<1, 14>(accB, r, srdW, srdH, sWW, FO(24));
    fn_stage<2, 14>(accB, r, srdW, srdH, sWW, FO(25));
    fn_stage<3, 14>(accB, r, srdW, srdH, sWW, FO(26));
    fn_stage<0, 14>(accA, r, srdW, srdH, sWW, FO(27)); fn_stage<1, 14>(accA, r, srdW, srdH, sWW, FO(28));
    fn_stage<2, 14>(accA, r, srdW, srdH, sWW, FO(29)); fn_stage<3, 14>(accA, r, srdW, srdH, sWW, OOB);
    fn_stage<0, 14>(accA, r, srdW, srdH, sWW, OOB); fn_stage<1, 14>(accA, r, srdW, srdH, sWW, OOB);
    asm volatile("s_waitcnt vmcnt(0) lgkmcnt(0)" : "+v"(r.h[0][0]), "+v"(r.h[0][1]), "+v"(r.h[0][2]), "+v"(r.h[1][0]), "+v"(r.h[1][1]), "+v"(r.h[1][2]),
                 "+v"(r.h[2][0]), "+v"(r.h[2][1]), "+v"(r.h[2][2]), "+v"(r.h[3][0]), "+v"(r.h[3][1]), "+v"(r.h[3][2])::"memory");
    PIN5(accA, "s_nop 7\n\ts_nop 1"); PIN5(accB, "");
    const __amdgpu_buffer_rsrc_t ro = __builtin_amdgcn_make_buffer_rsrc((void*)c.P.out, (short)0, S_ * D_ * 2, 0x00020000);
    const int tok = tt * 64 + 16 * w + (l & 15);
    const float gt = __uint_as_float(__builtin_amdgcn_raw_buffer_load_b32(c.ws, (int)(WS_GT + tok * 4), 0, 16));
#pragma unroll
    for (int n = 0; n < 5; ++n) {
        const int d = (db0 + n) * 16 + 4 * g;
        if (d >= 72 * j && d < 72 * j + 72) {
            float v[4];
#pragma unroll
            for (int rr = 0; rr < 4; ++rr) v[rr] = gt * accA[n][rr] + (1.0f - gt) * accB[n][rr];
            __builtin_amdgcn_raw_buffer_store_b64(u32x2{pk2(v[0], v[1]), pk2(v[2], v[3])}, ro, (tok * 576 + d) * 2, 0, 0);
        }
    }
}

constexpr int MEGA_LDS = 73728 + 1024;
#define FROT 1
#define A_DELAY 0
#define SP_PRIO 2
#define A1_PRIO 1
#define B1_PRIO 1
#define SROT 0
#define SROT2 0
__global__ __launch_bounds__(256, 2) void mega_kernel(Params P) {
    extern __shared__ __attribute__((aligned(16))) char smem[];
    const int b = blockIdx.x, tid = threadIdx.x;
    STAMPV(P, 0);
    const u64 ep = __hip_atomic_load((gu64*)(P.ws + WS_EP), __ATOMIC_RELAXED, __HIP_MEMORY_SCOPE_AGENT);
    const u64 tag = ep + 1ull;
    const Ctx c{P, __builtin_amdgcn_make_buffer_rsrc(P.ws, (short)0, (int)WS_END, 0x00020000), tag, (unsigned)(uintptr_t)smem, smem};
    if (b < 256) {
        __builtin_amdgcn_s_setprio(A1_PRIO);
        const int x = b & 7, i = b >> 3, panel = (x >> 2) * 8 + (i & 7), jt = (x & 3) * 4 + (i >> 3);
        u32x4 z1p[9];
        tile_z(c, panel, jt, ROT ? ((i & 7) + 2 * (i >> 3)) % 9 : 0, z1p);
        __builtin_amdgcn_s_setprio(SP_PRIO);
        spatial_tile(c, panel, jt, ((i & 7) * SROT + (i >> 3) * SROT2) & 63, z1p);
        final_tile(c, 2 * panel + (jt >> 3), jt & 7, FL_H + panel * 16 + jt, FROT ? ((i & 7) * 2 + (i >> 3) * 1) % 21 : 0);
        STAMPV(P, 9);
        if (b == 0) {
            if (tid < 64) {
                for (unsigned spins = 0; spins < SPIN_CAP; ++spins) {
                    bool ok = true;
#pragma unroll
                    for (int k = 0; k < 4; ++k) {
                        ok = ok && __hip_atomic_load(flag_ptr(c, FL_H + 64 * k + tid), __ATOMIC_RELAXED, __HIP_MEMORY_SCOPE_AGENT) == tag;
                        ok = ok && __hip_atomic_load(flag_ptr(c, FL_O + 64 * k + tid), __ATOMIC_RELAXED, __HIP_MEMORY_SCOPE_AGENT) == tag;
                    }
                    if (__all(ok)) break;
                    __builtin_amdgcn_s_sleep(POLL_SLEEP);
                }
                if (tid == 0) __hip_atomic_store((gu64*)(P.ws + WS_EP), tag, __ATOMIC_RELAXED, __HIP_MEMORY_SCOPE_AGENT);
            }
        }
    } else {
        const int a = b - 256, x = a & 7, idx = a >> 3;
        __builtin_amdgcn_s_setprio(B1_PRIO);
        if (idx < 24) {
            const int hq = x & 3, ty = idx % 3, panel = (x >> 2) * 8 + idx / 3;
            const int s0 = ROT ? (idx / 3 + 3 * ty + 1) % 9 : 0;
            if (ty < 2) tile_qk(c, panel, ty, hq, s0); else tile_v(c, panel, hq, s0);
            const unsigned qf = FL_QKV + (ty * 4 + hq) * 16 + panel;
            STAMPV(P, 6);
            publish(c, qf);
            STAMPV(P, 7);
            { const int k = idx * 8 + x; if (k < 90) prep_wcat(c, k, ~0u); else if (k < 154) prep_gate(c, k - 90, ~0u); }

        } else {
            const int n = (idx - 24) * 8 + x;
            for (int d = 0; d < PREP_DELAY; ++d) __builtin_amdgcn_s_sleep(32);
            prep_wsp(c, n);
        }
        STAMPV(P, 1);
        __builtin_amdgcn_s_setprio(0);
        attn_unit(c, x * 4 + (idx & 3), idx >> 2);
        STAMPV(P, 5);
    }
}

extern "C" void kernel_launch(void* const* d_in, const int* in_sizes, int n_in, void* d_out, int out_size, void* d_ws, size_t ws_size, hipStream_t stream) {
    Params P;
    P.x = (const u16*)d_in[0]; P.wqkv = (const u16*)d_in[1]; P.wo = (const u16*)d_in[2]; P.win = (const u16*)d_in[4]; P.wsp = (const u16*)d_in[5];
    P.bsp = (const u16*)d_in[6]; P.wout = (const u16*)d_in[7]; P.gatew = (const u16*)d_in[8]; P.out = (u16*)d_out; P.ws = (char*)d_ws;
    hipFuncSetAttribute((const void*)mega_kernel, hipFuncAttributeMaxDynamicSharedMemorySize, MEGA_LDS);
    mega_kernel<<<512, 256, MEGA_LDS, stream>>>(P);
}
```

```cpp
#include <hip/hip_runtime.h>
#include <stdint.h>

typedef __bf16 bf16x8 __attribute__((ext_vector_type(8)));
typedef __bf16 bf16x2 __attribute__((ext_vector_type(2)));
typedef short s16x4 __attribute__((ext_vector_type(4)));
typedef float f32x2 __attribute__((ext_vector_type(2)));
typedef float f32x4 __attribute__((ext_vector_type(4)));
typedef float f32x16 __attribute__((ext_vector_type(16)));
typedef unsigned u32x2 __attribute__((ext_vector_type(2)));
typedef unsigned u32x4 __attribute__((ext_vector_type(4)));
typedef unsigned short u16;
typedef unsigned long long u64;
typedef __attribute__((address_space(1))) unsigned long long gu64;
#define LAS __attribute__((address_space(3)))
#define DI __device__ __forceinline__

constexpr int S_ = 2048, D_ = 576, FH = 2304;
constexpr unsigned WS_EP = 0;
constexpr unsigned WS_FL = 256;
constexpr unsigned FL_ST = 0, FL_Z = 256, FL_H = 512, FL_QKV = 768, FL_WP = 960, FL_O = 1024, FL_WC = 1280, FL_N = FL_WC + 154;
constexpr unsigned WS_GT = 16384;
constexpr unsigned WS_ST = WS_GT + 8192;
constexpr unsigned WS_KM = WS_ST + 16 * 16 * 1024;
constexpr unsigned WS_KR = WS_KM + 32 * 64 * 1024;
constexpr unsigned WS_VT = WS_KR + 32 * 2048 * 4;
constexpr unsigned WS_QM = WS_VT + 32 * 128 * 640;
constexpr unsigned WS_QR = WS_QM + 32 * 64 * 1024;
constexpr unsigned WS_Z2 = WS_QR + 32 * 2048 * 4;
constexpr unsigned WS_WP = WS_Z2 + 144 * 64 * 1024;
constexpr unsigned WS_HC = WS_WP + 128 * 64 * 1024;
constexpr unsigned WS_WC = WS_HC + 128 * 90 * 1024;
constexpr unsigned WS_END = WS_WC + 36 * 90 * 1024;

struct Params {
    const u16* x; const u16* wqkv; const u16* wo; const u16* win; const u16* wsp; const u16* bsp; const u16* wout; const u16* gatew;
    u16* out; char* ws;
};


DI float bf2f(u16 v) { return __uint_as_float((unsigned)v << 16); }
DI float bflo(unsigned v) { return __uint_as_float(v << 16); }
DI float bfhi(unsigned v) { return __uint_as_float(v & 0xffff0000u); }
DI unsigned pk2(float lo, float hi) { f32x2 v = {lo, hi}; bf16x2 b = __builtin_convertvector(v, bf16x2); return __builtin_bit_cast(unsigned, b); }
DI unsigned rfl(unsigned v) { return __builtin_amdgcn_readfirstlane(v); }
DI u32x4 make_srd(const void* p, unsigned bytes) {
    uint64_t a = (uint64_t)p;
    u32x4 r; r[0] = rfl((unsigned)a); r[1] = rfl((unsigned)(a >> 32) & 0xffffu); r[2] = rfl(bytes); r[3] = 0x00020000u; return r;
}
#define SB() __builtin_amdgcn_sched_barrier(0)
#define BAR() asm volatile("s_waitcnt lgkmcnt(0)\n\ts_barrier" ::: "memory")
#define WAIT_BAR0() asm volatile("s_waitcnt vmcnt(0) lgkmcnt(0)\n\ts_barrier" ::: "memory")
DI void set_m0(unsigned base, unsigned imm) { asm volatile("s_add_u32 m0, %0, %1" ::"s"(base), "n"(imm) : "scc"); }
DI void set_m0s(unsigned a, unsigned b) { asm volatile("s_add_u32 m0, %0, %1" ::"s"(a), "s"(b) : "scc"); }
DI void dma_go(u32x4 srd, unsigned voff, unsigned soff) { asm volatile("buffer_load_dwordx4 %0, %1, %2 offen lds" ::"v"(voff), "s"(srd), "s"(soff) : "memory"); }
DI void dma_go_c(u32x4 srd, unsigned voff, unsigned soff) { asm volatile("buffer_load_dwordx4 %0, %1, %2 offen sc1 lds" ::"v"(voff), "s"(srd), "s"(soff) : "memory"); }
DI void dma_now(u32x4 srd, unsigned voff, unsigned soff, unsigned ldsaddr) { asm volatile("s_mov_b32 m0, %0\n\ts_nop 0\n\tbuffer_load_dwordx4 %1, %2, %3 offen nt lds" ::"s"(ldsaddr), "v"(voff), "s"(srd), "s"(soff) : "memory"); }
template <int IMM> DI void ld_frag(u32x4& d, u32x4 srd, unsigned voff, unsigned soff) {
    asm volatile("buffer_load_dwordx4 %0, %1, %2, %3 offen offset:%4 sc1" : "=v"(d) : "v"(voff), "s"(srd), "s"(soff), "n"(IMM) : "memory");
}
DI bf16x8 lds_rd128(unsigned a) { return *(const LAS bf16x8*)(uintptr_t)a; }
DI unsigned lds_rd32(unsigned a) { return *(const LAS unsigned*)(uintptr_t)a; }
DI s16x4 lds_rdtr(unsigned a) { return __builtin_amdgcn_ds_read_tr16_b64_v4i16((LAS s16x4*)(uintptr_t)a); }
DI bf16x8 cat8(s16x4 a, s16x4 b) { typedef short s16x8 __attribute__((ext_vector_type(8))); s16x8 v = __builtin_shufflevector(a, b, 0, 1, 2, 3, 4, 5, 6, 7); return __builtin_bit_cast(bf16x8, v); }
DI void mfma16v(f32x4& c, bf16x8 a, bf16x8 b) { asm volatile("v_mfma_f32_16x16x32_bf16 %0, %1, %2, %0" : "+v"(c) : "v"(a), "v"(b)); }
#define PIN18(acc, txt) asm volatile(txt : "+v"(acc[0][0]), "+v"(acc[0][1]), "+v"(acc[1][0]), "+v"(acc[1][1]), "+v"(acc[2][0]), "+v"(acc[2][1]), "+v"(acc[3][0]), "+v"(acc[3][1]), "+v"(acc[4][0]), "+v"(acc[4][1]), \
    "+v"(acc[5][0]), "+v"(acc[5][1]), "+v"(acc[6][0]), "+v"(acc[6][1]), "+v"(acc[7][0]), "+v"(acc[7][1]), "+v"(acc[8][0]), "+v"(acc[8][1]))
#define PIN5(acc, txt) asm volatile(txt : "+v"(acc[0]), "+v"(acc[1]), "+v"(acc[2]), "+v"(acc[3]), "+v"(acc[4]))
#define LGKM(n) __builtin_amdgcn_s_waitcnt(0xC07F | ((n) << 8))
#define OPAQUE(x) asm volatile("" : "+v"(x))
constexpr unsigned OOB = 0x40000000u;
#define EX2(x) __builtin_amdgcn_exp2f(x)
#define BC8(x) __builtin_bit_cast(bf16x8, x)
#define MFMA32(a, b, c) __builtin_amdgcn_mfma_f32_32x32x16_bf16(a, b, c, 0, 0, 0)

DI float gelu_t(float x) {
    const float c0 = -2.0f * 0.7978845608028654f * 1.4426950408889634f, c1 = c0 * 0.044715f;
    const float e = __builtin_amdgcn_exp2f(x * __builtin_fmaf(x * x, c1, c0));
    return x * __builtin_amdgcn_rcpf(1.0f + e);
}
template <int N> DI float dpp_ror_add(float v) { return v + __builtin_bit_cast(float, __builtin_amdgcn_update_dpp(0, __builtin_bit_cast(int, v), 0x120 + N, 0xf, 0xf, false)); }
DI float row16_sum(float v) { v = dpp_ror_add<8>(v); v = dpp_ror_add<4>(v); v = dpp_ror_add<2>(v); v = dpp_ror_add<1>(v); return v; }

struct Ctx { const Params& P; __amdgpu_buffer_rsrc_t ws; u64 tag; unsigned lds0; char* smem; };
DI void st128(const Ctx& c, unsigned off, u32x4 v) { __builtin_amdgcn_raw_buffer_store_b128(v, c.ws, (int)off, 0, 16); }
DI void st64(const Ctx& c, unsigned off, u32x2 v) { __builtin_amdgcn_raw_buffer_store_b64(v, c.ws, (int)off, 0, 16); }
DI void st32(const Ctx& c, unsigned off, unsigned v) { __builtin_amdgcn_raw_buffer_store_b32(v, c.ws, (int)off, 0, 16); }
DI gu64* flag_ptr(const Ctx& c, unsigned idx) { return (gu64*)(c.P.ws + WS_FL) + idx; }
DI void raise_flag(const Ctx& c, unsigned idx) { if (threadIdx.x == 0) __hip_atomic_store(flag_ptr(c, idx), c.tag, __ATOMIC_RELAXED, __HIP_MEMORY_SCOPE_AGENT); }
DI void publish(const Ctx& c, unsigned idx) { WAIT_BAR0(); raise_flag(c, idx); }
constexpr unsigned SPIN_CAP = 1u << 16;
template <int SLEEP = 6>
DI void poll_flags(const Ctx& c, unsigned idx) {
    gu64* p = flag_ptr(c, idx == ~0u ? 0u : idx);
    for (unsigned spins = 0; spins < SPIN_CAP; ++spins) {
        const u64 v = idx == ~0u ? c.tag : __hip_atomic_load(p, __ATOMIC_RELAXED, __HIP_MEMORY_SCOPE_AGENT);
        if (__all(v == c.tag)) break;
        __builtin_amdgcn_s_sleep(SLEEP);
    }
}
template <int SLEEP = 6>
DI void wait_flags(const Ctx& c, unsigned idx) { if (threadIdx.x < 64) poll_flags<SLEEP>(c, idx); BAR(); }

constexpr int G1_XS = 16384, G1_WS = 9216, G1_WOFF = 2 * G1_XS, G1_LDS = G1_WOFF + 4 * G1_WS;
struct G1Regs { bf16x8 wf[9], xf[2][2]; unsigned vX[4], vW[3], vw0, vw1, vx[2]; };
template <bool TRANS> DI void g1_mm(f32x4& c, bf16x8 wf, bf16x8 xf) { if (TRANS) mfma16v(c, wf, xf); else mfma16v(c, xf, wf); }
DI bf16x8 g1_rdw(unsigned lo, unsigned hi, int off) { return cat8(lds_rdtr(lo + off), lds_rdtr(hi + off)); }

template <bool TRANS, int J>
DI void g1_sub(f32x4 (&acc)[9][2], G1Regs& r, u32x4 srdX, u32x4 srdW, unsigned sWX, unsigned sWW, unsigned w, unsigned sX, unsigned sW) {
    constexpr int N1 = (J + 1) & 3, CUR = J & 1, NXT = CUR ^ 1, PW = (J + 3) & 3;
    constexpr int XS_RD = N1 >> 1, XS_WR = J >> 1;
    if (J & 1) asm volatile("s_waitcnt vmcnt(2) lgkmcnt(8)\n\ts_barrier" ::: "memory");
    else asm volatile("s_waitcnt vmcnt(6) lgkmcnt(8)\n\ts_barrier" ::: "memory");
    LGKM(8);
    SB();
#pragma unroll
    for (int n = 0; n < 9; ++n) {
        if (n == 5) LGKM(12);
        const bool dw = (n == 1 || n == 3), dx = (J & 1) && n >= 4 && n < 8;
        if (dw) set_m0(sWW, PW * G1_WS + (n == 1 ? 0 : 4096));
        if (dx) set_m0(sWX, XS_WR * G1_XS + (n - 4) * 1024);
        g1_mm<TRANS>(acc[n][0], r.wf[n], r.xf[CUR][0]);
        if (dw) dma_go(srdW, r.vW[n == 1 ? 0 : 1], sW);
        if (dx) dma_go(srdX, r.vX[n - 4], sX);
        g1_mm<TRANS>(acc[n][1], r.wf[n], r.xf[CUR][1]);
        if (n < 2) r.xf[NXT][n] = lds_rd128(r.vx[N1 & 1] + XS_RD * G1_XS + n * 2048);
        r.wf[n] = g1_rdw(r.vw0, r.vw1, N1 * G1_WS + n * 32);
        SB();
    }
    if (w == 0) { set_m0(sWW, PW * G1_WS + 8192); asm volatile("s_nop 0"); dma_go(srdW, r.vW[2], sW); }
    SB();
}

template <bool TRANS>
DI void gemm1_main(const Ctx& c, const u16* x, const u16* W, int ldw, int row0, int col0, unsigned lds0, f32x4 (&acc)[9][2], int s0, unsigned deferred_flag = ~0u) {
    const int tid = threadIdx.x, l = tid & 63; const unsigned w = rfl(tid >> 6);
    const u32x4 srdX = make_srd(x, S_ * D_ * 2), srdW = make_srd(W, (unsigned)(D_ * ldw * 2));
    const int g = l >> 4, q = (l & 15) >> 2, p = l & 3;
    G1Regs r;
#pragma unroll
    for (int i = 0; i < 4; ++i) { r.vX[i] = (unsigned)((row0 + 32 * w + 8 * i + (l >> 3)) * (D_ * 2) + (((l & 7) ^ (l >> 3)) << 4)); OPAQUE(r.vX[i]); }
#pragma unroll
    for (int i = 0; i < 3; ++i) { const int pc = (int)w + 4 * i, idx = 64 * pc + l, rho = idx / 18, cc = idx - rho * 18, k = rho ^ (((rho >> 3) & 1) << 2);
        r.vW[i] = pc < 9 ? (unsigned)(k * ldw * 2 + (col0 + 8 * cc) * 2) : OOB; OPAQUE(r.vW[i]); }
    r.vw0 = lds0 + (unsigned)(G1_WOFF + (8 * g + (q ^ (4 * (g & 1)))) * 288 + p * 8); r.vw1 = lds0 + (unsigned)(G1_WOFF + (8 * g + ((q + 4) ^ (4 * (g & 1)))) * 288 + p * 8);
    r.vx[0] = lds0 + (unsigned)((32 * w + (l & 15)) * 128) + ((g ^ (l & 7)) << 4); r.vx[1] = lds0 + (unsigned)((32 * w + (l & 15)) * 128) + (((4 + g) ^ (l & 7)) << 4);
    OPAQUE(r.vw0); OPAQUE(r.vw1); OPAQUE(r.vx[0]); OPAQUE(r.vx[1]);
    const unsigned sWX = lds0 + w * 4096, sWW = lds0 + G1_WOFF + w * 1024, kst = (unsigned)(32 * ldw * 2);
    auto xs = [&](int slot, unsigned so) {
#pragma unroll
        for (int i = 0; i < 4; ++i) { set_m0(sWX, slot * G1_XS + i * 1024); asm volatile("s_nop 0"); dma_go(srdX, r.vX[i], so); } };
    auto wsl = [&](int slot, unsigned so) {
#pragma unroll
        for (int i = 0; i < 2; ++i) { set_m0(sWW, slot * G1_WS + i * 4096); asm volatile("s_nop 0"); dma_go(srdW, r.vW[i], so); }
        if (w == 0) { set_m0(sWW, slot * G1_WS + 8192); asm volatile("s_nop 0"); dma_go(srdW, r.vW[2], so); } };
    auto rot9 = [&](int s) { const int t = s + s0; return (unsigned)(t >= 9 ? t - 9 : t); };
#define GW(v) ((v) < 18 ? (rot9((v) >> 1) * 2 + ((v) & 1)) * kst : OOB)
#define GX(s) ((s) < 9 ? rot9(s) * 128u : OOB)
    xs(0, GX(0)); wsl(0, GW(0)); wsl(1, GW(1)); wsl(2, GW(2)); xs(1, GX(1));
#pragma unroll
    for (int n = 0; n < 9; ++n) { acc[n][0] = f32x4{0, 0, 0, 0}; acc[n][1] = f32x4{0, 0, 0, 0}; }
    PIN18(acc, "s_nop 1");
    if (deferred_flag != ~0u) publish(c, deferred_flag);
    else asm volatile("s_waitcnt vmcnt(8)\n\ts_barrier" ::: "memory");
#pragma unroll
    for (int tb = 0; tb < 2; ++tb) r.xf[0][tb] = lds_rd128(r.vx[0] + tb * 2048);
#pragma unroll
    for (int n = 0; n < 9; ++n) r.wf[n] = g1_rdw(r.vw0, r.vw1, n * 32);
    LGKM(0);
    for (int u = 0; u < 16; u += 4) {
        g1_sub<TRANS, 0>(acc, r, srdX, srdW, sWX, sWW, w, 0, GW(u + 3));
        g1_sub<TRANS, 1>(acc, r, srdX, srdW, sWX, sWW, w, GX(u / 2 + 2), GW(u + 4));
        g1_sub<TRANS, 2>(acc, r, srdX, srdW, sWX, sWW, w, 0, GW(u + 5));
        g1_sub<TRANS, 3>(acc, r, srdX, srdW, sWX, sWW, w, GX(u / 2 + 3), GW(u + 6));
    }
    g1_sub<TRANS, 0>(acc, r, srdX, srdW, sWX, sWW, w, 0, OOB);
    g1_sub<TRANS, 1>(acc, r, srdX, srdW, sWX, sWW, w, OOB, OOB);
    asm volatile("s_waitcnt vmcnt(0) lgkmcnt(0)\n\ts_barrier" ::: "memory");
    PIN18(acc, "s_nop 7\n\ts_nop 1");
}

DI void tile_z(const Ctx& c, int panel, int jt, int s0, u32x4 (&z1p)[9]) {
    const int tid = threadIdx.x, l = tid & 63; const unsigned w = rfl(tid >> 6); const int g = l >> 4;
    f32x4 y[9][2];
    gemm1_main<false>(c, c.P.x, c.P.win, 4608, panel * 128, FH + jt * 144, c.lds0, y, s0);
    {
        float s1[2][4], s2[2][4];
#pragma unroll
        for (int tb = 0; tb < 2; ++tb)
#pragma unroll
            for (int r = 0; r < 4; ++r) { s1[tb][r] = 0.f; s2[tb][r] = 0.f; }
#pragma unroll
        for (int n = 0; n < 9; ++n)
#pragma unroll
            for (int tb = 0; tb < 2; ++tb)
#pragma unroll
                for (int r = 0; r < 4; ++r) { const float v = gelu_t(y[n][tb][r]); y[n][tb][r] = v; s1[tb][r] += v; s2[tb][r] = __builtin_fmaf(v, v, s2[tb][r]); }
#pragma unroll
        for (int tb = 0; tb < 2; ++tb)
#pragma unroll
            for (int r = 0; r < 4; ++r) {
                const float a = row16_sum(s1[tb][r]), b = row16_sum(s2[tb][r]);
                if ((l & 15) == 0) st64(c, WS_ST + (unsigned)((panel * 16 + jt) * 1024 + (32 * w + 16 * tb + 4 * g + r) * 8), u32x2{__float_as_uint(a), __float_as_uint(b)});
            }
    }
    f32x4 acc[9][2];
    gemm1_main<true>(c, c.P.x, c.P.win, 4608, panel * 128, jt * 144, c.lds0, acc, s0, FL_ST + panel * 16 + jt);
    wait_flags(c, tid < 16 ? FL_ST + panel * 16 + tid : ~0u);
    u32x2 pv[8];
#pragma unroll
    for (int i = 0; i < 8; ++i) pv[i] = __builtin_amdgcn_raw_buffer_load_b64(c.ws, (int)(WS_ST + (panel * 16 + 8 * (l >> 5) + i) * 1024 + (32 * w + (l & 31)) * 8), 0, 16);
    SB();
#pragma unroll
    for (int n = 0; n < 9; ++n)
#pragma unroll
        for (int tb = 0; tb < 2; ++tb) { z1p[n][2 * tb] = pk2(gelu_t(acc[n][tb][0]), gelu_t(acc[n][tb][1])); z1p[n][2 * tb + 1] = pk2(gelu_t(acc[n][tb][2]), gelu_t(acc[n][tb][3])); }
    asm volatile("" : "+v"(z1p[0]), "+v"(z1p[1]), "+v"(z1p[2]), "+v"(z1p[3]), "+v"(z1p[4]), "+v"(z1p[5]), "+v"(z1p[6]), "+v"(z1p[7]), "+v"(z1p[8]));
    SB();
    {
        float a = 0.f, b = 0.f;
#pragma unroll
        for (int i = 0; i < 8; ++i) { a += __uint_as_float(pv[i][0]); b += __uint_as_float(pv[i][1]); }
        auto ra = __builtin_amdgcn_permlane32_swap(__float_as_uint(a), __float_as_uint(a), false, false);
        auto rb = __builtin_amdgcn_permlane32_swap(__float_as_uint(b), __float_as_uint(b), false, false);
        a = __uint_as_float(ra[0]) + __uint_as_float(ra[1]); b = __uint_as_float(rb[0]) + __uint_as_float(rb[1]);
        const float m = a * (1.0f / FH), var = __builtin_fmaxf(b * (1.0f / FH) - m * m, 0.f);
        const float rs = 1.0f / __builtin_sqrtf(var + 1e-5f);
        if (l < 32) *(f32x2*)(c.smem + (w * 32 + l) * 8) = f32x2{m, rs};
        asm volatile("s_waitcnt lgkmcnt(0)" ::: "memory");
    }
    f32x2 ms[2][4];
#pragma unroll
    for (int tb = 0; tb < 2; ++tb)
#pragma unroll
        for (int r = 0; r < 4; ++r) ms[tb][r] = *(const f32x2*)(c.smem + (w * 32 + 16 * tb + 4 * g + r) * 8);
    const unsigned dst = WS_Z2 + (unsigned)(((jt * 9) * 64 + panel * 4 + w) * 1024 + l * 16);
#pragma unroll
    for (int n = 0; n < 9; ++n) {
        unsigned pk[2][2];
#pragma unroll
        for (int tb = 0; tb < 2; ++tb) {
            float v[4];
#pragma unroll
            for (int r = 0; r < 4; ++r) v[r] = (y[n][tb][r] - ms[tb][r][0]) * ms[tb][r][1];
            pk[tb][0] = pk2(v[0], v[1]); pk[tb][1] = pk2(v[2], v[3]);
        }
        u32x4 o;
#pragma unroll
        for (int i = 0; i < 2; ++i) {
            auto a = __builtin_amdgcn_permlane32_swap(pk[0][i], pk[1][i], false, false);
            auto b = __builtin_amdgcn_permlane16_swap(a[0], a[1], false, false);
            o[i] = b[0]; o[2 + i] = b[1];
        }
        st128(c, dst + n * 65536, o);
    }
    publish(c, FL_Z + jt * 16 + panel);
    if (tid < 64) { poll_flags(c, FL_WC + tid); poll_flags(c, FL_WC + 64 + tid); poll_flags(c, tid < 26 ? FL_WC + 128 + tid : ~0u); }
    wait_flags(c, tid < 16 ? FL_Z + jt * 16 + tid : tid < 20 ? FL_WP + 4 * panel + (tid - 16) : ~0u);
}

DI void tile_qk(const Ctx& c, int panel, int which, int hq, int s0) {
    f32x4 acc[9][2];
    gemm1_main<true>(c, c.P.x, c.P.wqkv, 1728, panel * 128, which * 576 + hq * 144, c.lds0, acc, s0);
    const int l = threadIdx.x & 63; const unsigned w = rfl(threadIdx.x >> 6); const int g = l >> 4;
    const float sc = which == 0 ? 0.23570226039551584f * 1.4426950408889634f : 1.0f;
    char* st = c.smem + w * 9216;
#pragma unroll
    for (int n = 0; n < 9; ++n)
#pragma unroll
        for (int pp = 0; pp < 2; ++pp) {
            const int cc = 16 * n + 4 * g + 2 * pp, hl = cc / 18, d = cc - 18 * hl;
#pragma unroll
            for (int tb = 0; tb < 2; ++tb) {
                const int tok = 16 * tb + (l & 15);
                const int off = d < 16 ? hl * 1024 + (tok + 32 * (d >> 3)) * 16 + (d & 7) * 2 : 8192 + hl * 128 + tok * 4;
                *(unsigned*)(st + off) = pk2(acc[n][tb][2 * pp] * sc, acc[n][tb][2 * pp + 1] * sc);
            }
        }
    asm volatile("s_waitcnt lgkmcnt(0)" ::: "memory");
    const unsigned dm = (which == 0 ? WS_QM : WS_KM) + (unsigned)(((hq * 8) * 64 + panel * 4 + w) * 1024 + l * 16);
#pragma unroll
    for (int h = 0; h < 8; ++h) st128(c, dm + h * 65536, *(const u32x4*)(st + h * 1024 + l * 16));
    st128(c, (which == 0 ? WS_QR : WS_KR) + (unsigned)(((hq * 8 + (l >> 3)) * 2048 + panel * 128 + 32 * w) * 4 + (l & 7) * 16), *(const u32x4*)(st + 8192 + l * 16));
}

DI void tile_v(const Ctx& c, int panel, int hq, int s0) {
    f32x4 acc[9][2];
    gemm1_main<false>(c, c.P.x, c.P.wqkv, 1728, panel * 128, 2 * 576 + hq * 144, c.lds0, acc, s0);
    const int l = threadIdx.x & 63; const unsigned w = rfl(threadIdx.x >> 6); const int g = l >> 4;
    char* st = c.smem + w * 10240;
    if (l < 32) {
        const int blk = l >> 1, hh = l & 1;
        *(u32x4*)(st + blk * 640 + (hh * 20 + 18) * 16) = u32x4{0x3f803f80u, 0x3f803f80u, 0x3f803f80u, 0x3f803f80u};
        *(u32x4*)(st + blk * 640 + (hh * 20 + 19) * 16) = u32x4{0, 0, 0, 0};
    }
#pragma unroll
    for (int n = 0; n < 9; ++n) {
        const int cc = 16 * n + (l & 15), hl = cc / 18, d = cc - 18 * hl;
#pragma unroll
        for (int tb = 0; tb < 2; ++tb)
            *(u32x2*)(st + (hl * 2 + tb) * 640 + ((g & 1) * 20 + d) * 16 + (g >> 1) * 8) = u32x2{pk2(acc[n][tb][0], acc[n][tb][1]), pk2(acc[n][tb][2], acc[n][tb][3])};
    }
    asm volatile("s_waitcnt lgkmcnt(0)" ::: "memory");
    const unsigned dv = WS_VT + (unsigned)(((hq * 8) * 128 + panel * 8 + 2 * w) * 640);
#pragma unroll
    for (int i = 0; i < 10; ++i) { const int idx = 64 * i + l, h = idx / 80, o = idx - 80 * h; st128(c, dv + (unsigned)(h * 128 * 640 + o * 16), *(const u32x4*)(st + idx * 16)); }
}

DI void prep_wsp(const Ctx& c, int n) {
    const int tid = threadIdx.x, l = tid & 63; const unsigned w = rfl(tid >> 6); const int g = l >> 4;
    const u32x4 srd = make_srd(c.P.wsp, S_ * S_ * 2);
    const int tb = 2 * n + (int)(w & 1), s0 = (int)(w >> 1) * 16;
    unsigned vsrc = (unsigned)((tb * 16 + (l >> 3)) * (S_ * 2) + (((l & 7) ^ (l >> 3)) << 4));
    const unsigned lw = c.lds0 + w * 16384;
    const unsigned rd0 = lw + (unsigned)((l & 15) * 128) + ((g ^ (l & 7)) << 4), rd1 = lw + (unsigned)((l & 15) * 128) + (((4 + g) ^ (l & 7)) << 4);
    auto batch = [&](int bt) {
#pragma unroll
        for (int i = 0; i < 8; ++i) {
            dma_now(srd, vsrc, (unsigned)((s0 + bt * 8 + i) * 128), lw + i * 2048);
            dma_now(srd, vsrc, (unsigned)((s0 + bt * 8 + i) * 128 + 8 * S_ * 2), lw + i * 2048 + 1024);
        }
    };
    u32x4 f[16];
    batch(0);
    asm volatile("s_waitcnt vmcnt(0)" ::: "memory");
#pragma unroll
    for (int i = 0; i < 8; ++i) { f[2 * i] = __builtin_bit_cast(u32x4, lds_rd128(rd0 + i * 2048)); f[2 * i + 1] = __builtin_bit_cast(u32x4, lds_rd128(rd1 + i * 2048)); }
#pragma unroll
    for (int i = 0; i < 16; ++i) OPAQUE(f[i]);
    batch(1);
#pragma unroll
    for (int i = 0; i < 16; ++i) st128(c, WS_WP + (unsigned)((tb * 64 + 2 * s0 + i) * 1024 + l * 16), f[i]);
    asm volatile("s_waitcnt vmcnt(0)" ::: "memory");
#pragma unroll
    for (int i = 0; i < 8; ++i) {
        st128(c, WS_WP + (unsigned)((tb * 64 + 2 * s0 + 16 + 2 * i) * 1024 + l * 16), __builtin_bit_cast(u32x4, lds_rd128(rd0 + i * 2048)));
        st128(c, WS_WP + (unsigned)((tb * 64 + 2 * s0 + 17 + 2 * i) * 1024 + l * 16), __builtin_bit_cast(u32x4, lds_rd128(rd1 + i * 2048)));
    }
    publish(c, FL_WP + n);
}

DI void prep_wcat(const Ctx& c, int kb) {
    const int tid = threadIdx.x, l = tid & 63; const unsigned w = rfl(tid >> 6); const int g = l >> 4;
    const u32x4 srd = kb < 18 ? make_srd(c.P.wo, D_ * D_ * 2) : make_srd(c.P.wout, FH * D_ * 2);
#pragma unroll
    for (int i = 0; i < 9; ++i) {
        const int idx = 64 * ((int)w + 4 * i) + l, sg = idx / 72, ch = idx - 72 * sg, gg = sg >> 3, j = sg & 7;
        int row;
        if (kb < 16) { const int head = 2 * kb + (gg >> 1), h = gg & 1; row = 18 * head + (j < 4 ? 4 * h + j : 8 + 4 * h + (j - 4)); }
        else if (kb < 18) row = 18 * (16 * (kb - 16) + 4 * gg + (j >> 1)) + 16 + (j & 1);
        else row = 32 * (kb - 18) + (j < 4 ? 4 * gg + j : 16 + 4 * gg + (j - 4));
        dma_now(srd, (unsigned)(row * 1152 + ch * 16), 0u, c.lds0 + (w + 4 * i) * 1024);
    }
    WAIT_BAR0();
    const int q = (l & 15) >> 2, p = l & 3;
    const unsigned r0 = c.lds0 + (unsigned)((8 * g + q) * 1152 + p * 8);
#pragma unroll
    for (int i = 0; i < 9; ++i) {
        const int db = (int)w + 4 * i;
        st128(c, WS_WC + (unsigned)((db * 90 + kb) * 1024 + l * 16), __builtin_bit_cast(u32x4, cat8(lds_rdtr(r0 + db * 32), lds_rdtr(r0 + 4 * 1152 + db * 32))));
    }
    publish(c, FL_WC + kb);
}

DI void prep_gate(const Ctx& c, int n) {
    const int tid = threadIdx.x, l = tid & 63; const int w = tid >> 6;
    const int row = n * 32 + w * 8 + (l >> 3);
    u32x4 xv[9], gv[9];
#pragma unroll
    for (int i = 0; i < 9; ++i) { xv[i] = *(const u32x4*)(c.P.x + row * 576 + ((l & 7) + 8 * i) * 8); gv[i] = *(const u32x4*)(c.P.gatew + ((l & 7) + 8 * i) * 8); }
    float a = 0.f;
#pragma unroll
    for (int i = 0; i < 9; ++i)
#pragma unroll
        for (int e = 0; e < 4; ++e) { a = __builtin_fmaf(bflo(xv[i][e]), bflo(gv[i][e]), a); a = __builtin_fmaf(bfhi(xv[i][e]), bfhi(gv[i][e]), a); }
    a += __shfl_xor(a, 1); a += __shfl_xor(a, 2); a += __shfl_xor(a, 4);
    if ((l & 7) == 0) st32(c, WS_GT + row * 4, __float_as_uint(1.0f / (1.0f + __expf(-a))));
    publish(c, FL_WC + 90 + n);
}

constexpr int AB_SLOT = 20 * 1024;
struct AttnState {
    f32x16 S[2], O[2]; u32x4 pl[2], ph[2];
    bf16x8 k0, v0, v1; u32x4 k1; u32x4 q0[2], q1[2];
};
template <int T, class F>
DI void attn_slot(AttnState& s, unsigned aK, unsigned aR, unsigned aV, const f32x16& zero, F&& hook) {
    constexpr int U = T ^ 1;
    s.S[U] = MFMA32(s.k0, BC8(s.q0[U]), zero);
    SB();
#pragma unroll
    for (int i = 0; i < 4; ++i) s.S[T][i] = EX2(s.S[T][i]);
    SB();
    s.S[U] = MFMA32(BC8(s.k1), BC8(s.q1[U]), s.S[U]);
    SB();
    if (T == 0) { s.k0 = lds_rd128(aK); s.k1[0] = lds_rd32(aR); }
#pragma unroll
    for (int i = 4; i < 8; ++i) s.S[T][i] = EX2(s.S[T][i]);
    s.pl[T][0] = pk2(s.S[T][0], s.S[T][1]); s.pl[T][1] = pk2(s.S[T][2], s.S[T][3]);
    SB();
    s.O[U] = MFMA32(s.v1, BC8(s.ph[U]), s.O[U]);
    SB();
    if (T == 0) s.v1 = lds_rd128(aV);
    hook();
    s.pl[T][2] = pk2(s.S[T][4], s.S[T][5]); s.pl[T][3] = pk2(s.S[T][6], s.S[T][7]);
    SB();
#pragma unroll
    for (int i = 8; i < 12; ++i) s.S[T][i] = EX2(s.S[T][i]);
    SB();
    s.O[T] = MFMA32(s.v0, BC8(s.pl[T]), s.O[T]);
    SB();
    if (T == 1) s.v0 = lds_rd128(aV);
#pragma unroll
    for (int i = 12; i < 16; ++i) s.S[T][i] = EX2(s.S[T][i]);
    SB();
    s.ph[T][0] = pk2(s.S[T][8], s.S[T][9]); s.ph[T][1] = pk2(s.S[T][10], s.S[T][11]);
    s.ph[T][2] = pk2(s.S[T][12], s.S[T][13]); s.ph[T][3] = pk2(s.S[T][14], s.S[T][15]);
    SB();
}

DI void attn_unit(const Ctx& c, int head, int qb) {
    const int tid = threadIdx.x, l = tid & 63; const unsigned w = rfl(tid >> 6); const int h = l >> 5, r32 = l & 31; const unsigned lds0 = c.lds0;
    {
        const int hq = head >> 3;
        __builtin_amdgcn_s_sleep(32); __builtin_amdgcn_s_sleep(32);
        wait_flags<16>(c, tid < 16 ? FL_QKV + (4 + hq) * 16 + tid : tid < 32 ? FL_QKV + (8 + hq) * 16 + (tid - 16) : tid < 34 ? FL_QKV + hq * 16 + 2 * qb + (tid - 32) : ~0u);
    }
    const u32x4 srd = make_srd(c.P.ws + WS_KM, WS_QM - WS_KM);
    unsigned vl = (unsigned)l * 16; OPAQUE(vl);
    unsigned sLds[5], sSrc[5], sStep[5];
#pragma unroll
    for (int i = 0; i < 5; ++i) {
        const unsigned p = w + 4 * i;
        sLds[i] = lds0 + p * 1024;
        sSrc[i] = p < 8 ? (unsigned)(head * 65536) + p * 1024 : p < 18 ? (WS_VT - WS_KM) + (unsigned)(head * 81920) + (p - 8) * 1024
                : p == 18 ? (WS_KR - WS_KM) + (unsigned)(head * 8192) : OOB;
        sStep[i] = p < 8 ? 8192u : (p < 18 ? 10240u : (p == 18 ? 1024u : 0u));
    }
    auto piece = [&](int i, int ch, unsigned boff) { set_m0s(sLds[i], rfl(boff)); const unsigned so = ch < 8 ? sSrc[i] + (unsigned)ch * sStep[i] : OOB; asm volatile("s_nop 0"); dma_go_c(srd, vl, so); };
    auto issue = [&](int ch, unsigned boff) {
#pragma unroll
        for (int i = 0; i < 5; ++i) piece(i, ch, boff); };
    AttnState s;
    const int tok0 = qb * 256 + 64 * (int)w;
    issue(0, 0);
#pragma unroll
    for (int t = 0; t < 2; ++t) {
        s.q0[t] = __builtin_amdgcn_raw_buffer_load_b128(c.ws, (int)(WS_QM + ((unsigned)head * 64 + (tok0 >> 5) + t) * 1024 + l * 16), 0, 16);
        const unsigned qr = __builtin_amdgcn_raw_buffer_load_b32(c.ws, (int)(WS_QR + ((unsigned)head * 2048 + tok0 + 32 * t + r32) * 4), 0, 16);
        s.q1[t] = u32x4{h ? 0u : qr, 0u, 0u, 0u};
    }
    asm volatile("" : "+v"(s.q0[0]), "+v"(s.q0[1]), "+v"(s.q1[0]), "+v"(s.q1[1]));
    unsigned ka = lds0 + (unsigned)l * 16, ra = lds0 + 18432u + (unsigned)r32 * 4, va = lds0 + 8192u + (unsigned)(h * 20 + (r32 < 19 ? r32 : 19)) * 16;
    OPAQUE(ka); OPAQUE(ra); OPAQUE(va);
    const f32x16 zero = {};
    volatile LAS unsigned* flag = (volatile LAS unsigned*)(uintptr_t)(lds0 + 2 * AB_SLOT);

    for (int attempt = 0; attempt < 2; ++attempt) {
        float mx[2] = {-3.0e38f, -3.0e38f};
        {
            const u32x4 qz[2] = {u32x4{s.q1[0][0], 0u, 0u, 0u}, u32x4{s.q1[1][0], 0u, 0u, 0u}};
            const int nch = attempt ? 8 : 1, nblk = attempt ? 8 : 1;
            for (int ch = 0; ch < nch; ++ch) {
                if (attempt) { WAIT_BAR0(); issue(ch, 0); }
                WAIT_BAR0();
                for (int kb = 0; kb < nblk; ++kb) {
                    const bf16x8 k0 = lds_rd128(ka + kb * 1024);
                    const u32x4 k1 = u32x4{lds_rd32(ra + kb * 128), 0u, 0u, 0u};
#pragma unroll
                    for (int t = 0; t < 2; ++t) {
                        f32x16 sc = MFMA32(k0, BC8(s.q0[t]), zero); sc = MFMA32(BC8(k1), BC8(qz[t]), sc);
#pragma unroll
                        for (int r = 0; r < 16; ++r) mx[t] = __builtin_fmaxf(mx[t], sc[r]);
                    }
                }
            }
            if (attempt) { WAIT_BAR0(); issue(0, 0); WAIT_BAR0(); }
        }
#pragma unroll
        for (int t = 0; t < 2; ++t) {
            auto rr = __builtin_amdgcn_permlane32_swap(__float_as_uint(mx[t]), __float_as_uint(mx[t]), false, false);
            const float m = __builtin_fmaxf(__uint_as_float(rr[0]), __uint_as_float(rr[1]));
            const unsigned nb = __float_as_uint(-m), hi = nb & 0xffff0000u;
            const float r1 = -m - __uint_as_float(hi); const unsigned mid = __float_as_uint(r1) & 0xffff0000u;
            const float r2 = r1 - __uint_as_float(mid); const unsigned lo = __float_as_uint(r2) & 0xffff0000u;
            s.q1[t][1] = h ? 0u : ((hi >> 16) | mid); s.q1[t][2] = h ? 0u : (lo >> 16);
        }
        s.O[0] = zero; s.O[1] = zero;
        s.k0 = lds_rd128(ka); s.k1 = u32x4{lds_rd32(ra), 0x3f803f80u, 0x00003f80u, 0u};
        s.v0 = lds_rd128(va); s.v1 = s.v0; s.ph[1] = u32x4{0u, 0u, 0u, 0u};
        s.S[0] = MFMA32(s.k0, BC8(s.q0[0]), zero); s.S[0] = MFMA32(BC8(s.k1), BC8(s.q1[0]), s.S[0]);
        unsigned cur = 0, nxt = AB_SLOT;
        auto nohook = [] {};
        for (int ch = 0; ch < 8; ++ch) {
            const unsigned cK = ka + cur, cR = ra + cur, cV = va + cur, nK = ka + nxt, nR = ra + nxt, nV = va + nxt;
#pragma unroll
            for (int j = 0; j < 7; ++j) {
                attn_slot<0>(s, cK + (j + 1) * 1024, cR + (j + 1) * 128, cV + j * 1280 + 640, zero, nohook);
                attn_slot<1>(s, 0, 0, cV + (j + 1) * 1280, zero, [&] { if (j < 5) piece(j, ch + 1, nxt); });
            }
            asm volatile("s_waitcnt vmcnt(0)\n\ts_barrier" ::: "memory");
            SB();
            attn_slot<0>(s, nK, nR, cV + 7 * 1280 + 640, zero, nohook);
            attn_slot<1>(s, 0, 0, nV, zero, [&] { asm volatile("s_barrier" ::: "memory"); });
            const unsigned t_ = cur; cur = nxt; nxt = t_;
        }
        s.O[1] = MFMA32(s.v1, BC8(s.ph[1]), s.O[1]);
        if (attempt == 1) break;
        bool bad = false;
#pragma unroll
        for (int t = 0; t < 2; ++t) {
            float am = 0.f; bool b = false;
#pragma unroll
            for (int r = 0; r < 11; ++r) { am = __builtin_fmaxf(am, __builtin_fabsf(s.O[t][r])); b = b || (s.O[t][r] != s.O[t][r]); }
            bad = bad || b || !(am < 1.0e30f);
        }
        if (tid == 0) *flag = 0u;
        WAIT_BAR0();
        if (__any(bad)) *flag = 1u;
        WAIT_BAR0();
        if (rfl(*flag) == 0u) break;
    }
#pragma unroll
    for (int t = 0; t < 2; ++t) {
        auto rr = __builtin_amdgcn_permlane32_swap(__float_as_uint(s.O[t][10]), __float_as_uint(s.O[t][10]), false, false);
        const float inv = 1.0f / __uint_as_float(rr[0]);
        const int tok = tok0 + 32 * t + r32, tb = tok >> 4;
        const unsigned base = WS_HC + (unsigned)(tb * 90 * 1024);
        const u32x4 m = {pk2(s.O[t][0] * inv, s.O[t][1] * inv), pk2(s.O[t][2] * inv, s.O[t][3] * inv), pk2(s.O[t][4] * inv, s.O[t][5] * inv), pk2(s.O[t][6] * inv, s.O[t][7] * inv)};
        st128(c, base + (unsigned)((head >> 1) * 1024 + ((tok & 15) + 16 * (2 * (head & 1) + h)) * 16), m);
        if (h == 0) st32(c, base + (unsigned)((16 + (head >> 4)) * 1024 + ((tok & 15) + 16 * ((head & 15) >> 2)) * 16 + (head & 3) * 4), pk2(s.O[t][8] * inv, s.O[t][9] * inv));
    }
    publish(c, FL_O + qb * 32 + head);
}

constexpr int SD_N = 8, SD_SLOT = 9216;
struct SpdRegs { bf16x8 zf[9]; u32x4 t[4][2]; unsigned vZ[3], vz, vT; };
template <int U>
DI void spd_sub(f32x4 (&acc)[9][2], SpdRegs& r, u32x4 srdT, u32x4 srdZ, unsigned sWZ, unsigned w, unsigned sTa, unsigned sTb, unsigned sZ) {
    constexpr int NX = (U + 1) & (SD_N - 1), PF = (U + SD_N - 1) & (SD_N - 1), TU = U & 3, TP = (U + 3) & 3;
    asm volatile("s_waitcnt vmcnt(10)\n\ts_barrier" : "+v"(r.t[TU][0]), "+v"(r.t[TU][1])::"memory");
    SB();
#pragma unroll
    for (int n = 0; n < 9; ++n) {
        if (n == 0) LGKM(4);
        if (n == 5) LGKM(5);
        if (n == 4 || n == 6) set_m0(sWZ, PF * SD_SLOT + (n == 4 ? 0 : 4096));
        mfma16v(acc[n][0], r.zf[n], BC8(r.t[TU][0]));
        if (n == 0) ld_frag<0>(r.t[TP][0], srdT, r.vT, sTa);
        if (n == 2) ld_frag<0>(r.t[TP][1], srdT, r.vT, sTb);
        if (n == 4) dma_go_c(srdZ, r.vZ[0], sZ);
        if (n == 6) dma_go_c(srdZ, r.vZ[1], sZ);
        mfma16v(acc[n][1], r.zf[n], BC8(r.t[TU][1]));
        r.zf[n] = lds_rd128(r.vz + NX * SD_SLOT + n * 1024);
        SB();
    }
    if (w == 0) { set_m0(sWZ, PF * SD_SLOT + 8192); asm volatile("s_nop 0"); dma_go_c(srdZ, r.vZ[2], sZ); }
    SB();
}

DI void spatial_tile(const Ctx& c, int panel, int jt, const u32x4 (&z1p)[9]) {
    const int tid = threadIdx.x, l = tid & 63; const unsigned w = rfl(tid >> 6); const unsigned lds0 = c.lds0;
    const u32x4 srdT = make_srd(c.P.ws + WS_WP + (size_t)(panel * 8 + 2 * w) * 65536, 2 * 65536), srdZ = make_srd(c.P.ws + WS_Z2 + (size_t)jt * 9 * 65536, 9 * 65536);
    SpdRegs r;
#pragma unroll
    for (int i = 0; i < 3; ++i) { r.vZ[i] = (w + 4 * i) * 65536 + l * 16; OPAQUE(r.vZ[i]); }
    r.vz = lds0 + l * 16; OPAQUE(r.vz);
    r.vT = l * 16; OPAQUE(r.vT);
    const unsigned sWZ = lds0 + w * 1024;
#define SO(v) ((v) < 64 ? (unsigned)(v) * 1024u : OOB)
#define SO2(v) ((v) < 64 ? (unsigned)(v) * 1024u + 65536u : OOB)
    f32x4 acc[9][2];
#pragma unroll
    for (int n = 0; n < 9; ++n) { acc[n][0] = f32x4{0, 0, 0, 0}; acc[n][1] = f32x4{0, 0, 0, 0}; }
    PIN18(acc, "s_nop 1");
#pragma unroll
    for (int u = 0; u < SD_N - 1; ++u) {
        if (u >= 4) { ld_frag<0>(r.t[u - 4][0], srdT, r.vT, SO(u - 4)); ld_frag<0>(r.t[u - 4][1], srdT, r.vT, SO2(u - 4)); }
#pragma unroll
        for (int i = 0; i < 2; ++i) { set_m0(sWZ, u * SD_SLOT + i * 4096); asm volatile("s_nop 0"); dma_go_c(srdZ, r.vZ[i], SO(u)); }
        if (w == 0) { set_m0(sWZ, u * SD_SLOT + 8192); asm volatile("s_nop 0"); dma_go_c(srdZ, r.vZ[2], SO(u)); }
    }
    asm volatile("s_waitcnt vmcnt(18)\n\ts_barrier" ::: "memory");
#pragma unroll
    for (int n = 0; n < 9; ++n) r.zf[n] = lds_rd128(r.vz + n * 1024);
    LGKM(0);
    for (int u = 0; u < 64; u += 8) {
        spd_sub<0>(acc, r, srdT, srdZ, sWZ, w, SO(u + 3), SO2(u + 3), SO(u + 7));
        spd_sub<1>(acc, r, srdT, srdZ, sWZ, w, SO(u + 4), SO2(u + 4), SO(u + 8));
        spd_sub<2>(acc, r, srdT, srdZ, sWZ, w, SO(u + 5), SO2(u + 5), SO(u + 9));
        spd_sub<3>(acc, r, srdT, srdZ, sWZ, w, SO(u + 6), SO2(u + 6), SO(u + 10));
        spd_sub<4>(acc, r, srdT, srdZ, sWZ, w, SO(u + 7), SO2(u + 7), SO(u + 11));
        spd_sub<5>(acc, r, srdT, srdZ, sWZ, w, SO(u + 8), SO2(u + 8), SO(u + 12));
        spd_sub<6>(acc, r, srdT, srdZ, sWZ, w, SO(u + 9), SO2(u + 9), SO(u + 13));
        spd_sub<7>(acc, r, srdT, srdZ, sWZ, w, SO(u + 10), SO2(u + 10), SO(u + 14));
    }
    asm volatile("s_waitcnt vmcnt(0) lgkmcnt(0)" : "+v"(r.t[0][0]), "+v"(r.t[0][1]), "+v"(r.t[1][0]), "+v"(r.t[1][1]), "+v"(r.t[2][0]), "+v"(r.t[2][1]), "+v"(r.t[3][0]), "+v"(r.t[3][1])::"memory");
    PIN18(acc, "s_nop 7\n\ts_nop 1");
    float bs[2];
#pragma unroll
    for (int tb = 0; tb < 2; ++tb) bs[tb] = bf2f(c.P.bsp[panel * 128 + 32 * w + 16 * tb + (l & 15)]);
    unsigned hp[9][2][2];
#pragma unroll
    for (int n = 0; n < 9; ++n)
#pragma unroll
        for (int tb = 0; tb < 2; ++tb) {
            hp[n][tb][0] = pk2(bflo(z1p[n][2 * tb]) * (acc[n][tb][0] + bs[tb]), bfhi(z1p[n][2 * tb]) * (acc[n][tb][1] + bs[tb]));
            hp[n][tb][1] = pk2(bflo(z1p[n][2 * tb + 1]) * (acc[n][tb][2] + bs[tb]), bfhi(z1p[n][2 * tb + 1]) * (acc[n][tb][3] + bs[tb]));
        }
    const int fb0 = jt * 9;
#pragma unroll
    for (int tb = 0; tb < 2; ++tb) {
        const unsigned base = WS_HC + (unsigned)(((panel * 8 + 2 * w + tb) * 90 + 18) * 1024 + l * 16);
        if ((jt & 1) == 0) {
#pragma unroll
            for (int n = 0; n < 8; n += 2) st128(c, base + (unsigned)(((fb0 + n) >> 1) * 1024), u32x4{hp[n][tb][0], hp[n][tb][1], hp[n + 1][tb][0], hp[n + 1][tb][1]});
            st64(c, base + (unsigned)(((fb0 + 8) >> 1) * 1024), u32x2{hp[8][tb][0], hp[8][tb][1]});
        } else {
            st64(c, base + (unsigned)((fb0 >> 1) * 1024 + 8), u32x2{hp[0][tb][0], hp[0][tb][1]});
#pragma unroll
            for (int n = 1; n < 9; n += 2) st128(c, base + (unsigned)(((fb0 + n) >> 1) * 1024), u32x4{hp[n][tb][0], hp[n][tb][1], hp[n + 1][tb][0], hp[n + 1][tb][1]});
        }
    }
}

constexpr int FN_STAGE = 16 * 1024;
struct FnRegs { bf16x8 wf[5]; u32x4 h[4][3]; unsigned vW[4], vrd, vl; };
template <int U, int VM>
DI void fn_stage(f32x4 (&acc)[5], FnRegs& r, u32x4 srdW, u32x4 srdH, unsigned sWW, unsigned sNext) {
    constexpr int PF = (U + 3) & 3;
    asm volatile("s_waitcnt vmcnt(%3)\n\ts_barrier" : "+v"(r.h[U][0]), "+v"(r.h[U][1]), "+v"(r.h[U][2]) : "n"(VM) : "memory");
    SB();
#pragma unroll
    for (int n = 0; n < 5; ++n) r.wf[n] = lds_rd128(r.vrd + U * FN_STAGE + (n * 3) * 1024);
    ld_frag<0>(r.h[PF][0], srdH, r.vl, sNext); ld_frag<1024>(r.h[PF][1], srdH, r.vl, sNext); ld_frag<2048>(r.h[PF][2], srdH, r.vl, sNext);
    SB();
#pragma unroll
    for (int kk = 0; kk < 3; ++kk) {
#pragma unroll
        for (int n = 0; n < 5; ++n) {
            const int i = kk * 5 + n;
            const bool dm = (i % 4 == 1);
            if (dm) set_m0(sWW, PF * FN_STAGE + (i / 4) * 4096);
            mfma16v(acc[n], r.wf[n], BC8(r.h[U][kk]));
            if (dm) dma_go_c(srdW, r.vW[i / 4], sNext);
            if (kk + 1 < 3) r.wf[n] = lds_rd128(r.vrd + U * FN_STAGE + (n * 3 + kk + 1) * 1024);
            SB();
        }
    }
}
DI void final_tile(const Ctx& c, int tt, int j, unsigned hflag, int rot) {
    const int tid = threadIdx.x, l = tid & 63; const unsigned w = rfl(tid >> 6); const int g = l >> 4;
    const int db0 = (9 * j) >> 1;
    const u32x4 srdW = make_srd(c.P.ws + WS_WC + (size_t)db0 * 90 * 1024, 5 * 90 * 1024);
    const u32x4 srdH = make_srd(c.P.ws + WS_HC + (size_t)(tt * 4 + w) * 90 * 1024, 90 * 1024);
    FnRegs r;
    r.vl = (unsigned)l * 16; OPAQUE(r.vl);
#pragma unroll
    for (int i = 0; i < 4; ++i) { const unsigned p = w + 4 * i, db = p / 3, kk = p - db * 3; r.vW[i] = p < 15 ? (db * 90 + kk) * 1024 + l * 16 : OOB; OPAQUE(r.vW[i]); }
    r.vrd = c.lds0 + l * 16; OPAQUE(r.vrd);
    const unsigned sWW = c.lds0 + w * 1024;
#define FO(s) ((s) < 24 ? (18u + 3u * (unsigned)((s) + rot >= 24 ? (s) + rot - 24 : (s) + rot)) * 1024u : (s) < 30 ? (unsigned)((s) - 24) * 3072u : OOB)
    asm volatile("s_waitcnt vmcnt(0) lgkmcnt(0)\n\ts_barrier" ::: "memory");
    raise_flag(c, hflag);
#pragma unroll
    for (int s = 0; s < 3; ++s)
#pragma unroll
        for (int i = 0; i < 4; ++i) { set_m0(sWW, s * FN_STAGE + i * 4096); asm volatile("s_nop 0"); dma_go_c(srdW, r.vW[i], FO(s)); }
    wait_flags(c, tid < 16 ? FL_H + (tt >> 1) * 16 + tid : ~0u);
    f32x4 accA[5], accB[5];
#pragma unroll
    for (int n = 0; n < 5; ++n) { accA[n] = f32x4{0, 0, 0, 0}; accB[n] = f32x4{0, 0, 0, 0}; }
    PIN5(accA, "s_nop 1"); PIN5(accB, "s_nop 1");
#pragma unroll
    for (int s = 0; s < 3; ++s) { ld_frag<0>(r.h[s][0], srdH, r.vl, FO(s)); ld_frag<1024>(r.h[s][1], srdH, r.vl, FO(s)); ld_frag<2048>(r.h[s][2], srdH, r.vl, FO(s)); }
    asm volatile("s_waitcnt vmcnt(6)" : "+v"(r.h[0][0]), "+v"(r.h[0][1]), "+v"(r.h[0][2])::"memory");
    fn_stage<0, 63>(accB, r, srdW, srdH, sWW, FO(3));
    asm volatile("s_waitcnt vmcnt(10)" : "+v"(r.h[1][0]), "+v"(r.h[1][1]), "+v"(r.h[1][2])::"memory");
    fn_stage<1, 63>(accB, r, srdW, srdH, sWW, FO(4));
    fn_stage<2, 14>(accB, r, srdW, srdH, sWW, FO(5));
    fn_stage<3, 14>(accB, r, srdW, srdH, sWW, FO(6));
    for (int s = 4; s < 20; s += 4) {
        fn_stage<0, 14>(accB, r, srdW, srdH, sWW, FO(s + 3)); fn_stage<1, 14>(accB, r, srdW, srdH, sWW, FO(s + 4));
        fn_stage<2, 14>(accB, r, srdW, srdH, sWW, FO(s + 5)); fn_stage<3, 14>(accB, r, srdW, srdH, sWW, FO(s + 6));
    }
    fn_stage<0, 14>(accB, r, srdW, srdH, sWW, FO(23));
    if (tid < 64) poll_flags(c, tid < 32 ? FL_O + (tt >> 2) * 32 + tid : ~0u);
    fn_stage<1, 14>(accB, r, srdW, srdH, sWW, FO(24));
    fn_stage<2, 14>(accB, r, srdW, srdH, sWW, FO(25));
    fn_stage<3, 14>(accB, r, srdW, srdH, sWW, FO(26));
    fn_stage<0, 14>(accA, r, srdW, srdH, sWW, FO(27)); fn_stage<1, 14>(accA, r, srdW, srdH, sWW, FO(28));
    fn_stage<2, 14>(accA, r, srdW, srdH, sWW, FO(29)); fn_stage<3, 14>(accA, r, srdW, srdH, sWW, OOB);
    fn_stage<0, 14>(accA, r, srdW, srdH, sWW, OOB); fn_stage<1, 14>(accA, r, srdW, srdH, sWW, OOB);
    asm volatile("s_waitcnt vmcnt(0) lgkmcnt(0)" : "+v"(r.h[0][0]), "+v"(r.h[0][1]), "+v"(r.h[0][2]), "+v"(r.h[1][0]), "+v"(r.h[1][1]), "+v"(r.h[1][2]),
                 "+v"(r.h[2][0]), "+v"(r.h[2][1]), "+v"(r.h[2][2]), "+v"(r.h[3][0]), "+v"(r.h[3][1]), "+v"(r.h[3][2])::"memory");
    PIN5(accA, "s_nop 7\n\ts_nop 1"); PIN5(accB, "");
    const __amdgpu_buffer_rsrc_t ro = __builtin_amdgcn_make_buffer_rsrc((void*)c.P.out, (short)0, S_ * D_ * 2, 0x00020000);
    const int tok = tt * 64 + 16 * w + (l & 15);
    const float gt = __uint_as_float(__builtin_amdgcn_raw_buffer_load_b32(c.ws, (int)(WS_GT + tok * 4), 0, 16));
#pragma unroll
    for (int n = 0; n < 5; ++n) {
        const int d = (db0 + n) * 16 + 4 * g;
        if (d >= 72 * j && d < 72 * j + 72) {
            float v[4];
#pragma unroll
            for (int rr = 0; rr < 4; ++rr) v[rr] = gt * accA[n][rr] + (1.0f - gt) * accB[n][rr];
            __builtin_amdgcn_raw_buffer_store_b64(u32x2{pk2(v[0], v[1]), pk2(v[2], v[3])}, ro, (tok * 576 + d) * 2, 0, 0);
        }
    }
}

constexpr int MEGA_LDS = 73728 + 1024;

__global__ __launch_bounds__(256, 2) void mega_kernel(Params P) {
    extern __shared__ __attribute__((aligned(16))) char smem[];
    const int b = blockIdx.x, tid = threadIdx.x;
    const u64 tag = __hip_atomic_load((gu64*)(P.ws + WS_EP), __ATOMIC_RELAXED, __HIP_MEMORY_SCOPE_AGENT) + 1ull;
    const Ctx c{P, __builtin_amdgcn_make_buffer_rsrc(P.ws, (short)0, (int)WS_END, 0x00020000), tag, (unsigned)(uintptr_t)smem, smem};
    if (b < 256) {
        __builtin_amdgcn_s_setprio(1);
        const int x = b & 7, i = b >> 3, panel = (x >> 2) * 8 + (i & 7), jt = (x & 3) * 4 + (i >> 3);
        u32x4 z1p[9];
        tile_z(c, panel, jt, ((i & 7) + 2 * (i >> 3)) % 9, z1p);
        __builtin_amdgcn_s_setprio(2);
        spatial_tile(c, panel, jt, z1p);
        final_tile(c, 2 * panel + (jt >> 3), jt & 7, FL_H + panel * 16 + jt, ((i & 7) * 2 + (i >> 3)) % 21);
        if (b == 0 && tid < 64) {
#pragma unroll
            for (int k = 0; k < 4; ++k) { poll_flags(c, FL_H + 64 * k + tid); poll_flags(c, FL_O + 64 * k + tid); }
            if (tid == 0) __hip_atomic_store((gu64*)(P.ws + WS_EP), tag, __ATOMIC_RELAXED, __HIP_MEMORY_SCOPE_AGENT);
        }
    } else {
        const int a = b - 256, x = a & 7, idx = a >> 3;
        __builtin_amdgcn_s_setprio(1);
        if (idx < 24) {
            const int hq = x & 3, ty = idx % 3, panel = (x >> 2) * 8 + idx / 3, s0 = (idx / 3 + 3 * ty + 1) % 9;
            if (ty < 2) tile_qk(c, panel, ty, hq, s0); else tile_v(c, panel, hq, s0);
            publish(c, FL_QKV + (ty * 4 + hq) * 16 + panel);
            const int k = idx * 8 + x;
            if (k < 90) prep_wcat(c, k); else if (k < 154) prep_gate(c, k - 90);
        } else prep_wsp(c, (idx - 24) * 8 + x);
        __builtin_amdgcn_s_setprio(0);
        attn_unit(c, x * 4 + (idx & 3), idx >> 2);
    }
}

extern "C" void kernel_launch(void* const* d_in, const int* in_sizes, int n_in, void* d_out, int out_size, void* d_ws, size_t ws_size, hipStream_t stream) {
    Params P;
    P.x = (const u16*)d_in[0]; P.wqkv = (const u16*)d_in[1]; P.wo = (const u16*)d_in[2]; P.win = (const u16*)d_in[4]; P.wsp = (const u16*)d_in[5];
    P.bsp = (const u16*)d_in[6]; P.wout = (const u16*)d_in[7]; P.gatew = (const u16*)d_in[8]; P.out = (u16*)d_out; P.ws = (char*)d_ws;
    hipFuncSetAttribute((const void*)mega_kernel, hipFuncAttributeMaxDynamicSharedMemorySize, MEGA_LDS);
    mega_kernel<<<512, 256, MEGA_LDS, stream>>>(P);
}
```

```cpp
#include <hip/hip_runtime.h>
#include <stdint.h>

typedef __bf16 bf16x8 __attribute__((ext_vector_type(8)));
typedef __bf16 bf16x2 __attribute__((ext_vector_type(2)));
typedef short s16x4 __attribute__((ext_vector_type(4)));
typedef float f32x2 __attribute__((ext_vector_type(2)));
typedef float f32x4 __attribute__((ext_vector_type(4)));
typedef float f32x16 __attribute__((ext_vector_type(16)));
typedef unsigned u32x2 __attribute__((ext_vector_type(2)));
typedef unsigned u32x4 __attribute__((ext_vector_type(4)));
typedef unsigned short u16;
typedef unsigned long long u64;
typedef __attribute__((address_space(1))) unsigned long long gu64;
#define LAS __attribute__((address_space(3)))
#define DI __device__ __forceinline__

constexpr int S_ = 2048, D_ = 576, FH = 2304;
constexpr unsigned WS_EP = 0;
constexpr unsigned WS_FL = 256;
constexpr unsigned FL_ST = 0, FL_Z = 256, FL_H = 512, FL_QKV = 768, FL_WP = 960, FL_O = 1024, FL_WC = 1280, FL_N = FL_WC + 154;
constexpr unsigned WS_GT = 16384;
constexpr unsigned WS_ST = WS_GT + 8192;
constexpr unsigned WS_KM = WS_ST + 16 * 16 * 1024;
constexpr unsigned WS_KR = WS_KM + 32 * 64 * 1024;
constexpr unsigned WS_VT = WS_KR + 32 * 2048 * 4;
constexpr unsigned WS_QM = WS_VT + 32 * 128 * 640;
constexpr unsigned WS_QR = WS_QM + 32 * 64 * 1024;
constexpr unsigned WS_Z2 = WS_QR + 32 * 2048 * 4;
constexpr unsigned WS_WP = WS_Z2 + 144 * 64 * 1024;
constexpr unsigned WS_HC = WS_WP + 128 * 64 * 1024;
constexpr unsigned WS_WC = WS_HC + 128 * 90 * 1024;
constexpr unsigned WS_END = WS_WC + 36 * 90 * 1024;

struct Params {
    const u16* x; const u16* wqkv; const u16* wo; const u16* win; const u16* wsp; const u16* bsp; const u16* wout; const u16* gatew;
    u16* out; char* ws;
};


DI float bf2f(u16 v) { return __uint_as_float((unsigned)v << 16); }
DI float bflo(unsigned v) { return __uint_as_float(v << 16); }
DI float bfhi(unsigned v) { return __uint_as_float(v & 0xffff0000u); }
DI unsigned pk2(float lo, float hi) { f32x2 v = {lo, hi}; bf16x2 b = __builtin_convertvector(v, bf16x2); return __builtin_bit_cast(unsigned, b); }
DI unsigned rfl(unsigned v) { return __builtin_amdgcn_readfirstlane(v); }
DI u32x4 make_srd(const void* p, unsigned bytes) {
    uint64_t a = (uint64_t)p;
    u32x4 r; r[0] = rfl((unsigned)a); r[1] = rfl((unsigned)(a >> 32) & 0xffffu); r[2] = rfl(bytes); r[3] = 0x00020000u; return r;
}
#define SB() __builtin_amdgcn_sched_barrier(0)
#define BAR() asm volatile("s_waitcnt lgkmcnt(0)\n\ts_barrier" ::: "memory")
#define WAIT_BAR0() asm volatile("s_waitcnt vmcnt(0) lgkmcnt(0)\n\ts_barrier" ::: "memory")
DI void set_m0(unsigned base, unsigned imm) { asm volatile("s_add_u32 m0, %0, %1" ::"s"(base), "n"(imm) : "scc"); }
DI void set_m0s(unsigned a, unsigned b) { asm volatile("s_add_u32 m0, %0, %1" ::"s"(a), "s"(b) : "scc"); }
DI void dma_go(u32x4 srd, unsigned voff, unsigned soff) { asm volatile("buffer_load_dwordx4 %0, %1, %2 offen lds" ::"v"(voff), "s"(srd), "s"(soff) : "memory"); }
DI void dma_go_c(u32x4 srd, unsigned voff, unsigned soff) { asm volatile("buffer_load_dwordx4 %0, %1, %2 offen sc1 lds" ::"v"(voff), "s"(srd), "s"(soff) : "memory"); }
DI void dma_now(u32x4 srd, unsigned voff, unsigned soff, unsigned ldsaddr) { asm volatile("s_mov_b32 m0, %0\n\ts_nop 0\n\tbuffer_load_dwordx4 %1, %2, %3 offen nt lds" ::"s"(ldsaddr), "v"(voff), "s"(srd), "s"(soff) : "memory"); }
template <int IMM> DI void ld_frag(u32x4& d, u32x4 srd, unsigned voff, unsigned soff) {
    asm volatile("buffer_load_dwordx4 %0, %1, %2, %3 offen offset:%4 sc1" : "=v"(d) : "v"(voff), "s"(srd), "s"(soff), "n"(IMM) : "memory");
}
DI bf16x8 lds_rd128(unsigned a) { return *(const LAS bf16x8*)(uintptr_t)a; }
DI unsigned lds_rd32(unsigned a) { return *(const LAS unsigned*)(uintptr_t)a; }
DI s16x4 lds_rdtr(unsigned a) { return __builtin_amdgcn_ds_read_tr16_b64_v4i16((LAS s16x4*)(uintptr_t)a); }
DI bf16x8 cat8(s16x4 a, s16x4 b) { typedef short s16x8 __attribute__((ext_vector_type(8))); s16x8 v = __builtin_shufflevector(a, b, 0, 1, 2, 3, 4, 5, 6, 7); return __builtin_bit_cast(bf16x8, v); }
DI void mfma16v(f32x4& c, bf16x8 a, bf16x8 b) { asm volatile("v_mfma_f32_16x16x32_bf16 %0, %1, %2, %0" : "+v"(c) : "v"(a), "v"(b)); }
#define PIN18(acc, txt) asm volatile(txt : "+v"(acc[0][0]), "+v"(acc[0][1]), "+v"(acc[1][0]), "+v"(acc[1][1]), "+v"(acc[2][0]), "+v"(acc[2][1]), "+v"(acc[3][0]), "+v"(acc[3][1]), "+v"(acc[4][0]), "+v"(acc[4][1]), \
    "+v"(acc[5][0]), "+v"(acc[5][1]), "+v"(acc[6][0]), "+v"(acc[6][1]), "+v"(acc[7][0]), "+v"(acc[7][1]), "+v"(acc[8][0]), "+v"(acc[8][1]))
#define PIN5(acc, txt) asm volatile(txt : "+v"(acc[0]), "+v"(acc[1]), "+v"(acc[2]), "+v"(acc[3]), "+v"(acc[4]))
#define LGKM(n) __builtin_amdgcn_s_waitcnt(0xC07F | ((n) << 8))
#define OPAQUE(x) asm volatile("" : "+v"(x))
constexpr unsigned OOB = 0x40000000u;
#define EX2(x) __builtin_amdgcn_exp2f(x)
#define BC8(x) __builtin_bit_cast(bf16x8, x)
#define MFMA32(a, b, c) __builtin_amdgcn_mfma_f32_32x32x16_bf16(a, b, c, 0, 0, 0)

DI float gelu_t(float x) {
    const float c0 = -2.0f * 0.7978845608028654f * 1.4426950408889634f, c1 = c0 * 0.044715f;
    const float e = __builtin_amdgcn_exp2f(x * __builtin_fmaf(x * x, c1, c0));
    return x * __builtin_amdgcn_rcpf(1.0f + e);
}
template <int N> DI float dpp_ror_add(float v) { return v + __builtin_bit_cast(float, __builtin_amdgcn_update_dpp(0, __builtin_bit_cast(int, v), 0x120 + N, 0xf, 0xf, false)); }
DI float row16_sum(float v) { v = dpp_ror_add<8>(v); v = dpp_ror_add<4>(v); v = dpp_ror_add<2>(v); v = dpp_ror_add<1>(v); return v; }

struct Ctx { const Params& P; __amdgpu_buffer_rsrc_t ws; u64 tag; unsigned lds0; char* smem; };
DI void st128(const Ctx& c, unsigned off, u32x4 v) { __builtin_amdgcn_raw_buffer_store_b128(v, c.ws, (int)off, 0, 16); }
DI void st64(const Ctx& c, unsigned off, u32x2 v) { __builtin_amdgcn_raw_buffer_store_b64(v, c.ws, (int)off, 0, 16); }
DI void st32(const Ctx& c, unsigned off, unsigned v) { __builtin_amdgcn_raw_buffer_store_b32(v, c.ws, (int)off, 0, 16); }
DI gu64* flag_ptr(const Ctx& c, unsigned idx) { return (gu64*)(c.P.ws + WS_FL) + idx; }
DI void raise_flag(const Ctx& c, unsigned idx) { if (threadIdx.x == 0) __hip_atomic_store(flag_ptr(c, idx), c.tag, __ATOMIC_RELAXED, __HIP_MEMORY_SCOPE_AGENT); }
DI void publish(const Ctx& c, unsigned idx) { WAIT_BAR0(); raise_flag(c, idx); }
constexpr int POLL_SLEEP = 16;
constexpr unsigned SPIN_CAP = 1u << 16;
DI void poll_flags(const Ctx& c, unsigned idx) {
    gu64* p = flag_ptr(c, idx == ~0u ? 0u : idx);
    for (unsigned spins = 0; spins < SPIN_CAP; ++spins) {
        const u64 v = idx == ~0u ? c.tag : __hip_atomic_load(p, __ATOMIC_RELAXED, __HIP_MEMORY_SCOPE_AGENT);
        if (__all(v == c.tag)) break;
        __builtin_amdgcn_s_sleep(POLL_SLEEP);
    }
}
DI void wait_flags(const Ctx& c, unsigned idx) { if (threadIdx.x < 64) poll_flags(c, idx); BAR(); }

constexpr int G1_XS = 16384, G1_WS = 9216, G1_WOFF = 2 * G1_XS, G1_LDS = G1_WOFF + 4 * G1_WS;
struct G1Regs { bf16x8 wf[9], xf[2][2]; unsigned vX[4], vW[3], vw0, vw1, vx[2]; };
template <bool TRANS> DI void g1_mm(f32x4& c, bf16x8 wf, bf16x8 xf) { if (TRANS) mfma16v(c, wf, xf); else mfma16v(c, xf, wf); }
DI bf16x8 g1_rdw(unsigned lo, unsigned hi, int off) { return cat8(lds_rdtr(lo + off), lds_rdtr(hi + off)); }

template <bool TRANS, int J>
DI void g1_sub(f32x4 (&acc)[9][2], G1Regs& r, u32x4 srdX, u32x4 srdW, unsigned sWX, unsigned sWW, unsigned w, unsigned sX, unsigned sW) {
    constexpr int N1 = (J + 1) & 3, CUR = J & 1, NXT = CUR ^ 1, PW = (J + 3) & 3;
    constexpr int XS_RD = N1 >> 1, XS_WR = J >> 1;
    if (J & 1) asm volatile("s_waitcnt vmcnt(2) lgkmcnt(8)\n\ts_barrier" ::: "memory");
    else asm volatile("s_waitcnt vmcnt(6) lgkmcnt(8)\n\ts_barrier" ::: "memory");
    LGKM(8);
    SB();
#pragma unroll
    for (int n = 0; n < 9; ++n) {
        if (n == 5) LGKM(12);
        const bool dw = (n == 1 || n == 3), dx = (J & 1) && n >= 4 && n < 8;
        if (dw) set_m0(sWW, PW * G1_WS + (n == 1 ? 0 : 4096));
        if (dx) set_m0(sWX, XS_WR * G1_XS + (n - 4) * 1024);
        g1_mm<TRANS>(acc[n][0], r.wf[n], r.xf[CUR][0]);
        if (dw) dma_go(srdW, r.vW[n == 1 ? 0 : 1], sW);
        if (dx) dma_go(srdX, r.vX[n - 4], sX);
        g1_mm<TRANS>(acc[n][1], r.wf[n], r.xf[CUR][1]);
        if (n < 2) r.xf[NXT][n] = lds_rd128(r.vx[N1 & 1] + XS_RD * G1_XS + n * 2048);
        r.wf[n] = g1_rdw(r.vw0, r.vw1, N1 * G1_WS + n * 32);
        SB();
    }
    if (w == 0) { set_m0(sWW, PW * G1_WS + 8192); asm volatile("s_nop 0"); dma_go(srdW, r.vW[2], sW); }
    SB();
}

template <bool TRANS>
DI void gemm1_main(const Ctx& c, const u16* x, const u16* W, int ldw, int row0, int col0, unsigned lds0, f32x4 (&acc)[9][2], int s0, unsigned deferred_flag = ~0u) {
    const int tid = threadIdx.x, l = tid & 63; const unsigned w = rfl(tid >> 6);
    const u32x4 srdX = make_srd(x, S_ * D_ * 2), srdW = make_srd(W, (unsigned)(D_ * ldw * 2));
    const int g = l >> 4, q = (l & 15) >> 2, p = l & 3;
    G1Regs r;
#pragma unroll
    for (int i = 0; i < 4; ++i) { r.vX[i] = (unsigned)((row0 + 32 * w + 8 * i + (l >> 3)) * (D_ * 2) + (((l & 7) ^ (l >> 3)) << 4)); OPAQUE(r.vX[i]); }
#pragma unroll
    for (int i = 0; i < 3; ++i) { const int pc = (int)w + 4 * i, idx = 64 * pc + l, rho = idx / 18, cc = idx - rho * 18, k = rho ^ (((rho >> 3) & 1) << 2);
        r.vW[i] = pc < 9 ? (unsigned)(k * ldw * 2 + (col0 + 8 * cc) * 2) : OOB; OPAQUE(r.vW[i]); }
    r.vw0 = lds0 + (unsigned)(G1_WOFF + (8 * g + (q ^ (4 * (g & 1)))) * 288 + p * 8); r.vw1 = lds0 + (unsigned)(G1_WOFF + (8 * g + ((q + 4) ^ (4 * (g & 1)))) * 288 + p * 8);
    r.vx[0] = lds0 + (unsigned)((32 * w + (l & 15)) * 128) + ((g ^ (l & 7)) << 4); r.vx[1] = lds0 + (unsigned)((32 * w + (l & 15)) * 128) + (((4 + g) ^ (l & 7)) << 4);
    OPAQUE(r.vw0); OPAQUE(r.vw1); OPAQUE(r.vx[0]); OPAQUE(r.vx[1]);
    const unsigned sWX = lds0 + w * 4096, sWW = lds0 + G1_WOFF + w * 1024, kst = (unsigned)(32 * ldw * 2);
    auto xs = [&](int slot, unsigned so) {
#pragma unroll
        for (int i = 0; i < 4; ++i) { set_m0(sWX, slot * G1_XS + i * 1024); asm volatile("s_nop 0"); dma_go(srdX, r.vX[i], so); } };
    auto wsl = [&](int slot, unsigned so) {
#pragma unroll
        for (int i = 0; i < 2; ++i) { set_m0(sWW, slot * G1_WS + i * 4096); asm volatile("s_nop 0"); dma_go(srdW, r.vW[i], so); }
        if (w == 0) { set_m0(sWW, slot * G1_WS + 8192); asm volatile("s_nop 0"); dma_go(srdW, r.vW[2], so); } };
    auto rot9 = [&](int s) { const int t = s + s0; return (unsigned)(t >= 9 ? t - 9 : t); };
#define GW(v) ((v) < 18 ? (rot9((v) >> 1) * 2 + ((v) & 1)) * kst : OOB)
#define GX(s) ((s) < 9 ? rot9(s) * 128u : OOB)
    xs(0, GX(0)); wsl(0, GW(0)); wsl(1, GW(1)); wsl(2, GW(2)); xs(1, GX(1));
#pragma unroll
    for (int n = 0; n < 9; ++n) { acc[n][0] = f32x4{0, 0, 0, 0}; acc[n][1] = f32x4{0, 0, 0, 0}; }
    PIN18(acc, "s_nop 1");
    if (deferred_flag != ~0u) publish(c, deferred_flag);
    else asm volatile("s_waitcnt vmcnt(8)\n\ts_barrier" ::: "memory");
#pragma unroll
    for (int tb = 0; tb < 2; ++tb) r.xf[0][tb] = lds_rd128(r.vx[0] + tb * 2048);
#pragma unroll
    for (int n = 0; n < 9; ++n) r.wf[n] = g1_rdw(r.vw0, r.vw1, n * 32);
    LGKM(0);
    for (int u = 0; u < 16; u += 4) {
        g1_sub<TRANS, 0>(acc, r, srdX, srdW, sWX, sWW, w, 0, GW(u + 3));
        g1_sub<TRANS, 1>(acc, r, srdX, srdW, sWX, sWW, w, GX(u / 2 + 2), GW(u + 4));
        g1_sub<TRANS, 2>(acc, r, srdX, srdW, sWX, sWW, w, 0, GW(u + 5));
        g1_sub<TRANS, 3>(acc, r, srdX, srdW, sWX, sWW, w, GX(u / 2 + 3), GW(u + 6));
    }
    g1_sub<TRANS, 0>(acc, r, srdX, srdW, sWX, sWW, w, 0, OOB);
    g1_sub<TRANS, 1>(acc, r, srdX, srdW, sWX, sWW, w, OOB, OOB);
    asm volatile("s_waitcnt vmcnt(0) lgkmcnt(0)\n\ts_barrier" ::: "memory");
    PIN18(acc, "s_nop 7\n\ts_nop 1");
}

DI void tile_z(const Ctx& c, int panel, int jt, int s0, u32x4 (&z1p)[9]) {
    const int tid = threadIdx.x, l = tid & 63; const unsigned w = rfl(tid >> 6); const int g = l >> 4;
    f32x4 y[9][2];
    gemm1_main<false>(c, c.P.x, c.P.win, 4608, panel * 128, FH + jt * 144, c.lds0, y, s0);
    {
        float s1[2][4], s2[2][4];
#pragma unroll
        for (int tb = 0; tb < 2; ++tb)
#pragma unroll
            for (int r = 0; r < 4; ++r) { s1[tb][r] = 0.f; s2[tb][r] = 0.f; }
#pragma unroll
        for (int n = 0; n < 9; ++n)
#pragma unroll
            for (int tb = 0; tb < 2; ++tb)
#pragma unroll
                for (int r = 0; r < 4; ++r) { const float v = gelu_t(y[n][tb][r]); y[n][tb][r] = v; s1[tb][r] += v; s2[tb][r] = __builtin_fmaf(v, v, s2[tb][r]); }
#pragma unroll
        for (int tb = 0; tb < 2; ++tb)
#pragma unroll
            for (int r = 0; r < 4; ++r) {
                const float a = row16_sum(s1[tb][r]), b = row16_sum(s2[tb][r]);
                if ((l & 15) == 0) st64(c, WS_ST + (unsigned)((panel * 16 + jt) * 1024 + (32 * w + 16 * tb + 4 * g + r) * 8), u32x2{__float_as_uint(a), __float_as_uint(b)});
            }
    }
    f32x4 acc[9][2];
    gemm1_main<true>(c, c.P.x, c.P.win, 4608, panel * 128, jt * 144, c.lds0, acc, s0, FL_ST + panel * 16 + jt);
    wait_flags(c, tid < 16 ? FL_ST + panel * 16 + tid : ~0u);
    u32x2 pv[8];
#pragma unroll
    for (int i = 0; i < 8; ++i) pv[i] = __builtin_amdgcn_raw_buffer_load_b64(c.ws, (int)(WS_ST + (panel * 16 + 8 * (l >> 5) + i) * 1024 + (32 * w + (l & 31)) * 8), 0, 16);
    SB();
#pragma unroll
    for (int n = 0; n < 9; ++n)
#pragma unroll
        for (int tb = 0; tb < 2; ++tb) { z1p[n][2 * tb] = pk2(gelu_t(acc[n][tb][0]), gelu_t(acc[n][tb][1])); z1p[n][2 * tb + 1] = pk2(gelu_t(acc[n][tb][2]), gelu_t(acc[n][tb][3])); }
    asm volatile("" : "+v"(z1p[0]), "+v"(z1p[1]), "+v"(z1p[2]), "+v"(z1p[3]), "+v"(z1p[4]), "+v"(z1p[5]), "+v"(z1p[6]), "+v"(z1p[7]), "+v"(z1p[8]));
    SB();
    {
        float a = 0.f, b = 0.f;
#pragma unroll
        for (int i = 0; i < 8; ++i) { a += __uint_as_float(pv[i][0]); b += __uint_as_float(pv[i][1]); }
        auto ra = __builtin_amdgcn_permlane32_swap(__float_as_uint(a), __float_as_uint(a), false, false);
        auto rb = __builtin_amdgcn_permlane32_swap(__float_as_uint(b), __float_as_uint(b), false, false);
        a = __uint_as_float(ra[0]) + __uint_as_float(ra[1]); b = __uint_as_float(rb[0]) + __uint_as_float(rb[1]);
        const float m = a * (1.0f / FH), var = __builtin_fmaxf(b * (1.0f / FH) - m * m, 0.f);
        const float rs = 1.0f / __builtin_sqrtf(var + 1e-5f);
        if (l < 32) *(f32x2*)(c.smem + (w * 32 + l) * 8) = f32x2{m, rs};
        asm volatile("s_waitcnt lgkmcnt(0)" ::: "memory");
    }
    f32x2 ms[2][4];
#pragma unroll
    for (int tb = 0; tb < 2; ++tb)
#pragma unroll
        for (int r = 0; r < 4; ++r) ms[tb][r] = *(const f32x2*)(c.smem + (w * 32 + 16 * tb + 4 * g + r) * 8);
    const unsigned dst = WS_Z2 + (unsigned)(((jt * 9) * 64 + panel * 4 + w) * 1024 + l * 16);
#pragma unroll
    for (int n = 0; n < 9; ++n) {
        unsigned pk[2][2];
#pragma unroll
        for (int tb = 0; tb < 2; ++tb) {
            float v[4];
#pragma unroll
            for (int r = 0; r < 4; ++r) v[r] = (y[n][tb][r] - ms[tb][r][0]) * ms[tb][r][1];
            pk[tb][0] = pk2(v[0], v[1]); pk[tb][1] = pk2(v[2], v[3]);
        }
        u32x4 o;
#pragma unroll
        for (int i = 0; i < 2; ++i) {
            auto a = __builtin_amdgcn_permlane32_swap(pk[0][i], pk[1][i], false, false);
            auto b = __builtin_amdgcn_permlane16_swap(a[0], a[1], false, false);
            o[i] = b[0]; o[2 + i] = b[1];
        }
        st128(c, dst + n * 65536, o);
    }
    publish(c, FL_Z + jt * 16 + panel);
    if (tid < 64) { poll_flags(c, FL_WC + tid); poll_flags(c, FL_WC + 64 + tid); poll_flags(c, tid < 26 ? FL_WC + 128 + tid : ~0u); }
    wait_flags(c, tid < 16 ? FL_Z + jt * 16 + tid : tid < 20 ? FL_WP + 4 * panel + (tid - 16) : ~0u);
}

DI void tile_qk(const Ctx& c, int panel, int which, int hq, int s0) {
    f32x4 acc[9][2];
    gemm1_main<true>(c, c.P.x, c.P.wqkv, 1728, panel * 128, which * 576 + hq * 144, c.lds0, acc, s0);
    const int l = threadIdx.x & 63; const unsigned w = rfl(threadIdx.x >> 6); const int g = l >> 4;
    const float sc = which == 0 ? 0.23570226039551584f * 1.4426950408889634f : 1.0f;
    char* st = c.smem + w * 9216;
#pragma unroll
    for (int n = 0; n < 9; ++n)
#pragma unroll
        for (int pp = 0; pp < 2; ++pp) {
            const int cc = 16 * n + 4 * g + 2 * pp, hl = cc / 18, d = cc - 18 * hl;
#pragma unroll
            for (int tb = 0; tb < 2; ++tb) {
                const int tok = 16 * tb + (l & 15);
                const int off = d < 16 ? hl * 1024 + (tok + 32 * (d >> 3)) * 16 + (d & 7) * 2 : 8192 + hl * 128 + tok * 4;
                *(unsigned*)(st + off) = pk2(acc[n][tb][2 * pp] * sc, acc[n][tb][2 * pp + 1] * sc);
            }
        }
    asm volatile("s_waitcnt lgkmcnt(0)" ::: "memory");
    const unsigned dm = (which == 0 ? WS_QM : WS_KM) + (unsigned)(((hq * 8) * 64 + panel * 4 + w) * 1024 + l * 16);
#pragma unroll
    for (int h = 0; h < 8; ++h) st128(c, dm + h * 65536, *(const u32x4*)(st + h * 1024 + l * 16));
    st128(c, (which == 0 ? WS_QR : WS_KR) + (unsigned)(((hq * 8 + (l >> 3)) * 2048 + panel * 128 + 32 * w) * 4 + (l & 7) * 16), *(const u32x4*)(st + 8192 + l * 16));
}

DI void tile_v(const Ctx& c, int panel, int hq, int s0) {
    f32x4 acc[9][2];
    gemm1_main<false>(c, c.P.x, c.P.wqkv, 1728, panel * 128, 2 * 576 + hq * 144, c.lds0, acc, s0);
    const int l = threadIdx.x & 63; const unsigned w = rfl(threadIdx.x >> 6); const int g = l >> 4;
    char* st = c.smem + w * 10240;
    if (l < 32) {
        const int blk = l >> 1, hh = l & 1;
        *(u32x4*)(st + blk * 640 + (hh * 20 + 18) * 16) = u32x4{0x3f803f80u, 0x3f803f80u, 0x3f803f80u, 0x3f803f80u};
        *(u32x4*)(st + blk * 640 + (hh * 20 + 19) * 16) = u32x4{0, 0, 0, 0};
    }
#pragma unroll
    for (int n = 0; n < 9; ++n) {
        const int cc = 16 * n + (l & 15), hl = cc / 18, d = cc - 18 * hl;
#pragma unroll
        for (int tb = 0; tb < 2; ++tb)
            *(u32x2*)(st + (hl * 2 + tb) * 640 + ((g & 1) * 20 + d) * 16 + (g >> 1) * 8) = u32x2{pk2(acc[n][tb][0], acc[n][tb][1]), pk2(acc[n][tb][2], acc[n][tb][3])};
    }
    asm volatile("s_waitcnt lgkmcnt(0)" ::: "memory");
    const unsigned dv = WS_VT + (unsigned)(((hq * 8) * 128 + panel * 8 + 2 * w) * 640);
#pragma unroll
    for (int i = 0; i < 10; ++i) { const int idx = 64 * i + l, h = idx / 80, o = idx - 80 * h; st128(c, dv + (unsigned)(h * 128 * 640 + o * 16), *(const u32x4*)(st + idx * 16)); }
}

DI void prep_wsp(const Ctx& c, int n) {
    const int tid = threadIdx.x, l = tid & 63; const unsigned w = rfl(tid >> 6); const int g = l >> 4;
    const u32x4 srd = make_srd(c.P.wsp, S_ * S_ * 2);
    const int tb = 2 * n + (int)(w & 1), s0 = (int)(w >> 1) * 16;
    unsigned vsrc = (unsigned)((tb * 16 + (l >> 3)) * (S_ * 2) + (((l & 7) ^ (l >> 3)) << 4));
    const unsigned lw = c.lds0 + w * 16384;
    const unsigned rd0 = lw + (unsigned)((l & 15) * 128) + ((g ^ (l & 7)) << 4), rd1 = lw + (unsigned)((l & 15) * 128) + (((4 + g) ^ (l & 7)) << 4);
    auto batch = [&](int bt) {
#pragma unroll
        for (int i = 0; i < 8; ++i) {
            dma_now(srd, vsrc, (unsigned)((s0 + bt * 8 + i) * 128), lw + i * 2048);
            dma_now(srd, vsrc, (unsigned)((s0 + bt * 8 + i) * 128 + 8 * S_ * 2), lw + i * 2048 + 1024);
        }
    };
    u32x4 f[16];
    batch(0);
    asm volatile("s_waitcnt vmcnt(0)" ::: "memory");
#pragma unroll
    for (int i = 0; i < 8; ++i) { f[2 * i] = __builtin_bit_cast(u32x4, lds_rd128(rd0 + i * 2048)); f[2 * i + 1] = __builtin_bit_cast(u32x4, lds_rd128(rd1 + i * 2048)); }
#pragma unroll
    for (int i = 0; i < 16; ++i) OPAQUE(f[i]);
    batch(1);
#pragma unroll
    for (int i = 0; i < 16; ++i) st128(c, WS_WP + (unsigned)((tb * 64 + 2 * s0 + i) * 1024 + l * 16), f[i]);
    asm volatile("s_waitcnt vmcnt(0)" ::: "memory");
#pragma unroll
    for (int i = 0; i < 8; ++i) {
        st128(c, WS_WP + (unsigned)((tb * 64 + 2 * s0 + 16 + 2 * i) * 1024 + l * 16), __builtin_bit_cast(u32x4, lds_rd128(rd0 + i * 2048)));
        st128(c, WS_WP + (unsigned)((tb * 64 + 2 * s0 + 17 + 2 * i) * 1024 + l * 16), __builtin_bit_cast(u32x4, lds_rd128(rd1 + i * 2048)));
    }
    publish(c, FL_WP + n);
}

DI void prep_wcat(const Ctx& c, int kb) {
    const int tid = threadIdx.x, l = tid & 63; const unsigned w = rfl(tid >> 6); const int g = l >> 4;
    const u32x4 srd = kb < 18 ? make_srd(c.P.wo, D_ * D_ * 2) : make_srd(c.P.wout, FH * D_ * 2);
#pragma unroll
    for (int i = 0; i < 9; ++i) {
        const int idx = 64 * ((int)w + 4 * i) + l, sg = idx / 72, ch = idx - 72 * sg, gg = sg >> 3, j = sg & 7;
        int row;
        if (kb < 16) { const int head = 2 * kb + (gg >> 1), h = gg & 1; row = 18 * head + (j < 4 ? 4 * h + j : 8 + 4 * h + (j - 4)); }
        else if (kb < 18) row = 18 * (16 * (kb - 16) + 4 * gg + (j >> 1)) + 16 + (j & 1);
        else row = 32 * (kb - 18) + (j < 4 ? 4 * gg + j : 16 + 4 * gg + (j - 4));
        dma_now(srd, (unsigned)(row * 1152 + ch * 16), 0u, c.lds0 + (w + 4 * i) * 1024);
    }
    WAIT_BAR0();
    const int q = (l & 15) >> 2, p = l & 3;
    const unsigned r0 = c.lds0 + (unsigned)((8 * g + q) * 1152 + p * 8);
#pragma unroll
    for (int i = 0; i < 9; ++i) {
        const int db = (int)w + 4 * i;
        st128(c, WS_WC + (unsigned)((db * 90 + kb) * 1024 + l * 16), __builtin_bit_cast(u32x4, cat8(lds_rdtr(r0 + db * 32), lds_rdtr(r0 + 4 * 1152 + db * 32))));
    }
    publish(c, FL_WC + kb);
}

DI void prep_gate(const Ctx& c, int n) {
    const int tid = threadIdx.x, l = tid & 63; const int w = tid >> 6;
    const int row = n * 32 + w * 8 + (l >> 3);
    u32x4 xv[9], gv[9];
#pragma unroll
    for (int i = 0; i < 9; ++i) { xv[i] = *(const u32x4*)(c.P.x + row * 576 + ((l & 7) + 8 * i) * 8); gv[i] = *(const u32x4*)(c.P.gatew + ((l & 7) + 8 * i) * 8); }
    float a = 0.f;
#pragma unroll
    for (int i = 0; i < 9; ++i)
#pragma unroll
        for (int e = 0; e < 4; ++e) { a = __builtin_fmaf(bflo(xv[i][e]), bflo(gv[i][e]), a); a = __builtin_fmaf(bfhi(xv[i][e]), bfhi(gv[i][e]), a); }
    a += __shfl_xor(a, 1); a += __shfl_xor(a, 2); a += __shfl_xor(a, 4);
    if ((l & 7) == 0) st32(c, WS_GT + row * 4, __float_as_uint(1.0f / (1.0f + __expf(-a))));
    publish(c, FL_WC + 90 + n);
}

constexpr int AB_SLOT = 20 * 1024;
struct AttnState {
    f32x16 S[2], O[2]; u32x4 pl[2], ph[2];
    bf16x8 k0, v0, v1; u32x4 k1; u32x4 q0[2], q1[2];
};
template <int T, class F>
DI void attn_slot(AttnState& s, unsigned aK, unsigned aR, unsigned aV, const f32x16& zero, F&& hook) {
    constexpr int U = T ^ 1;
    s.S[U] = MFMA32(s.k0, BC8(s.q0[U]), zero);
    SB();
#pragma unroll
    for (int i = 0; i < 4; ++i) s.S[T][i] = EX2(s.S[T][i]);
    SB();
    s.S[U] = MFMA32(BC8(s.k1), BC8(s.q1[U]), s.S[U]);
    SB();
    if (T == 0) { s.k0 = lds_rd128(aK); s.k1[0] = lds_rd32(aR); }
#pragma unroll
    for (int i = 4; i < 8; ++i) s.S[T][i] = EX2(s.S[T][i]);
    s.pl[T][0] = pk2(s.S[T][0], s.S[T][1]); s.pl[T][1] = pk2(s.S[T][2], s.S[T][3]);
    SB();
    s.O[U] = MFMA32(s.v1, BC8(s.ph[U]), s.O[U]);
    SB();
    if (T == 0) s.v1 = lds_rd128(aV);
    hook();
    s.pl[T][2] = pk2(s.S[T][4], s.S[T][5]); s.pl[T][3] = pk2(s.S[T][6], s.S[T][7]);
    SB();
#pragma unroll
    for (int i = 8; i < 12; ++i) s.S[T][i] = EX2(s.S[T][i]);
    SB();
    s.O[T] = MFMA32(s.v0, BC8(s.pl[T]), s.O[T]);
    SB();
    if (T == 1) s.v0 = lds_rd128(aV);
#pragma unroll
    for (int i = 12; i < 16; ++i) s.S[T][i] = EX2(s.S[T][i]);
    SB();
    s.ph[T][0] = pk2(s.S[T][8], s.S[T][9]); s.ph[T][1] = pk2(s.S[T][10], s.S[T][11]);
    s.ph[T][2] = pk2(s.S[T][12], s.S[T][13]); s.ph[T][3] = pk2(s.S[T][14], s.S[T][15]);
    SB();
}

DI void attn_unit(const Ctx& c, int head, int qb) {
    const int tid = threadIdx.x, l = tid & 63; const unsigned w = rfl(tid >> 6); const int h = l >> 5, r32 = l & 31; const unsigned lds0 = c.lds0;
    {
        const int hq = head >> 3;
        __builtin_amdgcn_s_sleep(32); __builtin_amdgcn_s_sleep(32);
        wait_flags(c, tid < 16 ? FL_QKV + (4 + hq) * 16 + tid : tid < 32 ? FL_QKV + (8 + hq) * 16 + (tid - 16) : tid < 34 ? FL_QKV + hq * 16 + 2 * qb + (tid - 32) : ~0u);
    }
    const u32x4 srd = make_srd(c.P.ws + WS_KM, WS_QM - WS_KM);
    unsigned vl = (unsigned)l * 16; OPAQUE(vl);
    unsigned sLds[5], sSrc[5], sStep[5];
#pragma unroll
    for (int i = 0; i < 5; ++i) {
        const unsigned p = w + 4 * i;
        sLds[i] = lds0 + p * 1024;
        sSrc[i] = p < 8 ? (unsigned)(head * 65536) + p * 1024 : p < 18 ? (WS_VT - WS_KM) + (unsigned)(head * 81920) + (p - 8) * 1024
                : p == 18 ? (WS_KR - WS_KM) + (unsigned)(head * 8192) : OOB;
        sStep[i] = p < 8 ? 8192u : (p < 18 ? 10240u : (p == 18 ? 1024u : 0u));
    }
    auto piece = [&](int i, int ch, unsigned boff) { set_m0s(sLds[i], rfl(boff)); const unsigned so = ch < 8 ? sSrc[i] + (unsigned)ch * sStep[i] : OOB; asm volatile("s_nop 0"); dma_go_c(srd, vl, so); };
    auto issue = [&](int ch, unsigned boff) {
#pragma unroll
        for (int i = 0; i < 5; ++i) piece(i, ch, boff); };
    AttnState s;
    const int tok0 = qb * 256 + 64 * (int)w;
    issue(0, 0);
#pragma unroll
    for (int t = 0; t < 2; ++t) {
        s.q0[t] = __builtin_amdgcn_raw_buffer_load_b128(c.ws, (int)(WS_QM + ((unsigned)head * 64 + (tok0 >> 5) + t) * 1024 + l * 16), 0, 16);
        const unsigned qr = __builtin_amdgcn_raw_buffer_load_b32(c.ws, (int)(WS_QR + ((unsigned)head * 2048 + tok0 + 32 * t + r32) * 4), 0, 16);
        s.q1[t] = u32x4{h ? 0u : qr, 0u, 0u, 0u};
    }
    asm volatile("" : "+v"(s.q0[0]), "+v"(s.q0[1]), "+v"(s.q1[0]), "+v"(s.q1[1]));
    unsigned ka = lds0 + (unsigned)l * 16, ra = lds0 + 18432u + (unsigned)r32 * 4, va = lds0 + 8192u + (unsigned)(h * 20 + (r32 < 19 ? r32 : 19)) * 16;
    OPAQUE(ka); OPAQUE(ra); OPAQUE(va);
    const f32x16 zero = {};
    volatile LAS unsigned* flag = (volatile LAS unsigned*)(uintptr_t)(lds0 + 2 * AB_SLOT);

    for (int attempt = 0; attempt < 2; ++attempt) {
        float mx[2] = {-3.0e38f, -3.0e38f};
        {
            const u32x4 qz[2] = {u32x4{s.q1[0][0], 0u, 0u, 0u}, u32x4{s.q1[1][0], 0u, 0u, 0u}};
            const int nch = attempt ? 8 : 1, nblk = attempt ? 8 : 1;
            for (int ch = 0; ch < nch; ++ch) {
                if (attempt) { WAIT_BAR0(); issue(ch, 0); }
                WAIT_BAR0();
                for (int kb = 0; kb < nblk; ++kb) {
                    const bf16x8 k0 = lds_rd128(ka + kb * 1024);
                    const u32x4 k1 = u32x4{lds_rd32(ra + kb * 128), 0u, 0u, 0u};
#pragma unroll
                    for (int t = 0; t < 2; ++t) {
                        f32x16 sc = MFMA32(k0, BC8(s.q0[t]), zero); sc = MFMA32(BC8(k1), BC8(qz[t]), sc);
#pragma unroll
                        for (int r = 0; r < 16; ++r) mx[t] = __builtin_fmaxf(mx[t], sc[r]);
                    }
                }
            }
            if (attempt) { WAIT_BAR0(); issue(0, 0); WAIT_BAR0(); }
        }
#pragma unroll
        for (int t = 0; t < 2; ++t) {
            auto rr = __builtin_amdgcn_permlane32_swap(__float_as_uint(mx[t]), __float_as_uint(mx[t]), false, false);
            const float m = __builtin_fmaxf(__uint_as_float(rr[0]), __uint_as_float(rr[1]));
            const unsigned nb = __float_as_uint(-m), hi = nb & 0xffff0000u;
            const float r1 = -m - __uint_as_float(hi); const unsigned mid = __float_as_uint(r1) & 0xffff0000u;
            const float r2 = r1 - __uint_as_float(mid); const unsigned lo = __float_as_uint(r2) & 0xffff0000u;
            s.q1[t][1] = h ? 0u : ((hi >> 16) | mid); s.q1[t][2] = h ? 0u : (lo >> 16);
        }
        s.O[0] = zero; s.O[1] = zero;
        s.k0 = lds_rd128(ka); s.k1 = u32x4{lds_rd32(ra), 0x3f803f80u, 0x00003f80u, 0u};
        s.v0 = lds_rd128(va); s.v1 = s.v0; s.ph[1] = u32x4{0u, 0u, 0u, 0u};
        s.S[0] = MFMA32(s.k0, BC8(s.q0[0]), zero); s.S[0] = MFMA32(BC8(s.k1), BC8(s.q1[0]), s.S[0]);
        unsigned cur = 0, nxt = AB_SLOT;
        auto nohook = [] {};
        for (int ch = 0; ch < 8; ++ch) {
            const unsigned cK = ka + cur, cR = ra + cur, cV = va + cur, nK = ka + nxt, nR = ra + nxt, nV = va + nxt;
#pragma unroll
            for (int j = 0; j < 7; ++j) {
                attn_slot<0>(s, cK + (j + 1) * 1024, cR + (j + 1) * 128, cV + j * 1280 + 640, zero, nohook);
                attn_slot<1>(s, 0, 0, cV + (j + 1) * 1280, zero, [&] { if (j < 5) piece(j, ch + 1, nxt); });
            }
            asm volatile("s_waitcnt vmcnt(0)\n\ts_barrier" ::: "memory");
            SB();
            attn_slot<0>(s, nK, nR, cV + 7 * 1280 + 640, zero, nohook);
            attn_slot<1>(s, 0, 0, nV, zero, [&] { asm volatile("s_barrier" ::: "memory"); });
            const unsigned t_ = cur; cur = nxt; nxt = t_;
        }
        s.O[1] = MFMA32(s.v1, BC8(s.ph[1]), s.O[1]);
        if (attempt == 1) break;
        bool bad = false;
#pragma unroll
        for (int t = 0; t < 2; ++t) {
            float am = 0.f; bool b = false;
#pragma unroll
            for (int r = 0; r < 11; ++r) { am = __builtin_fmaxf(am, __builtin_fabsf(s.O[t][r])); b = b || (s.O[t][r] != s.O[t][r]); }
            bad = bad || b || !(am < 1.0e30f);
        }
        if (tid == 0) *flag = 0u;
        WAIT_BAR0();
        if (__any(bad)) *flag = 1u;
        WAIT_BAR0();
        if (rfl(*flag) == 0u) break;
    }
#pragma unroll
    for (int t = 0; t < 2; ++t) {
        auto rr = __builtin_amdgcn_permlane32_swap(__float_as_uint(s.O[t][10]), __float_as_uint(s.O[t][10]), false, false);
        const float inv = 1.0f / __uint_as_float(rr[0]);
        const int tok = tok0 + 32 * t + r32, tb = tok >> 4;
        const unsigned base = WS_HC + (unsigned)(tb * 90 * 1024);
        const u32x4 m = {pk2(s.O[t][0] * inv, s.O[t][1] * inv), pk2(s.O[t][2] * inv, s.O[t][3] * inv), pk2(s.O[t][4] * inv, s.O[t][5] * inv), pk2(s.O[t][6] * inv, s.O[t][7] * inv)};
        st128(c, base + (unsigned)((head >> 1) * 1024 + ((tok & 15) + 16 * (2 * (head & 1) + h)) * 16), m);
        if (h == 0) st32(c, base + (unsigned)((16 + (head >> 4)) * 1024 + ((tok & 15) + 16 * ((head & 15) >> 2)) * 16 + (head & 3) * 4), pk2(s.O[t][8] * inv, s.O[t][9] * inv));
    }
    publish(c, FL_O + qb * 32 + head);
}

constexpr int SD_N = 8, SD_SLOT = 9216;
struct SpdRegs { bf16x8 zf[9]; u32x4 t[4][2]; unsigned vZ[3], vz, vT; };
template <int U>
DI void spd_sub(f32x4 (&acc)[9][2], SpdRegs& r, u32x4 srdT, u32x4 srdZ, unsigned sWZ, unsigned w, unsigned sTa, unsigned sTb, unsigned sZ) {
    constexpr int NX = (U + 1) & (SD_N - 1), PF = (U + SD_N - 1) & (SD_N - 1), TU = U & 3, TP = (U + 3) & 3;
    asm volatile("s_waitcnt vmcnt(10)\n\ts_barrier" : "+v"(r.t[TU][0]), "+v"(r.t[TU][1])::"memory");
    SB();
#pragma unroll
    for (int n = 0; n < 9; ++n) {
        if (n == 0) LGKM(4);
        if (n == 5) LGKM(5);
        if (n == 4 || n == 6) set_m0(sWZ, PF * SD_SLOT + (n == 4 ? 0 : 4096));
        mfma16v(acc[n][0], r.zf[n], BC8(r.t[TU][0]));
        if (n == 0) ld_frag<0>(r.t[TP][0], srdT, r.vT, sTa);
        if (n == 2) ld_frag<0>(r.t[TP][1], srdT, r.vT, sTb);
        if (n == 4) dma_go_c(srdZ, r.vZ[0], sZ);
        if (n == 6) dma_go_c(srdZ, r.vZ[1], sZ);
        mfma16v(acc[n][1], r.zf[n], BC8(r.t[TU][1]));
        r.zf[n] = lds_rd128(r.vz + NX * SD_SLOT + n * 1024);
        SB();
    }
    if (w == 0) { set_m0(sWZ, PF * SD_SLOT + 8192); asm volatile("s_nop 0"); dma_go_c(srdZ, r.vZ[2], sZ); }
    SB();
}

DI void spatial_tile(const Ctx& c, int panel, int jt, const u32x4 (&z1p)[9]) {
    const int tid = threadIdx.x, l = tid & 63; const unsigned w = rfl(tid >> 6); const unsigned lds0 = c.lds0;
    const u32x4 srdT = make_srd(c.P.ws + WS_WP + (size_t)(panel * 8 + 2 * w) * 65536, 2 * 65536), srdZ = make_srd(c.P.ws + WS_Z2 + (size_t)jt * 9 * 65536, 9 * 65536);
    SpdRegs r;
#pragma unroll
    for (int i = 0; i < 3; ++i) { r.vZ[i] = (w + 4 * i) * 65536 + l * 16; OPAQUE(r.vZ[i]); }
    r.vz = lds0 + l * 16; OPAQUE(r.vz);
    r.vT = l * 16; OPAQUE(r.vT);
    const unsigned sWZ = lds0 + w * 1024;
#define SO(v) ((v) < 64 ? (unsigned)(v) * 1024u : OOB)
#define SO2(v) ((v) < 64 ? (unsigned)(v) * 1024u + 65536u : OOB)
    f32x4 acc[9][2];
#pragma unroll
    for (int n = 0; n < 9; ++n) { acc[n][0] = f32x4{0, 0, 0, 0}; acc[n][1] = f32x4{0, 0, 0, 0}; }
    PIN18(acc, "s_nop 1");
#pragma unroll
    for (int u = 0; u < SD_N - 1; ++u) {
        if (u >= 4) { ld_frag<0>(r.t[u - 4][0], srdT, r.vT, SO(u - 4)); ld_frag<0>(r.t[u - 4][1], srdT, r.vT, SO2(u - 4)); }
#pragma unroll
        for (int i = 0; i < 2; ++i) { set_m0(sWZ, u * SD_SLOT + i * 4096); asm volatile("s_nop 0"); dma_go_c(srdZ, r.vZ[i], SO(u)); }
        if (w == 0) { set_m0(sWZ, u * SD_SLOT + 8192); asm volatile("s_nop 0"); dma_go_c(srdZ, r.vZ[2], SO(u)); }
    }
    asm volatile("s_waitcnt vmcnt(18)\n\ts_barrier" ::: "memory");
#pragma unroll
    for (int n = 0; n < 9; ++n) r.zf[n] = lds_rd128(r.vz + n * 1024);
    LGKM(0);
    for (int u = 0; u < 64; u += 8) {
        spd_sub<0>(acc, r, srdT, srdZ, sWZ, w, SO(u + 3), SO2(u + 3), SO(u + 7));
        spd_sub<1>(acc, r, srdT, srdZ, sWZ, w, SO(u + 4), SO2(u + 4), SO(u + 8));
        spd_sub<2>(acc, r, srdT, srdZ, sWZ, w, SO(u + 5), SO2(u + 5), SO(u + 9));
        spd_sub<3>(acc, r, srdT, srdZ, sWZ, w, SO(u + 6), SO2(u + 6), SO(u + 10));
        spd_sub<4>(acc, r, srdT, srdZ, sWZ, w, SO(u + 7), SO2(u + 7), SO(u + 11));
        spd_sub<5>(acc, r, srdT, srdZ, sWZ, w, SO(u + 8), SO2(u + 8), SO(u + 12));
        spd_sub<6>(acc, r, srdT, srdZ, sWZ, w, SO(u + 9), SO2(u + 9), SO(u + 13));
        spd_sub<7>(acc, r, srdT, srdZ, sWZ, w, SO(u + 10), SO2(u + 10), SO(u + 14));
    }
    asm volatile("s_waitcnt vmcnt(0) lgkmcnt(0)" : "+v"(r.t[0][0]), "+v"(r.t[0][1]), "+v"(r.t[1][0]), "+v"(r.t[1][1]), "+v"(r.t[2][0]), "+v"(r.t[2][1]), "+v"(r.t[3][0]), "+v"(r.t[3][1])::"memory");
    PIN18(acc, "s_nop 7\n\ts_nop 1");
    float bs[2];
#pragma unroll
    for (int tb = 0; tb < 2; ++tb) bs[tb] = bf2f(c.P.bsp[panel * 128 + 32 * w + 16 * tb + (l & 15)]);
    unsigned hp[9][2][2];
#pragma unroll
    for (int n = 0; n < 9; ++n)
#pragma unroll
        for (int tb = 0; tb < 2; ++tb) {
            hp[n][tb][0] = pk2(bflo(z1p[n][2 * tb]) * (acc[n][tb][0] + bs[tb]), bfhi(z1p[n][2 * tb]) * (acc[n][tb][1] + bs[tb]));
            hp[n][tb][1] = pk2(bflo(z1p[n][2 * tb + 1]) * (acc[n][tb][2] + bs[tb]), bfhi(z1p[n][2 * tb + 1]) * (acc[n][tb][3] + bs[tb]));
        }
    const int fb0 = jt * 9;
#pragma unroll
    for (int tb = 0; tb < 2; ++tb) {
        const unsigned base = WS_HC + (unsigned)(((panel * 8 + 2 * w + tb) * 90 + 18) * 1024 + l * 16);
        if ((jt & 1) == 0) {
#pragma unroll
            for (int n = 0; n < 8; n += 2) st128(c, base + (unsigned)(((fb0 + n) >> 1) * 1024), u32x4{hp[n][tb][0], hp[n][tb][1], hp[n + 1][tb][0], hp[n + 1][tb][1]});
            st64(c, base + (unsigned)(((fb0 + 8) >> 1) * 1024), u32x2{hp[8][tb][0], hp[8][tb][1]});
        } else {
            st64(c, base + (unsigned)((fb0 >> 1) * 1024 + 8), u32x2{hp[0][tb][0], hp[0][tb][1]});
#pragma unroll
            for (int n = 1; n < 9; n += 2) st128(c, base + (unsigned)(((fb0 + n) >> 1) * 1024), u32x4{hp[n][tb][0], hp[n][tb][1], hp[n + 1][tb][0], hp[n + 1][tb][1]});
        }
    }
}

constexpr int FN_STAGE = 16 * 1024;
struct FnRegs { bf16x8 wf[5]; u32x4 h[4][3]; unsigned vW[4], vrd, vl; };
template <int U, int VM>
DI void fn_stage(f32x4 (&acc)[5], FnRegs& r, u32x4 srdW, u32x4 srdH, unsigned sWW, unsigned sNext) {
    constexpr int PF = (U + 3) & 3;
    asm volatile("s_waitcnt vmcnt(%3)\n\ts_barrier" : "+v"(r.h[U][0]), "+v"(r.h[U][1]), "+v"(r.h[U][2]) : "n"(VM) : "memory");
    SB();
#pragma unroll
    for (int n = 0; n < 5; ++n) r.wf[n] = lds_rd128(r.vrd + U * FN_STAGE + (n * 3) * 1024);
    ld_frag<0>(r.h[PF][0], srdH, r.vl, sNext); ld_frag<1024>(r.h[PF][1], srdH, r.vl, sNext); ld_frag<2048>(r.h[PF][2], srdH, r.vl, sNext);
    SB();
#pragma unroll
    for (int kk = 0; kk < 3; ++kk) {
#pragma unroll
        for (int n = 0; n < 5; ++n) {
            const int i = kk * 5 + n;
            const bool dm = (i % 4 == 1);
            if (dm) set_m0(sWW, PF * FN_STAGE + (i / 4) * 4096);
            mfma16v(acc[n], r.wf[n], BC8(r.h[U][kk]));
            if (dm) dma_go_c(srdW, r.vW[i / 4], sNext);
            if (kk + 1 < 3) r.wf[n] = lds_rd128(r.vrd + U * FN_STAGE + (n * 3 + kk + 1) * 1024);
            SB();
        }
    }
}
DI void final_tile(const Ctx& c, int tt, int j, unsigned hflag, int rot) {
    const int tid = threadIdx.x, l = tid & 63; const unsigned w = rfl(tid >> 6); const int g = l >> 4;
    const int db0 = (9 * j) >> 1;
    const u32x4 srdW = make_srd(c.P.ws + WS_WC + (size_t)db0 * 90 * 1024, 5 * 90 * 1024);
    const u32x4 srdH = make_srd(c.P.ws + WS_HC + (size_t)(tt * 4 + w) * 90 * 1024, 90 * 1024);
    FnRegs r;
    r.vl = (unsigned)l * 16; OPAQUE(r.vl);
#pragma unroll
    for (int i = 0; i < 4; ++i) { const unsigned p = w + 4 * i, db = p / 3, kk = p - db * 3; r.vW[i] = p < 15 ? (db * 90 + kk) * 1024 + l * 16 : OOB; OPAQUE(r.vW[i]); }
    r.vrd = c.lds0 + l * 16; OPAQUE(r.vrd);
    const unsigned sWW = c.lds0 + w * 1024;
#define FO(s) ((s) < 24 ? (18u + 3u * (unsigned)((s) + rot >= 24 ? (s) + rot - 24 : (s) + rot)) * 1024u : (s) < 30 ? (unsigned)((s) - 24) * 3072u : OOB)
    asm volatile("s_waitcnt vmcnt(0) lgkmcnt(0)\n\ts_barrier" ::: "memory");
    raise_flag(c, hflag);
#pragma unroll
    for (int s = 0; s < 3; ++s)
#pragma unroll
        for (int i = 0; i < 4; ++i) { set_m0(sWW, s * FN_STAGE + i * 4096); asm volatile("s_nop 0"); dma_go_c(srdW, r.vW[i], FO(s)); }
    wait_flags(c, tid < 16 ? FL_H + (tt >> 1) * 16 + tid : ~0u);
    f32x4 accA[5], accB[5];
#pragma unroll
    for (int n = 0; n < 5; ++n) { accA[n] = f32x4{0, 0, 0, 0}; accB[n] = f32x4{0, 0, 0, 0}; }
    PIN5(accA, "s_nop 1"); PIN5(accB, "s_nop 1");
#pragma unroll
    for (int s = 0; s < 3; ++s) { ld_frag<0>(r.h[s][0], srdH, r.vl, FO(s)); ld_frag<1024>(r.h[s][1], srdH, r.vl, FO(s)); ld_frag<2048>(r.h[s][2], srdH, r.vl, FO(s)); }
    asm volatile("s_waitcnt vmcnt(6)" : "+v"(r.h[0][0]), "+v"(r.h[0][1]), "+v"(r.h[0][2])::"memory");
    fn_stage<0, 63>(accB, r, srdW, srdH, sWW, FO(3));
    asm volatile("s_waitcnt vmcnt(10)" : "+v"(r.h[1][0]), "+v"(r.h[1][1]), "+v"(r.h[1][2])::"memory");
    fn_stage<1, 63>(accB, r, srdW, srdH, sWW, FO(4));
    fn_stage<2, 14>(accB, r, srdW, srdH, sWW, FO(5));
    fn_stage<3, 14>(accB, r, srdW, srdH, sWW, FO(6));
    for (int s = 4; s < 20; s += 4) {
        fn_stage<0, 14>(accB, r, srdW, srdH, sWW, FO(s + 3)); fn_stage<1, 14>(accB, r, srdW, srdH, sWW, FO(s + 4));
        fn_stage<2, 14>(accB, r, srdW, srdH, sWW, FO(s + 5)); fn_stage<3, 14>(accB, r, srdW, srdH, sWW, FO(s + 6));
    }
    fn_stage<0, 14>(accB, r, srdW, srdH, sWW, FO(23));
    if (tid < 64) poll_flags(c, tid < 32 ? FL_O + (tt >> 2) * 32 + tid : ~0u);
    fn_stage<1, 14>(accB, r, srdW, srdH, sWW, FO(24));
    fn_stage<2, 14>(accB, r, srdW, srdH, sWW, FO(25));
    fn_stage<3, 14>(accB, r, srdW, srdH, sWW, FO(26));
    fn_stage<0, 14>(accA, r, srdW, srdH, sWW, FO(27)); fn_stage<1, 14>(accA, r, srdW, srdH, sWW, FO(28));
    fn_stage<2, 14>(accA, r, srdW, srdH, sWW, FO(29)); fn_stage<3, 14>(accA, r, srdW, srdH, sWW, OOB);
    fn_stage<0, 14>(accA, r, srdW, srdH, sWW, OOB); fn_stage<1, 14>(accA, r, srdW, srdH, sWW, OOB);
    asm volatile("s_waitcnt vmcnt(0) lgkmcnt(0)" : "+v"(r.h[0][0]), "+v"(r.h[0][1]), "+v"(r.h[0][2]), "+v"(r.h[1][0]), "+v"(r.h[1][1]), "+v"(r.h[1][2]),
                 "+v"(r.h[2][0]), "+v"(r.h[2][1]), "+v"(r.h[2][2]), "+v"(r.h[3][0]), "+v"(r.h[3][1]), "+v"(r.h[3][2])::"memory");
    PIN5(accA, "s_nop 7\n\ts_nop 1"); PIN5(accB, "");
    const __amdgpu_buffer_rsrc_t ro = __builtin_amdgcn_make_buffer_rsrc((void*)c.P.out, (short)0, S_ * D_ * 2, 0x00020000);
    const int tok = tt * 64 + 16 * w + (l & 15);
    const float gt = __uint_as_float(__builtin_amdgcn_raw_buffer_load_b32(c.ws, (int)(WS_GT + tok * 4), 0, 16));
#pragma unroll
    for (int n = 0; n < 5; ++n) {
        const int d = (db0 + n) * 16 + 4 * g;
        if (d >= 72 * j && d < 72 * j + 72) {
            float v[4];
#pragma unroll
            for (int rr = 0; rr < 4; ++rr) v[rr] = gt * accA[n][rr] + (1.0f - gt) * accB[n][rr];
            __builtin_amdgcn_raw_buffer_store_b64(u32x2{pk2(v[0], v[1]), pk2(v[2], v[3])}, ro, (tok * 576 + d) * 2, 0, 0);
        }
    }
}

constexpr int MEGA_LDS = 73728 + 1024;

__global__ __launch_bounds__(256, 2) void mega_kernel(Params P) {
    extern __shared__ __attribute__((aligned(16))) char smem[];
    const int b = blockIdx.x, tid = threadIdx.x;
    const u64 tag = __hip_atomic_load((gu64*)(P.ws + WS_EP), __ATOMIC_RELAXED, __HIP_MEMORY_SCOPE_AGENT) + 1ull;
    const Ctx c{P, __builtin_amdgcn_make_buffer_rsrc(P.ws, (short)0, (int)WS_END, 0x00020000), tag, (unsigned)(uintptr_t)smem, smem};
    if (b < 256) {
        __builtin_amdgcn_s_setprio(1);
        const int x = b & 7, i = b >> 3, panel = (x >> 2) * 8 + (i & 7), jt = (x & 3) * 4 + (i >> 3);
        u32x4 z1p[9];
        tile_z(c, panel, jt, ((i & 7) + 2 * (i >> 3)) % 9, z1p);
        __builtin_amdgcn_s_setprio(2);
        spatial_tile(c, panel, jt, z1p);
        final_tile(c, 2 * panel + (jt >> 3), jt & 7, FL_H + panel * 16 + jt, ((i & 7) * 2 + (i >> 3)) % 21);
        if (b == 0 && tid < 64) {
#pragma unroll
            for (int k = 0; k < 4; ++k) { poll_flags(c, FL_H + 64 * k + tid); poll_flags(c, FL_O + 64 * k + tid); }
            if (tid == 0) __hip_atomic_store((gu64*)(P.ws + WS_EP), tag, __ATOMIC_RELAXED, __HIP_MEMORY_SCOPE_AGENT);
        }
    } else {
        const int a = b - 256, x = a & 7, idx = a >> 3;
        __builtin_amdgcn_s_setprio(1);
        if (idx < 24) {
            const int hq = x & 3, ty = idx % 3, panel = (x >> 2) * 8 + idx / 3, s0 = (idx / 3 + 3 * ty + 1) % 9;
            if (ty < 2) tile_qk(c, panel, ty, hq, s0); else tile_v(c, panel, hq, s0);
            publish(c, FL_QKV + (ty * 4 + hq) * 16 + panel);
            const int k = idx * 8 + x;
            if (k < 90) prep_wcat(c, k); else if (k < 154) prep_gate(c, k - 90);
        } else prep_wsp(c, (idx - 24) * 8 + x);
        __builtin_amdgcn_s_setprio(0);
        attn_unit(c, x * 4 + (idx & 3), idx >> 2);
    }
}

extern "C" void kernel_launch(void* const* d_in, const int* in_sizes, int n_in, void* d_out, int out_size, void* d_ws, size_t ws_size, hipStream_t stream) {
    Params P;
    P.x = (const u16*)d_in[0]; P.wqkv = (const u16*)d_in[1]; P.wo = (const u16*)d_in[2]; P.win = (const u16*)d_in[4]; P.wsp = (const u16*)d_in[5];
    P.bsp = (const u16*)d_in[6]; P.wout = (const u16*)d_in[7]; P.gatew = (const u16*)d_in[8]; P.out = (u16*)d_out; P.ws = (char*)d_ws;
    hipFuncSetAttribute((const void*)mega_kernel, hipFuncAttributeMaxDynamicSharedMemorySize, MEGA_LDS);
    mega_kernel<<<512, 256, MEGA_LDS, stream>>>(P);
}
```
